```python
import torch

def _softmax(x, axis=-1):
    return torch.softmax(x, dim=axis)
def _gelu(x):
    return torch.nn.functional.gelu(x, approximate="tanh")
def _silu(x):
    return torch.nn.functional.silu(x)
def _sigmoid(x):
    return torch.sigmoid(x)
def _layernorm(x, axis=-1, eps=1e-5):
    m = x.mean(dim=axis, keepdim=True)
    v = x.var(dim=axis, keepdim=True, unbiased=False)
    return (x - m) / torch.sqrt(v + eps)
def _scatter_add(tgt, idx, src, axis=0):
    return tgt.clone().index_add_(axis, idx, src)
def _segment_sum(src, seg, n):
    out = torch.zeros((n,) + src.shape[1:], dtype=src.dtype)
    return out.index_add_(0, seg, src)
def _pad(x, pw):
    flat = tuple(v for pair in reversed(pw) for v in pair)
    return torch.nn.functional.pad(x, flat)

kernel_name = 'parallel_sum__mha_block__glu_gated_swiglu__prompt'

def setup_inputs():

    _w = (lambda a: (lambda t: t.to(torch.bfloat16) if t.is_floating_point() else t)(torch.as_tensor(a)))
    def _A_setup():
        B, S, D, H = 1, 2048, 1024, 32
        return {
            "x": _w((lambda *s: torch.randn(*s, dtype=torch.bfloat16) * 0.05)(B, S, D)),
            "wqkv": _w((lambda *s: torch.randn(*s, dtype=torch.bfloat16) * 0.05)(D, 3 * D)),
            "wo": _w((lambda *s: torch.randn(*s, dtype=torch.bfloat16) * 0.05)(D, D)),
        }
    _A_in = _A_setup()

    def _B_setup():
        B, S, D, F = 1, 2048, 1024, 3584
        return {
            "x": _w((lambda *s: torch.randn(*s, dtype=torch.bfloat16) * 0.05)(B, S, D)),
            "w1": _w((lambda *s: torch.randn(*s, dtype=torch.bfloat16) * 0.8)(D, F) * (2.0 / D) ** 0.5),
            "w3": _w((lambda *s: torch.randn(*s, dtype=torch.bfloat16) * 0.8)(D, F) * (2.0 / D) ** 0.5),
            "w2": _w((lambda *s: torch.randn(*s, dtype=torch.bfloat16) * 0.8)(F, D) * (2.0 / F) ** 0.5),
        }
    _B_in = _B_setup()

    return {
        "A_x": _A_in["x"],
        "A_wqkv": _A_in["wqkv"],
        "A_wo": _A_in["wo"],
        "B_x": _B_in["x"],
        "B_w1": _B_in["w1"],
        "B_w3": _B_in["w3"],
        "B_w2": _B_in["w2"],
    }

def reference(A_x, A_wqkv, A_wo, B_x, B_w1, B_w3, B_w2):
    def _A_ref(x, wqkv, wo):
        B, S, D = x.shape
        H = 32
        Dh = D // H
        qkv = (lambda x, *d: x.permute(*d))((x @ wqkv).reshape(B, S, 3, H, Dh), 2, 0, 3, 1, 4)
        q, k, v = qkv[0], qkv[1], qkv[2]
        scores = torch.einsum("bhqd,bhkd->bhqk", q, k) * (Dh ** -0.5)
        p = _softmax(scores, axis=-1)
        o = torch.einsum("bhqk,bhkd->bhqd", p, v)
        return (lambda x, *d: x.permute(*d))(o, 0, 2, 1, 3).reshape(B, S, D) @ wo

    def _B_ref(x, w1, w3, w2):
        gate = torch.einsum("bsd,df->bsf", x, w1)
        up = torch.einsum("bsd,df->bsf", x, w3)
        h = _silu(gate) * up
        out = torch.einsum("bsf,fd->bsd", h, w2)
        return out

    x = A_x
    xn = _layernorm(x, axis=-1)
    return x + _A_ref(xn, A_wqkv, A_wo) + _B_ref(xn, B_w1, B_w3, B_w2)
```

```cpp
#include <hip/hip_runtime.h>
#include <stdint.h>

typedef unsigned short u16;
typedef short bf16x8 __attribute__((ext_vector_type(8)));
typedef short s16x4 __attribute__((ext_vector_type(4)));
typedef float f32x2 __attribute__((ext_vector_type(2)));
typedef float f32x4 __attribute__((ext_vector_type(4)));
typedef float f32x16 __attribute__((ext_vector_type(16)));
typedef unsigned u32x2 __attribute__((ext_vector_type(2)));
typedef unsigned u32x4 __attribute__((ext_vector_type(4)));
typedef __bf16 bf16x2_t __attribute__((ext_vector_type(2)));
#define LAS __attribute__((address_space(3)))
typedef const LAS char* lds_cptr;

constexpr int SEQ = 2048, DM = 1024, FF = 3584, NH = 32;
constexpr size_t MB = 1024 * 1024;
constexpr size_t WS_CTR = 0;
constexpr size_t WS_XN = 4096;
constexpr size_t WS_QKV = WS_XN + 4 * MB;
constexpr size_t WS_ACT = WS_QKV + 12 * MB;
constexpr float QSCALE = 0.17677669529663687f * 1.4426950408889634f;

#define SCHED() __builtin_amdgcn_sched_barrier(0)
#define PIN(x) asm volatile("" : "+v"(x))
#define MF32(a, b, c) __builtin_amdgcn_mfma_f32_32x32x16_bf16(a, b, c, 0, 0, 0)
#define MF16(a, b, c) __builtin_amdgcn_mfma_f32_16x16x32_bf16(a, b, c, 0, 0, 0)

__device__ __forceinline__ unsigned cvtpk(float lo, float hi) {
    f32x2 v = {lo, hi};
    bf16x2_t b = __builtin_convertvector(v, bf16x2_t);
    return __builtin_bit_cast(unsigned, b);
}
__device__ __forceinline__ float bf_lo(unsigned w) { return __uint_as_float(w << 16); }
__device__ __forceinline__ float bf_hi(unsigned w) { return __uint_as_float(w & 0xffff0000u); }
__device__ __forceinline__ float rbf(float x) { return bf_lo(cvtpk(x, x)); }
__device__ __forceinline__ s16x4 tr_read(lds_cptr p) {
    return __builtin_bit_cast(s16x4, __builtin_amdgcn_ds_read_tr16_b64_v4i16((LAS s16x4*)p));
}
__device__ __forceinline__ bf16x8 cat8(s16x4 lo, s16x4 hi) { return __builtin_shufflevector(lo, hi, 0, 1, 2, 3, 4, 5, 6, 7); }
__device__ __forceinline__ void dma_v(const void* g, unsigned lds) {
    asm volatile("s_mov_b32 m0, %1\n\ts_nop 0\n\tglobal_load_lds_dwordx4 %0, off" ::"v"(g), "s"(lds) : "memory");
}
__device__ __forceinline__ void dma_s2(unsigned voff, const void* sbase, unsigned lds) {
    asm volatile("s_mov_b32 m0, %2\n\ts_nop 0\n\tglobal_load_lds_dwordx4 %0, %1\n\tglobal_load_lds_dwordx4 %0, %1 offset:1024" ::"v"(voff), "s"(sbase), "s"(lds) : "memory");
}
__device__ __forceinline__ void swap16(unsigned& a, unsigned& b) {
    auto r = __builtin_amdgcn_permlane16_swap(a, b, false, false);
    a = r[0]; b = r[1];
}
__device__ __forceinline__ void swap32(unsigned& a, unsigned& b) {
    auto r = __builtin_amdgcn_permlane32_swap(a, b, false, false);
    a = r[0]; b = r[1];
}

__global__ __launch_bounds__(512) void k_ln(const u16* __restrict__ x, char* __restrict__ xnF, unsigned* __restrict__ ctr) {
    __shared__ __attribute__((aligned(16))) char sm[16384];
    const int tid = threadIdx.x, lane = tid & 63, wid = tid >> 6;
    if (blockIdx.x == 0) { ctr[tid] = 0u; ctr[tid + 512] = 0u; }
    const int row = blockIdx.x * 8 + wid;
    const u32x4* src = (const u32x4*)(x + (size_t)row * DM);
    const u32x4 a = src[lane], b = src[64 + lane];
    float v[16];
#pragma unroll
    for (int i = 0; i < 4; ++i) { v[2 * i] = bf_lo(a[i]); v[2 * i + 1] = bf_hi(a[i]); v[8 + 2 * i] = bf_lo(b[i]); v[8 + 2 * i + 1] = bf_hi(b[i]); }
    float s = 0.f;
#pragma unroll
    for (int i = 0; i < 16; ++i) s += v[i];
#pragma unroll
    for (int o = 32; o >= 1; o >>= 1) s += __shfl_xor(s, o);
    const float mean = s * (1.0f / DM);
    float q = 0.f;
#pragma unroll
    for (int i = 0; i < 16; ++i) { const float d = v[i] - mean; q += d * d; }
#pragma unroll
    for (int o = 32; o >= 1; o >>= 1) q += __shfl_xor(q, o);
    const float mb = rbf(mean), vb = rbf(q * (1.0f / DM));
    const float sd = rbf(sqrtf(rbf(vb + 1e-5f)));
    u32x4 oa, ob;
#pragma unroll
    for (int i = 0; i < 4; ++i) {
        oa[i] = cvtpk(rbf(v[2 * i] - mb) / sd, rbf(v[2 * i + 1] - mb) / sd);
        ob[i] = cvtpk(rbf(v[8 + 2 * i] - mb) / sd, rbf(v[8 + 2 * i + 1] - mb) / sd);
    }
    *(u32x4*)(sm + lane * 128 + wid * 16) = oa;
    *(u32x4*)(sm + (64 + lane) * 128 + wid * 16) = ob;
    __syncthreads();
    const int row0 = blockIdx.x * 8;
#pragma unroll
    for (int i = 0; i < 2; ++i) {
        const int p = tid + i * 512, chunk = p >> 3, r = p & 7;
        const u32x4 w = *(const u32x4*)(sm + p * 16);
        *(u32x4*)(xnF + ((size_t)(chunk >> 2) * 128 + (row0 >> 4)) * 1024 + (chunk & 3) * 256 + ((row0 & 15) + r) * 16) = w;
    }
}

__device__ __forceinline__ float silu_mul(float g, float u) {
    return g * u * __builtin_amdgcn_rcpf(1.0f + __builtin_amdgcn_exp2f(g * -1.4426950408889634f));
}
__global__ __launch_bounds__(512, 2) void k_gemm1(const char* __restrict__ xnF, const char* __restrict__ wqkv, const char* __restrict__ w1,
                                                  const char* __restrict__ w3, char* __restrict__ qkv, char* __restrict__ act) {
    __shared__ __attribute__((aligned(1024))) char smem[147456];
    const int tid = threadIdx.x, lane = tid & 63;
    const int wid = __builtin_amdgcn_readfirstlane(tid >> 6), wr = wid & 3, wc = wid >> 2;
    const int bx = blockIdx.x, j = (bx & 7) + 8 * ((bx >> 3) & 3), rb = bx >> 5;
    const unsigned lds0 = (unsigned)(size_t)(LAS char*)smem;
    const int s0 = (j >= 10) ? 2 + 5 * (j - 10) : 0, odd = s0 & 1;
    const int pA = s0 + odd, pB = (j == 9) ? 0 : s0 + odd + 2, sg = odd ? s0 : s0 + 4;
    const char* bsrc[3]; unsigned binc[3];
#pragma unroll
    for (int i = 0; i < 3; ++i) {
        const int id = (wid + 8 * i) % 20, kb = id / 5, blk = id % 5;
        const int r = lane >> 3, j8 = lane & 7, f = ((r >> 1) & 1) | ((kb & 1) << 1), cp = (j8 >> 1) ^ f;
        const bool ffn = (j >= 10) || (j == 9 && blk >= 3);
        const char* base; int pitch, inrow;
        if (!ffn) { base = wqkv; pitch = 6144; inrow = (320 * j + 64 * blk) * 2 + cp * 32; }
        else {
            pitch = 7168;
            if (blk == 2) { base = (cp < 2) ? w1 : w3; inrow = sg * 64 + (cp & 1) * 32; }
            else { base = (blk == 0 || blk == 3) ? w1 : w3; inrow = ((blk < 2) ? pA : pB) * 64 + cp * 32; }
        }
        bsrc[i] = base + (size_t)(kb * 8 + r) * pitch + inrow + (j8 & 1) * 16;
        binc[i] = 32u * pitch;
    }
    const char* asrc = xnF + ((size_t)rb * 16 + 2 * wid) * 1024;
    const unsigned avoff = lane * 16;
    const unsigned adst = lds0 + wid * 2048, bdst = lds0 + 65536 + wid * 1024;
#define G1_DMA(SL) do { \
        dma_s2(avoff, asrc, adst + (SL) * 16384); asrc += 131072; \
        dma_v(bsrc[0], bdst + (SL) * 20480); bsrc[0] += binc[0]; \
        dma_v(bsrc[1], bdst + (SL) * 20480 + 8192); bsrc[1] += binc[1]; \
        if (wc == 0) { dma_v(bsrc[2], bdst + (SL) * 20480 + 16384); bsrc[2] += binc[2]; } } while (0)
    const lds_cptr pa = (lds_cptr)smem + wr * 4096 + lane * 16;
    lds_cptr pb[4], pe0, pe1;
    {
        const int g = lane >> 4, q = (lane >> 2) & 3, p = lane & 3, f = (q >> 1) | ((g & 1) << 1);
        const lds_cptr b0 = (lds_cptr)smem + 65536 + g * 5120 + q * 128 + p * 8;
#pragma unroll
        for (int c = 0; c < 4; ++c) pb[c] = b0 + ((c ^ f) << 5) + wc * 3072;
        pe0 = b0 + ((wc ^ f) << 5) + 2048; pe1 = b0 + (((2 + wc) ^ f) << 5) + 2048;
    }
    f32x4 acc[4][10];
#pragma unroll
    for (int m = 0; m < 4; ++m)
#pragma unroll
        for (int n = 0; n < 10; ++n) acc[m][n] = (f32x4){0.f, 0.f, 0.f, 0.f};
    G1_DMA(0); G1_DMA(1); G1_DMA(2);
    asm volatile("s_waitcnt vmcnt(8)\n\ts_barrier" ::: "memory");
    if (wc == 1) asm volatile("s_barrier" ::: "memory");
#define G1_STEP(SL, DO_DMA, VM) do { \
        bf16x8 af[4], bf[10]; \
        _Pragma("unroll") for (int c = 0; c < 4; ++c) { \
            bf[c] = cat8(tr_read(pb[c] + (SL) * 20480), tr_read(pb[c] + (SL) * 20480 + 512)); \
            bf[4 + c] = cat8(tr_read(pb[c] + (SL) * 20480 + 1024), tr_read(pb[c] + (SL) * 20480 + 1536)); } \
        bf[8] = cat8(tr_read(pe0 + (SL) * 20480), tr_read(pe0 + (SL) * 20480 + 512)); \
        bf[9] = cat8(tr_read(pe1 + (SL) * 20480), tr_read(pe1 + (SL) * 20480 + 512)); \
        _Pragma("unroll") for (int m = 0; m < 4; ++m) af[m] = *(const LAS bf16x8*)(pa + (SL) * 16384 + m * 1024); \
        if (DO_DMA) G1_DMA(((SL) + 3) & 3); \
        asm volatile("s_waitcnt vmcnt(" #VM ") lgkmcnt(0)\n\ts_barrier" ::: "memory"); \
        SCHED(); __builtin_amdgcn_s_setprio(1); \
        _Pragma("unroll") for (int n = 0; n < 10; ++n) \
            _Pragma("unroll") for (int m = 0; m < 4; ++m) acc[m][n] = MF16(bf[n], af[m], acc[m][n]); \
        __builtin_amdgcn_s_setprio(0); SCHED(); \
        asm volatile("s_barrier" ::: "memory"); } while (0)
    for (int it = 0; it < 7; ++it) { G1_STEP(0, true, 8); G1_STEP(1, true, 8); G1_STEP(2, true, 8); G1_STEP(3, true, 8); }
    G1_STEP(0, true, 8); G1_STEP(1, false, 4); G1_STEP(2, false, 0); G1_STEP(3, false, 0);
    if (wc == 0) asm volatile("s_barrier" ::: "memory");
#undef G1_STEP
#undef G1_DMA
    int tz = threadIdx.x; asm volatile("" : "+v"(tz));
    const int fr = tz & 15, fq = (tz >> 4) & 3;
    const int tok0 = rb * 256 + wr * 64;
    const bool ffn_main = (j >= 10) || (j == 9 && wc == 1), ffn_ext = (j >= 10);
    if (ffn_main) {
        const int hs = wc ? pB : pA;
#pragma unroll
        for (int m = 0; m < 4; ++m) {
            const int tok = tok0 + m * 16 + fr;
#pragma unroll
            for (int sub = 0; sub < 2; ++sub) {
                const f32x4 g0 = acc[m][2 * sub], u0 = acc[m][4 + 2 * sub], g1 = acc[m][2 * sub + 1], u1 = acc[m][4 + 2 * sub + 1];
                unsigned a0 = cvtpk(silu_mul(g0[0], u0[0]), silu_mul(g0[1], u0[1])), a1 = cvtpk(silu_mul(g0[2], u0[2]), silu_mul(g0[3], u0[3]));
                unsigned b0 = cvtpk(silu_mul(g1[0], u1[0]), silu_mul(g1[1], u1[1])), b1 = cvtpk(silu_mul(g1[2], u1[2]), silu_mul(g1[3], u1[3]));
                swap16(a0, b0); swap16(a1, b1);
                const int ks = (hs + sub) * 2 + (fq & 1);
                *(u32x4*)(act + ((size_t)ks * 64 + (tok >> 5)) * 1024 + (fq >> 1) * 512 + (tok & 31) * 16) = (u32x4){a0, a1, b0, b1};
            }
        }
    } else {
#pragma unroll
        for (int sub = 0; sub < 4; ++sub) {
            const int cb = 10 * j + 6 * wc + sub, which = cb >> 5, head = cb & 31;
            const float sc = which == 0 ? QSCALE : 1.0f;
            char* base = qkv + (size_t)which * 4 * MB + (size_t)head * 131072;
#pragma unroll
            for (int m = 0; m < 4; ++m) {
                const int tok = tok0 + m * 16 + fr;
                const f32x4 va = acc[m][2 * sub] * sc, vb = acc[m][2 * sub + 1] * sc;
                unsigned a0 = cvtpk(va[0], va[1]), a1 = cvtpk(va[2], va[3]), b0 = cvtpk(vb[0], vb[1]), b1 = cvtpk(vb[2], vb[3]);
                swap16(a0, b0); swap16(a1, b1);
                const int off = which == 2 ? tok * 64 + (fq & 1) * 32 + (fq >> 1) * 16 : (tok >> 5) * 2048 + (fq & 1) * 1024 + (fq >> 1) * 512 + (tok & 31) * 16;
                *(u32x4*)(base + off) = (u32x4){a0, a1, b0, b1};
            }
        }
    }
    if (ffn_ext) {
#pragma unroll
        for (int mp = 0; mp < 2; ++mp) {
            const f32x4 g0 = acc[2 * mp][8], u0 = acc[2 * mp][9], g1 = acc[2 * mp + 1][8], u1 = acc[2 * mp + 1][9];
            unsigned a0 = cvtpk(silu_mul(g0[0], u0[0]), silu_mul(g0[1], u0[1])), a1 = cvtpk(silu_mul(g0[2], u0[2]), silu_mul(g0[3], u0[3]));
            unsigned b0 = cvtpk(silu_mul(g1[0], u1[0]), silu_mul(g1[1], u1[1])), b1 = cvtpk(silu_mul(g1[2], u1[2]), silu_mul(g1[3], u1[3]));
            swap16(a0, b0); swap16(a1, b1);
            const int tok = tok0 + (2 * mp + (fq & 1)) * 16 + fr, ks = sg * 2 + wc;
            *(u32x4*)(act + ((size_t)ks * 64 + (tok >> 5)) * 1024 + (fq >> 1) * 512 + (tok & 31) * 16) = (u32x4){a0, a1, b0, b1};
        }
    } else {
        const int cb0 = 10 * j + 4, which = cb0 >> 5, head = (cb0 & 31) + (fq & 1);
        const float sc = which == 0 ? QSCALE : 1.0f;
        char* base = qkv + (size_t)which * 4 * MB + (size_t)head * 131072;
#pragma unroll
        for (int m = 0; m < 4; ++m) {
            const int tok = tok0 + m * 16 + fr;
            const f32x4 va = acc[m][8] * sc, vb = acc[m][9] * sc;
            unsigned a0 = cvtpk(va[0], va[1]), a1 = cvtpk(va[2], va[3]), b0 = cvtpk(vb[0], vb[1]), b1 = cvtpk(vb[2], vb[3]);
            swap16(a0, b0); swap16(a1, b1);
            const int off = which == 2 ? tok * 64 + wc * 32 + (fq >> 1) * 16 : (tok >> 5) * 2048 + wc * 1024 + (fq >> 1) * 512 + (tok & 31) * 16;
            *(u32x4*)(base + off) = (u32x4){a0, a1, b0, b1};
        }
    }
}

__global__ __launch_bounds__(256, 2) void k_attn(const char* __restrict__ qkv, char* __restrict__ act) {
    __shared__ __attribute__((aligned(1024))) char smem[65536 + 16];
    const int tid = threadIdx.x, lane = tid & 63;
    const int wid = __builtin_amdgcn_readfirstlane(tid >> 6);
    const int bx = blockIdx.x, head = (bx & 7) * 4 + (bx >> 6), qb = (bx >> 3) & 7;
    const char* Qf = qkv + (size_t)head * 131072;
    const char* Kf = Qf + 4 * MB;
    const char* Vr = Qf + 8 * MB;
    const unsigned lds0 = (unsigned)(size_t)(LAS char*)smem;
    const unsigned voff = lane * 16;
    const int tq0 = qb * 8 + wid * 2;
    bf16x8 q[2][2];
#pragma unroll
    for (int i = 0; i < 2; ++i)
#pragma unroll
        for (int s = 0; s < 2; ++s) q[i][s] = *(const bf16x8*)(Qf + (tq0 + i) * 2048 + s * 1024 + voff);
    bf16x8 sel;
    { const short one = (((lane >> 2) & 1) == ((lane >> 4) & 1)) ? (short)0x3f80 : (short)0; sel = (bf16x8){one, one, one, one, one, one, one, one}; }
    const lds_cptr kp = (lds_cptr)smem + voff;
    const lds_cptr vp = (lds_cptr)smem + 8192 + (4 * (lane >> 5) + ((lane >> 2) & 3)) * 64 + ((lane >> 4) & 1) * 32 + (lane & 3) * 8;
    volatile LAS unsigned* flag = (volatile LAS unsigned*)(smem + 65536);
#define AT_DMA(ST) do { const unsigned d_ = lds0 + ((ST) & 3) * 16384 + wid * 2048; \
        dma_s2(voff, Kf + (ST) * 8192 + wid * 2048, d_); dma_s2(voff, Vr + (ST) * 8192 + wid * 2048, d_ + 8192); } while (0)
    f32x16 o[2]; f32x4 l[2];
    for (int attempt = 0; attempt < 2; ++attempt) {
        AT_DMA(0); AT_DMA(1); AT_DMA(2);
        if (tid == 0) *flag = 0u;
        f32x16 negm[2];
        {
            float m0 = -INFINITY, m1 = -INFINITY;
            const int nt = attempt == 0 ? 1 : 64;
            for (int kt = 0; kt < nt; ++kt) {
                const bf16x8 k0 = *(const bf16x8*)(Kf + kt * 2048 + voff), k1 = *(const bf16x8*)(Kf + kt * 2048 + 1024 + voff);
                f32x16 s0 = MF32(k0, q[0][0], (f32x16){}), s1 = MF32(k0, q[1][0], (f32x16){});
                s0 = MF32(k1, q[0][1], s0); s1 = MF32(k1, q[1][1], s1);
#pragma unroll
                for (int r = 0; r < 16; ++r) { m0 = fmaxf(m0, s0[r]); m1 = fmaxf(m1, s1[r]); }
            }
            m0 = fmaxf(m0, __shfl_xor(m0, 32)); m1 = fmaxf(m1, __shfl_xor(m1, 32));
#pragma unroll
            for (int r = 0; r < 16; ++r) { negm[0][r] = -m0; negm[1][r] = -m1; }
            PIN(negm[0]); PIN(negm[1]);
        }
        o[0] = (f32x16){}; o[1] = (f32x16){}; l[0] = (f32x4){}; l[1] = (f32x4){};
        asm volatile("s_waitcnt vmcnt(0)\n\ts_barrier" ::: "memory");
        bf16x8 kf0 = *(const LAS bf16x8*)(kp), kf1 = *(const LAS bf16x8*)(kp + 1024);
        bf16x8 vf0 = (bf16x8){}, vf1 = (bf16x8){};
        u32x4 pa0 = (u32x4){}, pa1 = (u32x4){}, pb0 = (u32x4){}, pb1 = (u32x4){};
        f32x16 sA = MF32(kf0, q[0][0], negm[0]); sA = MF32(kf1, q[0][1], sA);
        f32x16 sB;
#define EX(X, i) X[i] = __builtin_amdgcn_exp2f(X[i])
#define PK(X, i) cvtpk(X[i], X[(i) + 1])
#define BC8(x) __builtin_bit_cast(bf16x8, x)
#define AT_UNIT(SC, PC0, PC1, SN, QN, NM, PP0, PP1, OP, LP, LOADS) do { \
            SN = MF32(kf0, QN[0], NM); EX(SC, 0); EX(SC, 1); EX(SC, 2); PIN(SC); SCHED(); \
            OP = MF32(vf0, BC8(PP0), OP); EX(SC, 3); EX(SC, 4); EX(SC, 5); PIN(SC); SCHED(); \
            SN = MF32(kf1, QN[1], SN); EX(SC, 6); EX(SC, 7); EX(SC, 8); PIN(SC); SCHED(); \
            OP = MF32(vf1, BC8(PP1), OP); LOADS; EX(SC, 9); EX(SC, 10); EX(SC, 11); PIN(SC); SCHED(); \
            LP = MF16(sel, BC8(PP0), LP); EX(SC, 12); EX(SC, 13); PIN(SC); SCHED(); \
            LP = MF16(sel, BC8(PP1), LP); EX(SC, 14); EX(SC, 15); PIN(SC); SCHED(); \
            PC0 = (u32x4){PK(SC, 0), PK(SC, 2), PK(SC, 4), PK(SC, 6)}; PC1 = (u32x4){PK(SC, 8), PK(SC, 10), PK(SC, 12), PK(SC, 14)}; PIN(PC0); PIN(PC1); SCHED(); } while (0)
        for (int st = 0; st < 16; ++st) {
            if (st < 15) {
                if (st < 14) asm volatile("s_waitcnt vmcnt(4)\n\ts_barrier" ::: "memory");
                else asm volatile("s_waitcnt vmcnt(0)\n\ts_barrier" ::: "memory");
                if (st < 13) AT_DMA(st + 3);
            }
            const lds_cptr kc = kp + (st & 3) * 16384, kn = kp + ((st + 1) & 3) * 16384, vc = vp + (st & 3) * 16384;
#pragma unroll
            for (int kt = 0; kt < 4; ++kt) {
                const lds_cptr knx = kt < 3 ? kc + (kt + 1) * 2048 : kn;
                AT_UNIT(sA, pa0, pa1, sB, q[1], negm[1], pb0, pb1, o[1], l[1],
                        { kf0 = *(const LAS bf16x8*)(knx); kf1 = *(const LAS bf16x8*)(knx + 1024);
                          vf0 = cat8(tr_read(vc + kt * 2048), tr_read(vc + kt * 2048 + 512)); vf1 = cat8(tr_read(vc + kt * 2048 + 1024), tr_read(vc + kt * 2048 + 1536)); });
                AT_UNIT(sB, pb0, pb1, sA, q[0], negm[0], pa0, pa1, o[0], l[0], {});
            }
        }
        o[1] = MF32(vf0, BC8(pb0), o[1]); o[1] = MF32(vf1, BC8(pb1), o[1]);
        l[1] = MF16(sel, BC8(pb0), l[1]); l[1] = MF16(sel, BC8(pb1), l[1]);
#undef AT_UNIT
        bool bad = false;
#pragma unroll
        for (int i = 0; i < 2; ++i) {
            const float li = l[i][0], r = 1.0f / li;
            bad |= !(li < 1.0995e12f);
#pragma unroll
            for (int e = 0; e < 16; ++e) { o[i][e] *= r; bad |= !(fabsf(o[i][e]) < INFINITY); }
        }
        if (attempt == 1) break;
        if (bad) *flag = 1u;
        __syncthreads();
        const unsigned redo = *flag;
        __syncthreads();
        if (!redo) break;
    }
#undef AT_DMA
#pragma unroll
    for (int i = 0; i < 2; ++i)
#pragma unroll
        for (int s = 0; s < 2; ++s) {
            unsigned a0 = cvtpk(o[i][8 * s + 0], o[i][8 * s + 1]), a1 = cvtpk(o[i][8 * s + 2], o[i][8 * s + 3]);
            unsigned b0 = cvtpk(o[i][8 * s + 4], o[i][8 * s + 5]), b1 = cvtpk(o[i][8 * s + 6], o[i][8 * s + 7]);
            swap32(a0, b0); swap32(a1, b1);
            *(u32x4*)(act + ((size_t)(224 + 2 * head + s) * 64 + tq0 + i) * 1024 + voff) = (u32x4){a0, a1, b0, b1};
        }
}

__global__ __launch_bounds__(256, 2) void k_gemm2(const char* __restrict__ act, const char* __restrict__ w2, const char* __restrict__ wo,
                                                  const char* __restrict__ x, char* __restrict__ out) {
    __shared__ __attribute__((aligned(1024))) char smem[65536];
    const int tid = threadIdx.x, lane = tid & 63;
    const int wid = __builtin_amdgcn_readfirstlane(tid >> 6);
    const int bx = blockIdx.x, xc = bx & 7, ii = bx >> 3, tm = (xc >> 1) * 4 + (ii >> 3), tn = (xc & 1) * 8 + (ii & 7);
    const unsigned lds0 = (unsigned)(size_t)(LAS char*)smem + wid * 16384;
    const char* asrc = act + ((size_t)wid * 64 + tm * 4) * 1024;
    unsigned av[4];
#pragma unroll
    for (int i = 0; i < 4; ++i) av[i] = lane * 16 + (i ^ wid) * 1024;
    const char* wsrc = w2 + (size_t)wid * 16 * 2048 + tn * 128;
    const unsigned wv0 = (lane >> 3) * 2048 + ((((lane >> 2) & 1) ^ ((lane >> 4) & 1)) * 64) + (lane & 3) * 16, wv1 = wv0 + 16384 - 1024;
    lds_cptr wp[2];
    {
        const int gq = lane >> 4, q4 = (lane >> 2) & 3, p = lane & 3;
#pragma unroll
        for (int n = 0; n < 2; ++n) wp[n] = (lds_cptr)smem + wid * 16384 + (gq >> 1) * 1024 + q4 * 128 + ((n ^ (q4 >> 1)) * 64) + (gq & 1) * 32 + p * 8;
    }
    f32x16 acc[4][2];
#pragma unroll
    for (int i = 0; i < 4; ++i) { acc[i][0] = (f32x16){}; acc[i][1] = (f32x16){}; }
    bf16x8 a0[4], a1[4], a2[4], a3[4], wfa[2], wfb[2];
#define G2_ISSUE(A, SL) do { \
        asm volatile("global_load_dwordx4 %0, %4, %8\n\tglobal_load_dwordx4 %1, %5, %8\n\tglobal_load_dwordx4 %2, %6, %8\n\tglobal_load_dwordx4 %3, %7, %8" \
                     : "=&v"(A[0]), "=&v"(A[1]), "=&v"(A[2]), "=&v"(A[3]) : "v"(av[0]), "v"(av[1]), "v"(av[2]), "v"(av[3]), "s"(asrc) : "memory"); \
        asm volatile("s_mov_b32 m0, %3\n\ts_nop 0\n\tglobal_load_lds_dwordx4 %0, %2\n\tglobal_load_lds_dwordx4 %1, %2 offset:1024" \
                     ::"v"(wv0), "v"(wv1), "s"(wsrc), "s"(lds0 + (SL) * 2048) : "memory"); \
        asrc += 262144; wsrc += 131072; } while (0)
#define G2_WAIT(N, A) asm volatile("s_waitcnt vmcnt(" #N ")" : "+v"(A[0]), "+v"(A[1]), "+v"(A[2]), "+v"(A[3])::"memory")
#define G2_WREAD(W, SL) do { _Pragma("unroll") for (int n = 0; n < 2; ++n) W[n] = cat8(tr_read(wp[n] + (SL) * 2048), tr_read(wp[n] + (SL) * 2048 + 512)); } while (0)
#define G2_MMA(A, W) do { _Pragma("unroll") for (int i = 0; i < 4; ++i) { acc[i][0] = MF32(W[0], A[i], acc[i][0]); acc[i][1] = MF32(W[1], A[i], acc[i][1]); } } while (0)
#define G2_STEP(N, AC, WC, AN, WN, SL, RD, IS) do { \
        if (RD) { G2_WAIT(N, AN); G2_WREAD(WN, ((SL) + 1) & 3); } SCHED(); G2_MMA(AC, WC); SCHED(); if (IS) G2_ISSUE(AC, SL); } while (0)
    G2_ISSUE(a0, 0); G2_ISSUE(a1, 1); G2_ISSUE(a2, 2); G2_ISSUE(a3, 3);
    G2_WAIT(18, a0); G2_WREAD(wfa, 0);
    for (int it = 0; it < 17; ++it) {
        if (it == 13) wsrc = wo + (size_t)wid * 16 * 2048 + tn * 128;
        G2_STEP(12, a0, wfa, a1, wfb, 0, true, true);
        G2_STEP(12, a1, wfb, a2, wfa, 1, true, true);
        G2_STEP(12, a2, wfa, a3, wfb, 2, true, true);
        G2_STEP(12, a3, wfb, a0, wfa, 3, true, true);
    }
    G2_STEP(12, a0, wfa, a1, wfb, 0, true, false);
    G2_STEP(6, a1, wfb, a2, wfa, 1, true, false);
    G2_STEP(0, a2, wfa, a3, wfb, 2, true, false);
    G2_STEP(0, a3, wfb, a0, wfa, 3, false, false);
#undef G2_STEP
#undef G2_MMA
#undef G2_WREAD
#undef G2_WAIT
#undef G2_ISSUE
    const size_t orow = (size_t)(tm * 128 + wid * 32 + (lane & 31)) * 2048 + tn * 128 + (lane >> 5) * 16;
    u32x4 xr[4];
#pragma unroll
    for (int c = 0; c < 4; ++c) xr[c] = *(const u32x4*)(x + orow + c * 32);
    char* mine = smem + wid * 16384 + lane * 16;
#pragma unroll
    for (int i = 0; i < 2; ++i)
#pragma unroll
        for (int n = 0; n < 2; ++n)
#pragma unroll
            for (int c = 0; c < 4; ++c)
                *(f32x4*)(mine + ((i * 2 + n) * 4 + c) * 1024) = (f32x4){acc[2 + i][n][4 * c], acc[2 + i][n][4 * c + 1], acc[2 + i][n][4 * c + 2], acc[2 + i][n][4 * c + 3]};
    __syncthreads();
    {
        const char* oth = smem + (wid ^ 2) * 16384 + lane * 16;
#pragma unroll
        for (int i = 0; i < 2; ++i)
#pragma unroll
            for (int n = 0; n < 2; ++n)
#pragma unroll
                for (int c = 0; c < 4; ++c) {
                    const f32x4 v = *(const f32x4*)(oth + ((i * 2 + n) * 4 + c) * 1024);
#pragma unroll
                    for (int e = 0; e < 4; ++e) acc[i][n][4 * c + e] += v[e];
                }
    }
    __syncthreads();
#pragma unroll
    for (int n = 0; n < 2; ++n)
#pragma unroll
        for (int c = 0; c < 4; ++c)
            *(f32x4*)(mine + (n * 4 + c) * 1024) = (f32x4){acc[1][n][4 * c], acc[1][n][4 * c + 1], acc[1][n][4 * c + 2], acc[1][n][4 * c + 3]};
    __syncthreads();
    {
        const char* oth = smem + (wid ^ 1) * 16384 + lane * 16;
#pragma unroll
        for (int n = 0; n < 2; ++n)
#pragma unroll
            for (int c = 0; c < 4; ++c) {
                const f32x4 v = *(const f32x4*)(oth + (n * 4 + c) * 1024);
#pragma unroll
                for (int e = 0; e < 4; ++e) acc[0][n][4 * c + e] += v[e];
            }
    }
#pragma unroll
    for (int n = 0; n < 2; ++n)
#pragma unroll
        for (int hp = 0; hp < 2; ++hp) {
            float v[8];
#pragma unroll
            for (int e = 0; e < 4; ++e) {
                unsigned a = __float_as_uint(acc[0][n][8 * hp + e]), b = __float_as_uint(acc[0][n][8 * hp + 4 + e]);
                swap32(a, b);
                v[e] = __uint_as_float(a); v[4 + e] = __uint_as_float(b);
            }
            const u32x4 xv = xr[n * 2 + hp];
            u32x4 w;
#pragma unroll
            for (int e = 0; e < 4; ++e) w[e] = cvtpk(v[2 * e] + bf_lo(xv[e]), v[2 * e + 1] + bf_hi(xv[e]));
            *(u32x4*)(out + orow + (n * 2 + hp) * 32) = w;
        }
}

extern "C" void kernel_launch(void* const* d_in, const int* in_sizes, int n_in, void* d_out, int out_size, void* d_ws, size_t ws_size,
                              hipStream_t stream) {
    const u16* x = (const u16*)d_in[0];
    const char* wqkv = (const char*)d_in[1];
    const char* wo = (const char*)d_in[2];
    const char* w1 = (const char*)d_in[4];
    const char* w3 = (const char*)d_in[5];
    const char* w2 = (const char*)d_in[6];
    char* ws = (char*)d_ws;
    k_ln<<<256, 512, 0, stream>>>(x, ws + WS_XN, (unsigned*)(ws + WS_CTR));
    k_gemm1<<<256, 512, 0, stream>>>(ws + WS_XN, wqkv, w1, w3, ws + WS_QKV, ws + WS_ACT);
    k_attn<<<256, 256, 0, stream>>>(ws + WS_QKV, ws + WS_ACT);
    k_gemm2<<<256, 256, 0, stream>>>(ws + WS_ACT, w2, wo, (const char*)x, (char*)d_out);
}
```

```cpp
#include <hip/hip_runtime.h>
#include <stdint.h>

typedef unsigned short u16;
typedef short bf16x8 __attribute__((ext_vector_type(8)));
typedef short s16x4 __attribute__((ext_vector_type(4)));
typedef float f32x2 __attribute__((ext_vector_type(2)));
typedef float f32x4 __attribute__((ext_vector_type(4)));
typedef float f32x16 __attribute__((ext_vector_type(16)));
typedef unsigned u32x2 __attribute__((ext_vector_type(2)));
typedef unsigned u32x4 __attribute__((ext_vector_type(4)));
typedef __bf16 bf16x2_t __attribute__((ext_vector_type(2)));
#define LAS __attribute__((address_space(3)))
typedef const LAS char* lds_cptr;

constexpr int SEQ = 2048, DM = 1024, FF = 3584, NH = 32;
constexpr size_t MB = 1024 * 1024;
constexpr size_t WS_CTR = 0;
constexpr size_t WS_XN = 4096;
constexpr size_t WS_QKV = WS_XN + 4 * MB;
constexpr size_t WS_ACT = WS_QKV + 12 * MB;
constexpr float QSCALE = 0.17677669529663687f * 1.4426950408889634f;

#define ABL_NODMA 0
#define ABL_NOLDS 0
#define ROLE_MODE 0
#define AT_PRIO 2
#define AT_ORDER 0
#define NO_ATTN 0
#define NO_GEMM 0
#define AT_NOBAR 0
#define AT_NODMA 0
#define ST_MODE 2
#define PREFETCH 1
#define SPLIT 0
#define G2_DELAY 16
#define AT_SPREAD 0
#define SCHED() __builtin_amdgcn_sched_barrier(0)
#define PIN(x) asm volatile("" : "+v"(x))
#define MF32(a, b, c) __builtin_amdgcn_mfma_f32_32x32x16_bf16(a, b, c, 0, 0, 0)
#define MF16(a, b, c) __builtin_amdgcn_mfma_f32_16x16x32_bf16(a, b, c, 0, 0, 0)

#define STAMP(i)
#define RSTAMP(i)
#define DIAG_OUT(K, a, b, c, d, e, f)

__device__ __forceinline__ unsigned cvtpk(float lo, float hi) {
    f32x2 v = {lo, hi};
    bf16x2_t b = __builtin_convertvector(v, bf16x2_t);
    return __builtin_bit_cast(unsigned, b);
}
__device__ __forceinline__ float bf_lo(unsigned w) { return __uint_as_float(w << 16); }
__device__ __forceinline__ float bf_hi(unsigned w) { return __uint_as_float(w & 0xffff0000u); }
__device__ __forceinline__ float rbf(float x) { return bf_lo(cvtpk(x, x)); }
__device__ __forceinline__ s16x4 tr_read(lds_cptr p) {
    return __builtin_bit_cast(s16x4, __builtin_amdgcn_ds_read_tr16_b64_v4i16((LAS s16x4*)p));
}
__device__ __forceinline__ bf16x8 cat8(s16x4 lo, s16x4 hi) { return __builtin_shufflevector(lo, hi, 0, 1, 2, 3, 4, 5, 6, 7); }
__device__ __forceinline__ void dma_v(const void* g, unsigned lds) {
    asm volatile("s_mov_b32 m0, %1\n\ts_nop 0\n\tglobal_load_lds_dwordx4 %0, off" ::"v"(g), "s"(lds) : "memory");
}
__device__ __forceinline__ void dma_s1(unsigned voff, const void* sbase, unsigned lds) {
    asm volatile("s_mov_b32 m0, %2\n\ts_nop 0\n\tglobal_load_lds_dwordx4 %0, %1" ::"v"(voff), "s"(sbase), "s"(lds) : "memory");
}
__device__ __forceinline__ void dma_s2(unsigned voff, const void* sbase, unsigned lds) {
    asm volatile("s_mov_b32 m0, %2\n\ts_nop 0\n\tglobal_load_lds_dwordx4 %0, %1\n\tglobal_load_lds_dwordx4 %0, %1 offset:1024" ::"v"(voff), "s"(sbase), "s"(lds) : "memory");
}
__device__ __forceinline__ void swap16(unsigned& a, unsigned& b) {
    auto r = __builtin_amdgcn_permlane16_swap(a, b, false, false);
    a = r[0]; b = r[1];
}
__device__ __forceinline__ void swap32(unsigned& a, unsigned& b) {
    auto r = __builtin_amdgcn_permlane32_swap(a, b, false, false);
    a = r[0]; b = r[1];
}

__global__ __launch_bounds__(512) void k_ln(const u16* __restrict__ x, char* __restrict__ xnF, unsigned* __restrict__ ctr) {
    __shared__ __attribute__((aligned(16))) char sm[16384];
    const int tid = threadIdx.x, lane = tid & 63, wid = tid >> 6;
    if (blockIdx.x == 0) { ctr[tid] = 0u; ctr[tid + 512] = 0u; }
    const int row = blockIdx.x * 8 + wid;
    const u32x4* src = (const u32x4*)(x + (size_t)row * DM);
    const u32x4 a = src[lane], b = src[64 + lane];
    float v[16];
#pragma unroll
    for (int i = 0; i < 4; ++i) { v[2 * i] = bf_lo(a[i]); v[2 * i + 1] = bf_hi(a[i]); v[8 + 2 * i] = bf_lo(b[i]); v[8 + 2 * i + 1] = bf_hi(b[i]); }
    float s = 0.f;
#pragma unroll
    for (int i = 0; i < 16; ++i) s += v[i];
#pragma unroll
    for (int o = 32; o >= 1; o >>= 1) s += __shfl_xor(s, o);
    const float mean = s * (1.0f / DM);
    float q = 0.f;
#pragma unroll
    for (int i = 0; i < 16; ++i) { const float d = v[i] - mean; q += d * d; }
#pragma unroll
    for (int o = 32; o >= 1; o >>= 1) q += __shfl_xor(q, o);
    const float mb = rbf(mean), vb = rbf(q * (1.0f / DM));
    const float sd = rbf(sqrtf(rbf(vb + 1e-5f)));
    u32x4 oa, ob;
#pragma unroll
    for (int i = 0; i < 4; ++i) {
        oa[i] = cvtpk(rbf(v[2 * i] - mb) / sd, rbf(v[2 * i + 1] - mb) / sd);
        ob[i] = cvtpk(rbf(v[8 + 2 * i] - mb) / sd, rbf(v[8 + 2 * i + 1] - mb) / sd);
    }
    *(u32x4*)(sm + lane * 128 + wid * 16) = oa;
    *(u32x4*)(sm + (64 + lane) * 128 + wid * 16) = ob;
    __syncthreads();
    const int row0 = blockIdx.x * 8;
#pragma unroll
    for (int i = 0; i < 2; ++i) {
        const int p = tid + i * 512, chunk = p >> 3, r = p & 7;
        const u32x4 w = *(const u32x4*)(sm + p * 16);
        *(u32x4*)(xnF + ((size_t)(chunk >> 2) * 128 + (row0 >> 4)) * 1024 + (chunk & 3) * 256 + ((row0 & 15) + r) * 16) = w;
    }
}

__device__ __forceinline__ unsigned silu_mul2(f32x2 g, f32x2 u) {
    const f32x2 t = g * -1.4426950408889634f;
    f32x2 e; e.x = __builtin_amdgcn_exp2f(t.x); e.y = __builtin_amdgcn_exp2f(t.y);
    e = e + 1.0f;
    f32x2 r; r.x = __builtin_amdgcn_rcpf(e.x); r.y = __builtin_amdgcn_rcpf(e.y);
    const f32x2 h = (g * u) * r;
    return cvtpk(h.x, h.y);
}
__device__ __forceinline__ u32x2 silu_mul4(f32x4 g, f32x4 u) {
    return (u32x2){silu_mul2((f32x2){g[0], g[1]}, (f32x2){u[0], u[1]}), silu_mul2((f32x2){g[2], g[3]}, (f32x2){u[2], u[3]})};
}
#define ST16(p, v) asm volatile("global_store_dwordx4 %0, %1, off sc1\n\ts_nop 1" ::"v"(p), "v"(v) : "memory")
__global__ __launch_bounds__(512, 2) void k_gemm1(const char* __restrict__ xnF, const char* __restrict__ wqkv, const char* __restrict__ w1,
                                                  const char* __restrict__ w3, char* __restrict__ qkv, char* __restrict__ act,
                                                  const char* __restrict__ w2, const char* __restrict__ wo) {
    __shared__ __attribute__((aligned(1024))) char smem[147456 + 1024];
    RSTAMP(4); STAMP(0);
    const int tid = threadIdx.x, lane = tid & 63;
    const int wid = __builtin_amdgcn_readfirstlane(tid >> 6), wr = wid & 3, wc = wid >> 2;
    const int bx = blockIdx.x, j = (bx & 7) + 8 * ((bx >> 3) & 3), rb = bx >> 5;
    const unsigned lds0 = (unsigned)(size_t)(LAS char*)smem;
    const int s0 = (j >= 10) ? 2 + 5 * (j - 10) : 0, odd = s0 & 1;
    const int pA = s0 + odd, pB = (j == 9) ? 0 : s0 + odd + 2, sg = odd ? s0 : s0 + 4;
    const char* bsrc[3]; unsigned binc[3];
#pragma unroll
    for (int i = 0; i < 3; ++i) {
        const int id = (wid + 8 * i) % 20, kb = id / 5, blk = id % 5;
        const int r = lane >> 3, j8 = lane & 7, f = ((r >> 1) & 1) | ((kb & 1) << 1), cp = (j8 >> 1) ^ f;
        const bool ffn = (j >= 10) || (j == 9 && blk >= 3);
        const char* base; int pitch, inrow;
        if (!ffn) { base = wqkv; pitch = 6144; inrow = (320 * j + 64 * blk) * 2 + cp * 32; }
        else {
            pitch = 7168;
            if (blk == 2) { base = (cp < 2) ? w1 : w3; inrow = sg * 64 + (cp & 1) * 32; }
            else { base = (blk == 0 || blk == 3) ? w1 : w3; inrow = ((blk < 2) ? pA : pB) * 64 + cp * 32; }
        }
        bsrc[i] = base + (size_t)(kb * 8 + r) * pitch + inrow + (j8 & 1) * 16;
        binc[i] = 32u * pitch;
    }
    const char* asrc = xnF + ((size_t)rb * 16 + 2 * wid) * 1024;
    const unsigned avoff = lane * 16;
    const unsigned adst = lds0 + wid * 2048, bdst = lds0 + 65536 + wid * 1024;
#define G1_DMA(SL) do { \
        dma_s2(avoff, asrc, adst + (SL) * 16384); asrc += 131072; \
        dma_v(bsrc[0], bdst + (SL) * 20480); bsrc[0] += binc[0]; \
        dma_v(bsrc[1], bdst + (SL) * 20480 + 8192); bsrc[1] += binc[1]; \
        if (wc == 0) { dma_v(bsrc[2], bdst + (SL) * 20480 + 16384); bsrc[2] += binc[2]; } } while (0)
    const lds_cptr pa = (lds_cptr)smem + wr * 4096 + lane * 16;
    lds_cptr pb[4], pe0, pe1;
    {
        const int g = lane >> 4, q = (lane >> 2) & 3, p = lane & 3, f = (q >> 1) | ((g & 1) << 1);
        const lds_cptr b0 = (lds_cptr)smem + 65536 + g * 5120 + q * 128 + p * 8;
#pragma unroll
        for (int c = 0; c < 4; ++c) pb[c] = b0 + ((c ^ f) << 5) + wc * 3072;
        pe0 = b0 + ((wc ^ f) << 5) + 2048; pe1 = b0 + (((2 + wc) ^ f) << 5) + 2048;
    }
    f32x4 acc[4][10];
#pragma unroll
    for (int m = 0; m < 4; ++m)
#pragma unroll
        for (int n = 0; n < 10; ++n) acc[m][n] = (f32x4){0.f, 0.f, 0.f, 0.f};
    G1_DMA(0); G1_DMA(1); G1_DMA(2);
    asm volatile("s_waitcnt vmcnt(8)\n\ts_barrier" ::: "memory");
    STAMP(1);
    if (wc == 1) asm volatile("s_barrier" ::: "memory");
    bf16x8 af[4], bf[10];
#define G1_STEP(SL, DO_DMA, VM, LAST) do { \
        if (!ABL_NOLDS || (SL) == 0) { \
        _Pragma("unroll") for (int c = 0; c < 4; ++c) { \
            bf[c] = cat8(tr_read(pb[c] + (SL) * 20480), tr_read(pb[c] + (SL) * 20480 + 512)); \
            bf[4 + c] = cat8(tr_read(pb[c] + (SL) * 20480 + 1024), tr_read(pb[c] + (SL) * 20480 + 1536)); } \
        bf[8] = cat8(tr_read(pe0 + (SL) * 20480), tr_read(pe0 + (SL) * 20480 + 512)); \
        bf[9] = cat8(tr_read(pe1 + (SL) * 20480), tr_read(pe1 + (SL) * 20480 + 512)); \
        _Pragma("unroll") for (int m = 0; m < 4; ++m) af[m] = *(const LAS bf16x8*)(pa + (SL) * 16384 + m * 1024); } \
        if (DO_DMA && !ABL_NODMA) G1_DMA(((SL) + 3) & 3); \
        asm volatile("s_waitcnt vmcnt(" #VM ") lgkmcnt(0)\n\ts_barrier" ::: "memory"); \
        SCHED(); __builtin_amdgcn_s_setprio(1); \
        _Pragma("unroll") for (int n = 0; n < 10; ++n) \
            _Pragma("unroll") for (int m = 0; m < 4; ++m) acc[m][n] = MF16(bf[n], af[m], acc[m][n]); \
        __builtin_amdgcn_s_setprio(0); SCHED(); \
        if (!(LAST) || wc == 0) asm volatile("s_barrier" ::: "memory"); } while (0)
    const char* pf = (wid < 7 ? w2 + (size_t)(bx * 7 + wid) * 4096 : wo + (size_t)bx * 8192);
    for (int it = 0; it < 7; ++it) {
        if (PREFETCH && it >= 1 && it < 5) {
            dma_s1(avoff, pf, lds0 + 147456); pf += 1024;
            if (wid == 7) { dma_s1(avoff, pf, lds0 + 147456); pf += 1024; }
        }
        G1_STEP(0, true, 8, false); G1_STEP(1, true, 8, false); G1_STEP(2, true, 8, false); G1_STEP(3, true, 8, false); }
    G1_STEP(0, true, 8, false); G1_STEP(1, false, 4, false); G1_STEP(2, false, 0, false); G1_STEP(3, false, 0, true);
    STAMP(2);
#undef G1_STEP
#undef G1_DMA
    int tz = threadIdx.x; asm volatile("" : "+v"(tz));
    const int fr = tz & 15, fq = (tz >> 4) & 3;
    const int tb = (rb * 16 + wr * 4) * 1024;
    const bool ffn_main = (j >= 10) || (j == 9 && wc == 1), ffn_ext = (j >= 10);
    const int cm = ((fq & 1) << 1) | (fq >> 1), ce = 2 * wc + (fq >> 1);
    if (ffn_main) {
        char* dst = act + (size_t)(wc ? pB : pA) * 131072 + tb + cm * 256 + fr * 16;
#pragma unroll
        for (int m = 0; m < 4; ++m)
#pragma unroll
            for (int sub = 0; sub < 2; ++sub) {
                const u32x2 a = silu_mul4(acc[m][2 * sub], acc[m][4 + 2 * sub]), b = silu_mul4(acc[m][2 * sub + 1], acc[m][4 + 2 * sub + 1]);
                unsigned a0 = a[0], a1 = a[1], b0 = b[0], b1 = b[1];
                swap16(a0, b0); swap16(a1, b1);
                ST16(dst + sub * 131072 + m * 1024, ((u32x4){a0, a1, b0, b1}));
            }
    } else {
        const int lo = tb + cm * 256 + ((fr ^ (4 * cm)) * 16);
#pragma unroll
        for (int sub = 0; sub < 4; ++sub) {
            const int cb = 10 * j + 6 * wc + sub;
            const float sc = cb < 32 ? QSCALE : 1.0f;
            char* dst = qkv + (size_t)cb * 131072 + lo;
#pragma unroll
            for (int m = 0; m < 4; ++m) {
                const f32x4 va = acc[m][2 * sub] * sc, vb = acc[m][2 * sub + 1] * sc;
                unsigned a0 = cvtpk(va[0], va[1]), a1 = cvtpk(va[2], va[3]), b0 = cvtpk(vb[0], vb[1]), b1 = cvtpk(vb[2], vb[3]);
                swap16(a0, b0); swap16(a1, b1);
                ST16(dst + m * 1024, ((u32x4){a0, a1, b0, b1}));
            }
        }
    }
    if (ffn_ext) {
        char* dst = act + (size_t)sg * 131072 + tb + (fq & 1) * 1024 + ce * 256 + fr * 16;
#pragma unroll
        for (int mp = 0; mp < 2; ++mp) {
            const u32x2 a = silu_mul4(acc[2 * mp][8], acc[2 * mp][9]), b = silu_mul4(acc[2 * mp + 1][8], acc[2 * mp + 1][9]);
            unsigned a0 = a[0], a1 = a[1], b0 = b[0], b1 = b[1];
            swap16(a0, b0); swap16(a1, b1);
            ST16(dst + mp * 2048, ((u32x4){a0, a1, b0, b1}));
        }
    } else {
        const int cb0 = 10 * j + 4;
        const float sc = cb0 < 32 ? QSCALE : 1.0f;
        char* dst = qkv + (size_t)(cb0 + (fq & 1)) * 131072 + tb + ce * 256 + ((fr ^ (4 * ce)) * 16);
#pragma unroll
        for (int m = 0; m < 4; ++m) {
            const f32x4 va = acc[m][8] * sc, vb = acc[m][9] * sc;
            unsigned a0 = cvtpk(va[0], va[1]), a1 = cvtpk(va[2], va[3]), b0 = cvtpk(vb[0], vb[1]), b1 = cvtpk(vb[2], vb[3]);
            swap16(a0, b0); swap16(a1, b1);
            ST16(dst + m * 1024, ((u32x4){a0, a1, b0, b1}));
        }
    }
    STAMP(3); RSTAMP(5); DIAG_OUT(1, ts0, ts1, ts2, ts3, ts4, ts5);
}

__device__ __forceinline__ void attn_role(char* smem, const int bx, const char* __restrict__ qkv, char* __restrict__ act, unsigned* __restrict__ ctr) {
    RSTAMP(4); STAMP(0);
    const int tid = threadIdx.x, lane = tid & 63;
    const int wid = __builtin_amdgcn_readfirstlane(tid >> 6);
    const int head = (bx & 7) * 4 + (bx >> 6), qb = (bx >> 3) & 7;
    __builtin_amdgcn_s_setprio(AT_PRIO);
    const char* Qf = qkv + (size_t)head * 131072;
    const char* Kf = Qf + 4 * MB;
    const char* Vr = Qf + 8 * MB;
    const unsigned lds0 = (unsigned)(size_t)(LAS char*)smem;
    const unsigned voff = lane * 16;
    const int tq0 = qb * 8 + wid * 2;
    unsigned fo[2];
#pragma unroll
    for (int s = 0; s < 2; ++s) { const int c = 2 * s + (lane >> 5); fo[s] = ((lane >> 4) & 1) * 1024 + c * 256 + (((lane & 15) ^ (4 * c)) * 16); }
    bf16x8 q[2][2];
#pragma unroll
    for (int i = 0; i < 2; ++i)
#pragma unroll
        for (int s = 0; s < 2; ++s) q[i][s] = *(const bf16x8*)(Qf + (tq0 + i) * 2048 + fo[s]);
    bf16x8 sel;
    { const short one = (((lane >> 2) & 1) == ((lane >> 4) & 1)) ? (short)0x3f80 : (short)0; sel = (bf16x8){one, one, one, one, one, one, one, one}; }
    const lds_cptr kp0 = (lds_cptr)smem + fo[0], kp1 = (lds_cptr)smem + fo[1];
    lds_cptr vp0, vp1;
    { const int h = lane >> 5, c = 2 * ((lane >> 4) & 1) + ((lane >> 1) & 1), q4 = (lane >> 2) & 3;
      vp0 = (lds_cptr)smem + 8192 + c * 256 + (h ^ c) * 64 + q4 * 16 + (lane & 1) * 8; vp1 = (lds_cptr)smem + 8192 + c * 256 + ((2 + h) ^ c) * 64 + q4 * 16 + (lane & 1) * 8; }
    volatile LAS unsigned* flag = (volatile LAS unsigned*)(smem);
    const unsigned ooff = ((lane >> 4) & 1) * 1024 + (lane >> 5) * 256 + (lane & 15) * 16;
#define AT_DMA(ST, SLOT) do { const unsigned d_ = lds0 + (SLOT) + wid * 2048; \
        dma_s2(voff, Kf + (ST) * 8192 + wid * 2048, d_); dma_s2(voff, Vr + (ST) * 8192 + wid * 2048, d_ + 8192); } while (0)
    for (int attempt = 0; attempt < 2; ++attempt) {
        f32x16 o[2]; f32x4 l[2];
        bf16x8 k0 = *(const bf16x8*)(Kf + fo[0]), k1 = *(const bf16x8*)(Kf + fo[1]);
        AT_DMA(0, 0); AT_DMA(1, 16384); AT_DMA(2, 32768);
        f32x16 negm[2];
        {
            float m0 = -INFINITY, m1 = -INFINITY;
            const int nt = attempt == 0 ? 1 : 64;
            for (int kt = 0; kt < nt; ++kt) {
                if (kt) { k0 = *(const bf16x8*)(Kf + kt * 2048 + fo[0]); k1 = *(const bf16x8*)(Kf + kt * 2048 + fo[1]); }
                f32x16 s0 = MF32(k0, q[0][0], (f32x16){}), s1 = MF32(k0, q[1][0], (f32x16){});
                s0 = MF32(k1, q[0][1], s0); s1 = MF32(k1, q[1][1], s1);
#pragma unroll
                for (int r = 0; r < 16; ++r) { m0 = fmaxf(m0, s0[r]); m1 = fmaxf(m1, s1[r]); }
            }
            m0 = fmaxf(m0, __shfl_xor(m0, 32)); m1 = fmaxf(m1, __shfl_xor(m1, 32));
#pragma unroll
            for (int r = 0; r < 16; ++r) { negm[0][r] = -m0; negm[1][r] = -m1; }
            PIN(negm[0]); PIN(negm[1]);
        }
        o[0] = (f32x16){}; o[1] = (f32x16){}; l[0] = (f32x4){}; l[1] = (f32x4){};
        asm volatile("s_waitcnt vmcnt(0)\n\ts_barrier" ::: "memory");
        bf16x8 kf0 = *(const LAS bf16x8*)(kp0), kf1 = *(const LAS bf16x8*)(kp1);
        bf16x8 vf0 = (bf16x8){}, vf1 = (bf16x8){};
        u32x4 pa0 = (u32x4){}, pa1 = (u32x4){}, pb0 = (u32x4){}, pb1 = (u32x4){};
        f32x16 sA = MF32(kf0, q[0][0], negm[0]); sA = MF32(kf1, q[0][1], sA);
        f32x16 sB;
#define MF16_ACC(acc, a, b) asm volatile("v_mfma_f32_16x16x32_bf16 %0, %1, %2, %0" : "+v"(acc) : "v"(a), "v"(b))
#define EX(X, i) X[i] = __builtin_amdgcn_exp2f(X[i])
#define PK(X, i) cvtpk(X[i], X[(i) + 1])
#define BC8(x) __builtin_bit_cast(bf16x8, x)
#define AT_UNIT(SC, PC0, PC1, SN, QN, NM, PP0, PP1, OP, LP, K0, K1, V0, V1, L0, L1, L2, L3) do { unsigned c0, c1, c2, c3, c4, c5, c6, c7; \
            SN = MF32(K0, QN[0], NM); SCHED(); L0; SCHED(); EX(SC, 0); EX(SC, 1); EX(SC, 2); SCHED(); \
            OP = MF32(V0, BC8(PP0), OP); SCHED(); L1; SCHED(); c0 = PK(SC, 0); EX(SC, 3); EX(SC, 4); EX(SC, 5); SCHED(); \
            SN = MF32(K1, QN[1], SN); SCHED(); L2; SCHED(); c1 = PK(SC, 2); c2 = PK(SC, 4); EX(SC, 6); EX(SC, 7); EX(SC, 8); SCHED(); \
            OP = MF32(V1, BC8(PP1), OP); SCHED(); L3; SCHED(); c3 = PK(SC, 6); EX(SC, 9); EX(SC, 10); EX(SC, 11); SCHED(); \
            MF16_ACC(LP, sel, PP0); SCHED(); c4 = PK(SC, 8); c5 = PK(SC, 10); EX(SC, 12); EX(SC, 13); SCHED(); \
            MF16_ACC(LP, sel, PP1); SCHED(); EX(SC, 14); EX(SC, 15); SCHED(); c6 = PK(SC, 12); SCHED(); c7 = PK(SC, 14); \
            PC0 = (u32x4){c0, c1, c2, c3}; PC1 = (u32x4){c4, c5, c6, c7}; SCHED(); } while (0)
#define AT_PIECE(ST, SLOT, V, H) dma_s1(voff, ((V) ? Vr : Kf) + ((ST) & 15) * 8192 + wid * 2048 + (H) * 1024, lds0 + (SLOT) + wid * 2048 + (H) * 1024 + (V) * 8192)
#define AT_STAGE(SO, SN_, DM, ST, SLA, SLB) do { _Pragma("unroll") for (int kt = 0; kt < 4; ++kt) { \
                const int ko = kt < 3 ? (SO) + (kt + 1) * 2048 : (SN_), vo = (SO) + kt * 2048; \
                bf16x8 kn0, kn1, vn0, vn1; \
                AT_UNIT(sA, pa0, pa1, sB, q[1], negm[1], pb0, pb1, o[1], l[1], kf0, kf1, vf0, vf1, \
                        kn0 = *(const LAS bf16x8*)(kp0 + ko), vn0 = cat8(tr_read(vp0 + vo), tr_read(vp1 + vo)), \
                        kn1 = *(const LAS bf16x8*)(kp1 + ko), vn1 = cat8(tr_read(vp0 + vo + 1024), tr_read(vp1 + vo + 1024))); \
                kf0 = kn0; kf1 = kn1; vf0 = vn0; vf1 = vn1; \
                AT_UNIT(sB, pb0, pb1, sA, q[0], negm[0], pa0, pa1, o[0], l[0], kf0, kf1, vf0, vf1, \
                        if (DM) AT_PIECE((ST) + (kt >> 1), (kt >> 1) ? (SLB) : (SLA), kt & 1, 0), {}, if (DM) AT_PIECE((ST) + (kt >> 1), (kt >> 1) ? (SLB) : (SLA), kt & 1, 1), {}); } } while (0)
#define NXT(x) ((x) == 65536 ? 0 : (x) + 16384)
        int s0 = 0;
        for (int k = 0; k < 8; ++k) {
            const int s1 = NXT(s0), s2 = NXT(s1), s3 = NXT(s2), s4 = NXT(s3);
            AT_STAGE(s0, s1, !AT_NODMA, 2 * k + 3, s3, s4);
            AT_STAGE(s1, s2, false, 0, 0, 0);
            s0 = s2;
            if (!AT_NOBAR) asm volatile("s_waitcnt vmcnt(0)\n\ts_barrier" ::: "memory");
        }
#undef NXT
#undef AT_STAGE
#undef AT_PIECE
        o[1] = MF32(vf0, BC8(pb0), o[1]); o[1] = MF32(vf1, BC8(pb1), o[1]);
        l[1] = MF16(sel, BC8(pb0), l[1]); l[1] = MF16(sel, BC8(pb1), l[1]);
#undef AT_UNIT
        asm volatile("s_nop 7\n\ts_nop 7" : "+v"(l[0]), "+v"(l[1]));
        bool bad = false;
#pragma unroll
        for (int i = 0; i < 2; ++i) {
            const float li = l[i][0], r = 1.0f / li;
            bad |= !(li < 1.0995e12f);
#pragma unroll
            for (int e = 0; e < 16; ++e) { o[i][e] *= r; bad |= !(fabsf(o[i][e]) < INFINITY); }
        }
        if (lane == 0) flag[wid] = 0u;
        if (bad) flag[wid] = 1u;
#pragma unroll
        for (int i = 0; i < 2; ++i)
#pragma unroll
            for (int s = 0; s < 2; ++s) {
                unsigned a0 = cvtpk(o[i][8 * s + 0], o[i][8 * s + 1]), a1 = cvtpk(o[i][8 * s + 2], o[i][8 * s + 3]);
                unsigned b0 = cvtpk(o[i][8 * s + 4], o[i][8 * s + 5]), b1 = cvtpk(o[i][8 * s + 6], o[i][8 * s + 7]);
                swap32(a0, b0); swap32(a1, b1);
                const u32x4 w = (u32x4){a0, a1, b0, b1};
                asm volatile("global_store_dwordx4 %0, %1, %2 sc1\n\ts_nop 1" ::"v"(ooff), "v"(w), "s"(act + (size_t)(112 + head) * 131072 + (tq0 + i) * 2048 + s * 512) : "memory");
            }
        asm volatile("s_waitcnt vmcnt(0) lgkmcnt(0)\n\ts_barrier" ::: "memory");
        const unsigned redo = flag[0] | flag[1] | flag[2] | flag[3];
        if (attempt == 1 || !__builtin_amdgcn_readfirstlane(redo)) break;
        asm volatile("s_waitcnt lgkmcnt(0)\n\ts_barrier" ::: "memory");
    }
#undef AT_DMA
    if (tid == 0) __hip_atomic_fetch_add(ctr + qb * 64, 1u, __ATOMIC_RELAXED, __HIP_MEMORY_SCOPE_AGENT);
    STAMP(3); RSTAMP(5); DIAG_OUT(2, ts0, tA, tB, ts3, ts4, ts5);
}

__device__ __forceinline__ void gemm2_role(char* smem, const int bx, const char* __restrict__ act, const char* __restrict__ w2, const char* __restrict__ wo,
                                           const char* __restrict__ x, char* __restrict__ out, unsigned* __restrict__ ctr) {
    RSTAMP(4); STAMP(0);
    const int tid = threadIdx.x, lane = tid & 63;
    const int wid = __builtin_amdgcn_readfirstlane(tid >> 6);
    const int xc = bx & 7, ii = bx >> 3, tm = (xc >> 1) * 4 + (ii >> 3), tn = (xc & 1) * 8 + (ii & 7);
    const unsigned lds0 = (unsigned)(size_t)(LAS char*)smem + wid * 16384;
    if (G2_DELAY) __builtin_amdgcn_s_sleep(G2_DELAY);
    const char* asrc = act + (size_t)(wid >> 1) * 131072 + tm * 8192 + (wid & 1) * 512;
    unsigned av[4];
#pragma unroll
    for (int i = 0; i < 4; ++i) av[i] = (i ^ wid) * 2048 + ((lane >> 4) & 1) * 1024 + (lane >> 5) * 256 + (lane & 15) * 16;
    const char* wsrc = w2 + (size_t)wid * 16 * 2048 + tn * 128;
    const unsigned wv0 = (lane >> 3) * 2048 + ((((lane >> 2) & 1) ^ ((lane >> 4) & 1)) * 64) + (lane & 3) * 16, wv1 = wv0 + 16384 - 1024;
    lds_cptr wp[2];
    {
        const int gq = lane >> 4, q4 = (lane >> 2) & 3, p = lane & 3;
#pragma unroll
        for (int n = 0; n < 2; ++n) wp[n] = (lds_cptr)smem + wid * 16384 + (gq >> 1) * 1024 + q4 * 128 + ((n ^ (q4 >> 1)) * 64) + (gq & 1) * 32 + p * 8;
    }
    f32x16 acc[4][2];
#pragma unroll
    for (int i = 0; i < 4; ++i) { acc[i][0] = (f32x16){}; acc[i][1] = (f32x16){}; }
    bf16x8 a0[4], a1[4], a2[4], a3[4], wfa[2], wfb[2];
#define G2_LDA(A, SC) do { \
        asm volatile("global_load_dwordx4 %0, %4, %8" SC "\n\tglobal_load_dwordx4 %1, %5, %8" SC "\n\tglobal_load_dwordx4 %2, %6, %8" SC "\n\tglobal_load_dwordx4 %3, %7, %8" SC \
                     : "=&v"(A[0]), "=&v"(A[1]), "=&v"(A[2]), "=&v"(A[3]) : "v"(av[0]), "v"(av[1]), "v"(av[2]), "v"(av[3]), "s"(asrc) : "memory"); \
        asrc += 262144; } while (0)
#define G2_LDW(SL) do { \
        asm volatile("s_mov_b32 m0, %3\n\ts_nop 0\n\tglobal_load_lds_dwordx4 %0, %2\n\tglobal_load_lds_dwordx4 %1, %2 offset:1024" \
                     ::"v"(wv0), "v"(wv1), "s"(wsrc), "s"(lds0 + (SL) * 2048) : "memory"); \
        wsrc += 131072; } while (0)
#define G2_WAIT(N, A) asm volatile("s_waitcnt vmcnt(" #N ")" : "+v"(A[0]), "+v"(A[1]), "+v"(A[2]), "+v"(A[3])::"memory")
#define G2_WREAD(W, SL) do { _Pragma("unroll") for (int n = 0; n < 2; ++n) W[n] = cat8(tr_read(wp[n] + (SL) * 2048), tr_read(wp[n] + (SL) * 2048 + 512)); } while (0)
#define G2_MMA(A, W) do { _Pragma("unroll") for (int i = 0; i < 4; ++i) { acc[i][0] = MF32(W[0], A[i], acc[i][0]); acc[i][1] = MF32(W[1], A[i], acc[i][1]); } } while (0)
#define G2_STEP(N, AC, WC, AN, WN, SL, IA, IW, SC) do { \
        if ((N) >= 0) G2_WAIT(N, AN); if ((N) >= -1) G2_WREAD(WN, ((SL) + 1) & 7); SCHED(); G2_MMA(AC, WC); SCHED(); if (IA) G2_LDA(AC, SC); if (IW) G2_LDW(SL); } while (0)
#define G2_STEP4(N0, N1, N2, N3, SL, IA, IW, SC) do { \
        G2_STEP(N0, a0, wfa, a1, wfb, (SL), IA, IW, SC); G2_STEP(N1, a1, wfb, a2, wfa, (SL) + 1, IA, IW, SC); \
        G2_STEP(N2, a2, wfa, a3, wfb, (SL) + 2, IA, IW, SC); G2_STEP(N3, a3, wfb, a0, wfa, (SL) + 3, IA, IW, SC); } while (0)
    G2_LDW(0); G2_LDW(1); G2_LDW(2); G2_LDW(3);
    G2_LDA(a0, ""); G2_LDW(4); G2_LDA(a1, ""); G2_LDW(5); G2_LDA(a2, ""); G2_LDW(6); G2_LDA(a3, ""); G2_LDW(7);
    G2_WAIT(20, a0); STAMP(1); G2_WREAD(wfa, 0);
    for (int it = 0; it < 6; ++it) { G2_STEP4(14, 14, 14, 14, 0, true, true, ""); G2_STEP4(14, 14, 14, 14, 4, true, true, ""); }
    wsrc = wo + (size_t)wid * 16 * 2048 + tn * 128;
    G2_STEP4(14, 14, 14, 14, 0, true, true, "");
    G2_STEP4(14, 10, 6, -1, 4, false, true, "");
    const size_t orow = (size_t)(tm * 128 + wid * 32 + (lane & 31)) * 2048 + tn * 128 + (lane >> 5) * 16;
    u32x4 xr[4];
#pragma unroll
    for (int c = 0; c < 4; ++c) xr[c] = *(const u32x4*)(x + orow + c * 32);
    {
        const unsigned long long t0 = __builtin_amdgcn_s_memrealtime();
        while ((unsigned)__builtin_amdgcn_readfirstlane(__hip_atomic_load(ctr + (tm >> 1) * 64, __ATOMIC_RELAXED, __HIP_MEMORY_SCOPE_AGENT)) < 32u) {
            if (__builtin_amdgcn_s_memrealtime() - t0 > 5000000ull) break;
            __builtin_amdgcn_s_sleep(2);
        }
        __builtin_amdgcn_fence(__ATOMIC_ACQUIRE, "wavefront");
        asm volatile("" ::: "memory");
    }
    G2_LDA(a0, " sc1"); G2_LDA(a1, " sc1"); G2_LDA(a2, " sc1"); G2_LDA(a3, " sc1");
    G2_WAIT(12, a0);
    G2_STEP4(8, 10, 12, 14, 0, true, true, " sc1");
    G2_STEP4(14, 14, 14, 14, 4, true, true, " sc1");
    G2_STEP4(14, 12, 10, 8, 0, true, false, " sc1");
    G2_STEP4(8, 4, 0, -2, 4, false, false, "");
    STAMP(2);
#undef G2_STEP4
#undef G2_STEP
#undef G2_MMA
#undef G2_WREAD
#undef G2_WAIT
#undef G2_LDW
#undef G2_LDA
    char* mine = smem + wid * 16384 + lane * 16;
#pragma unroll
    for (int i = 0; i < 2; ++i)
#pragma unroll
        for (int n = 0; n < 2; ++n)
#pragma unroll
            for (int c = 0; c < 4; ++c)
                *(f32x4*)(mine + ((i * 2 + n) * 4 + c) * 1024) = (f32x4){acc[2 + i][n][4 * c], acc[2 + i][n][4 * c + 1], acc[2 + i][n][4 * c + 2], acc[2 + i][n][4 * c + 3]};
    __syncthreads();
    {
        const char* oth = smem + (wid ^ 2) * 16384 + lane * 16;
#pragma unroll
        for (int i = 0; i < 2; ++i)
#pragma unroll
            for (int n = 0; n < 2; ++n)
#pragma unroll
                for (int c = 0; c < 4; ++c) {
                    const f32x4 v = *(const f32x4*)(oth + ((i * 2 + n) * 4 + c) * 1024);
#pragma unroll
                    for (int e = 0; e < 4; ++e) acc[i][n][4 * c + e] += v[e];
                }
    }
    __syncthreads();
#pragma unroll
    for (int n = 0; n < 2; ++n)
#pragma unroll
        for (int c = 0; c < 4; ++c)
            *(f32x4*)(mine + (n * 4 + c) * 1024) = (f32x4){acc[1][n][4 * c], acc[1][n][4 * c + 1], acc[1][n][4 * c + 2], acc[1][n][4 * c + 3]};
    __syncthreads();
    {
        const char* oth = smem + (wid ^ 1) * 16384 + lane * 16;
#pragma unroll
        for (int n = 0; n < 2; ++n)
#pragma unroll
            for (int c = 0; c < 4; ++c) {
                const f32x4 v = *(const f32x4*)(oth + (n * 4 + c) * 1024);
#pragma unroll
                for (int e = 0; e < 4; ++e) acc[0][n][4 * c + e] += v[e];
            }
    }
#pragma unroll
    for (int n = 0; n < 2; ++n)
#pragma unroll
        for (int hp = 0; hp < 2; ++hp) {
            float v[8];
#pragma unroll
            for (int e = 0; e < 4; ++e) {
                unsigned a = __float_as_uint(acc[0][n][8 * hp + e]), b = __float_as_uint(acc[0][n][8 * hp + 4 + e]);
                swap32(a, b);
                v[e] = __uint_as_float(a); v[4 + e] = __uint_as_float(b);
            }
            const u32x4 xv = xr[n * 2 + hp];
            u32x4 w;
#pragma unroll
            for (int e = 0; e < 4; ++e) w[e] = cvtpk(v[2 * e] + bf_lo(xv[e]), v[2 * e + 1] + bf_hi(xv[e]));
            *(u32x4*)(out + orow + (n * 2 + hp) * 32) = w;
        }
    STAMP(3); RSTAMP(5); DIAG_OUT(3, ts0, ts1, ts2, ts3, ts4, ts5); DIAG_OUT(0, ts0, ts6, ts7, ts3, ts4, ts5);
}

__global__ __launch_bounds__(256, 2) void k_fused(const char* __restrict__ qkv, char* __restrict__ act, const char* __restrict__ w2, const char* __restrict__ wo,
                                                  const char* __restrict__ x, char* __restrict__ out, unsigned* __restrict__ ctr, int boff) {
    __shared__ __attribute__((aligned(1024))) char smem[81920];
    const int b = blockIdx.x + boff;
    if (b < 256) {
        if (NO_ATTN) { if (threadIdx.x == 0) __hip_atomic_fetch_add(ctr + ((b >> 3) & 7) * 64, 1u, __ATOMIC_RELAXED, __HIP_MEMORY_SCOPE_AGENT); }
        else attn_role(smem, b, qkv, act, ctr);
    } else if (!NO_GEMM) gemm2_role(smem, b - 256, act, w2, wo, x, out, ctr);
}

extern "C" void kernel_launch(void* const* d_in, const int* in_sizes, int n_in, void* d_out, int out_size, void* d_ws, size_t ws_size,
                              hipStream_t stream) {
    const u16* x = (const u16*)d_in[0];
    const char* wqkv = (const char*)d_in[1];
    const char* wo = (const char*)d_in[2];
    const char* w1 = (const char*)d_in[4];
    const char* w3 = (const char*)d_in[5];
    const char* w2 = (const char*)d_in[6];
    char* ws = (char*)d_ws;
    k_ln<<<256, 512, 0, stream>>>(x, ws + WS_XN, (unsigned*)(ws + WS_CTR));
    k_gemm1<<<256, 512, 0, stream>>>(ws + WS_XN, wqkv, w1, w3, ws + WS_QKV, ws + WS_ACT, w2, wo);
    k_fused<<<512, 256, 0, stream>>>(ws + WS_QKV, ws + WS_ACT, w2, wo, (const char*)x, (char*)d_out, (unsigned*)(ws + WS_CTR), 0);
}
```

```cpp
#include <hip/hip_runtime.h>
#include <stdint.h>

typedef unsigned short u16;
typedef short bf16x8 __attribute__((ext_vector_type(8)));
typedef short s16x4 __attribute__((ext_vector_type(4)));
typedef float f32x2 __attribute__((ext_vector_type(2)));
typedef float f32x4 __attribute__((ext_vector_type(4)));
typedef float f32x16 __attribute__((ext_vector_type(16)));
typedef unsigned u32x2 __attribute__((ext_vector_type(2)));
typedef unsigned u32x4 __attribute__((ext_vector_type(4)));
typedef __bf16 bf16x2_t __attribute__((ext_vector_type(2)));
#define LAS __attribute__((address_space(3)))
typedef const LAS char* lds_cptr;

constexpr int SEQ = 2048, DM = 1024, FF = 3584, NH = 32;
constexpr size_t MB = 1024 * 1024;
constexpr size_t WS_CTR = 0;
constexpr size_t WS_XN = 65536;
constexpr size_t WS_QKV = WS_XN + 4 * MB;
constexpr size_t WS_ACT = WS_QKV + 12 * MB;
constexpr float QSCALE = 0.17677669529663687f * 1.4426950408889634f;

#define ABL_NODMA 0
#define ABL_NOLDS 0
#define ROLE_MODE 0
#define AT_PRIO 2
#define AT_ORDER 0
#define NO_ATTN 0
#define NO_GEMM 0
#define AT_NOBAR 0
#define AT_NODMA 0
#define ST_MODE 2
#define PREFETCH 1
#define SPLIT 0
#define G2_DELAY 16
#define AT_SPREAD 0
#define DMA_FIRST 0
#define P3_MODE 0
#define VS_(x) #x
#define VS(x) VS_(x)
#define VA 10
#define VB 9
#define PF_ROUNDS 1
#define ACQ_MODE 0
#define MORD 3
#define SCHED() __builtin_amdgcn_sched_barrier(0)
#define PIN(x) asm volatile("" : "+v"(x))
#define MF32(a, b, c) __builtin_amdgcn_mfma_f32_32x32x16_bf16(a, b, c, 0, 0, 0)
#define MF16(a, b, c) __builtin_amdgcn_mfma_f32_16x16x32_bf16(a, b, c, 0, 0, 0)

#define MF16_ACC(acc, a, b) asm volatile("v_mfma_f32_16x16x32_bf16 %0, %1, %2, %0" : "+v"(acc) : "v"(a), "v"(b))

#define STAMP(i)
#define RSTAMP(i)
#define DIAG_OUT(K, a, b, c, d, e, f)
#define STEPSTAMP()
#define SEGSTAMP(k)

__device__ __forceinline__ unsigned cvtpk(float lo, float hi) {
    f32x2 v = {lo, hi};
    bf16x2_t b = __builtin_convertvector(v, bf16x2_t);
    return __builtin_bit_cast(unsigned, b);
}
__device__ __forceinline__ float bf_lo(unsigned w) { return __uint_as_float(w << 16); }
__device__ __forceinline__ float bf_hi(unsigned w) { return __uint_as_float(w & 0xffff0000u); }
__device__ __forceinline__ float rbf(float x) { return bf_lo(cvtpk(x, x)); }
__device__ __forceinline__ s16x4 tr_read(lds_cptr p) {
    return __builtin_bit_cast(s16x4, __builtin_amdgcn_ds_read_tr16_b64_v4i16((LAS s16x4*)p));
}
__device__ __forceinline__ bf16x8 cat8(s16x4 lo, s16x4 hi) { return __builtin_shufflevector(lo, hi, 0, 1, 2, 3, 4, 5, 6, 7); }
__device__ __forceinline__ void dma_v(const void* g, unsigned lds) {
    asm volatile("s_mov_b32 m0, %1\n\ts_nop 0\n\tglobal_load_lds_dwordx4 %0, off" ::"v"(g), "s"(lds) : "memory");
}
__device__ __forceinline__ void dma_s1(unsigned voff, const void* sbase, unsigned lds) {
    asm volatile("s_mov_b32 m0, %2\n\ts_nop 0\n\tglobal_load_lds_dwordx4 %0, %1" ::"v"(voff), "s"(sbase), "s"(lds) : "memory");
}
__device__ __forceinline__ void dma_s2(unsigned voff, const void* sbase, unsigned lds) {
    asm volatile("s_mov_b32 m0, %2\n\ts_nop 0\n\tglobal_load_lds_dwordx4 %0, %1\n\tglobal_load_lds_dwordx4 %0, %1 offset:1024" ::"v"(voff), "s"(sbase), "s"(lds) : "memory");
}
__device__ __forceinline__ void swap16(unsigned& a, unsigned& b) {
    auto r = __builtin_amdgcn_permlane16_swap(a, b, false, false);
    a = r[0]; b = r[1];
}
__device__ __forceinline__ void swap32(unsigned& a, unsigned& b) {
    auto r = __builtin_amdgcn_permlane32_swap(a, b, false, false);
    a = r[0]; b = r[1];
}

__device__ __forceinline__ float wave_sum(float v) {
    v += __builtin_bit_cast(float, __builtin_amdgcn_update_dpp(0, __builtin_bit_cast(int, v), 0xB1, 0xf, 0xf, true));
    v += __builtin_bit_cast(float, __builtin_amdgcn_update_dpp(0, __builtin_bit_cast(int, v), 0x4E, 0xf, 0xf, true));
    v += __builtin_bit_cast(float, __builtin_amdgcn_update_dpp(0, __builtin_bit_cast(int, v), 0x141, 0xf, 0xf, true));
    v += __builtin_bit_cast(float, __builtin_amdgcn_update_dpp(0, __builtin_bit_cast(int, v), 0x140, 0xf, 0xf, true));
    auto a = __builtin_amdgcn_permlane16_swap(__float_as_uint(v), __float_as_uint(v), false, false);
    v = __uint_as_float(a[0]) + __uint_as_float(a[1]);
    auto b = __builtin_amdgcn_permlane32_swap(__float_as_uint(v), __float_as_uint(v), false, false);
    return __uint_as_float(b[0]) + __uint_as_float(b[1]);
}
__global__ __launch_bounds__(512) void k_ln(const u16* __restrict__ x, char* __restrict__ xnF, unsigned* __restrict__ ctr,
                                            const char* __restrict__ wqkv, const char* __restrict__ w1, const char* __restrict__ w3) {
    __shared__ __attribute__((aligned(16))) char sm[16384];
    const int tid = threadIdx.x, lane = tid & 63, wid = tid >> 6;
    if (blockIdx.x >= 256) {
        u32x4 pw[3 * PF_ROUNDS + 1];
#pragma unroll
        for (int i = 0; i < PF_ROUNDS; ++i) {
            const size_t o = (size_t)i * 2097152 + (size_t)((blockIdx.x - 256) * 512 + tid) * 16;
            pw[3 * i] = *(const u32x4*)(wqkv + o); pw[3 * i + 1] = *(const u32x4*)(w1 + o); pw[3 * i + 2] = *(const u32x4*)(w3 + o);
        }
#pragma unroll
        for (int i = 0; i < 3 * PF_ROUNDS; ++i) asm volatile("" ::"v"(pw[i]));
        return;
    }
    if (blockIdx.x == 0) { ctr[tid] = 0u; ctr[tid + 512] = 0u; }
    const int row = blockIdx.x * 8 + wid;
    const u32x4* src = (const u32x4*)(x + (size_t)row * DM);
    const u32x4 a = src[lane], b = src[64 + lane];
    float v[16];
#pragma unroll
    for (int i = 0; i < 4; ++i) { v[2 * i] = bf_lo(a[i]); v[2 * i + 1] = bf_hi(a[i]); v[8 + 2 * i] = bf_lo(b[i]); v[8 + 2 * i + 1] = bf_hi(b[i]); }
    float s = 0.f;
#pragma unroll
    for (int i = 0; i < 16; ++i) s += v[i];
    s = wave_sum(s);
    const float mean = s * (1.0f / DM);
    float q = 0.f;
#pragma unroll
    for (int i = 0; i < 16; ++i) { const float d = v[i] - mean; q += d * d; }
    q = wave_sum(q);
    const float mb = rbf(mean), vb = rbf(q * (1.0f / DM));
    const float rs = 1.0f / rbf(sqrtf(rbf(vb + 1e-5f)));
    u32x4 oa, ob;
#pragma unroll
    for (int i = 0; i < 4; ++i) {
        oa[i] = cvtpk(rbf(v[2 * i] - mb) * rs, rbf(v[2 * i + 1] - mb) * rs);
        ob[i] = cvtpk(rbf(v[8 + 2 * i] - mb) * rs, rbf(v[8 + 2 * i + 1] - mb) * rs);
    }
    *(u32x4*)(sm + lane * 128 + wid * 16) = oa;
    *(u32x4*)(sm + (64 + lane) * 128 + wid * 16) = ob;
    __syncthreads();
    const int row0 = blockIdx.x * 8;
#pragma unroll
    for (int i = 0; i < 2; ++i) {
        const int p = tid + i * 512, chunk = p >> 3, r = p & 7;
        const u32x4 w = *(const u32x4*)(sm + p * 16);
        *(u32x4*)(xnF + ((size_t)(chunk >> 2) * 128 + (row0 >> 4)) * 1024 + (chunk & 3) * 256 + ((row0 & 15) + r) * 16) = w;
    }
}

__device__ __forceinline__ unsigned silu_mul2(f32x2 g, f32x2 u) {
    const f32x2 t = g * -1.4426950408889634f;
    f32x2 e; e.x = __builtin_amdgcn_exp2f(t.x); e.y = __builtin_amdgcn_exp2f(t.y);
    e = e + 1.0f;
    f32x2 r; r.x = __builtin_amdgcn_rcpf(e.x); r.y = __builtin_amdgcn_rcpf(e.y);
    const f32x2 h = (g * u) * r;
    return cvtpk(h.x, h.y);
}
__device__ __forceinline__ u32x2 silu_mul4(f32x4 g, f32x4 u) {
    return (u32x2){silu_mul2((f32x2){g[0], g[1]}, (f32x2){u[0], u[1]}), silu_mul2((f32x2){g[2], g[3]}, (f32x2){u[2], u[3]})};
}
#define ST16(p, v) asm volatile("global_store_dwordx4 %0, %1, off sc1\n\ts_nop 1" ::"v"(p), "v"(v) : "memory")
__global__ __launch_bounds__(512, 2) void k_gemm1(const char* __restrict__ xnF, const char* __restrict__ wqkv, const char* __restrict__ w1,
                                                  const char* __restrict__ w3, char* __restrict__ qkv, char* __restrict__ act,
                                                  const char* __restrict__ w2, const char* __restrict__ wo) {
    __shared__ __attribute__((aligned(1024))) char smem[147456 + 2048];
    RSTAMP(4); STAMP(0);
    const int tid = threadIdx.x, lane = tid & 63;
    const int wid = __builtin_amdgcn_readfirstlane(tid >> 6), wr = wid & 3, wc = wid >> 2;
    const int bx = blockIdx.x, j = (bx & 7) + 8 * ((bx >> 3) & 3), rb = bx >> 5;
    const unsigned lds0 = (unsigned)(size_t)(LAS char*)smem;
    const bool tq = j < 8;
    const int qb0 = 3 * j, qbx = tq ? 3 * j + 2 : 16 + j, pA = 2 * j - 8, pB = tq ? j : 2 * j - 7;
    const char* bsrc[3]; unsigned binc[3];
#pragma unroll
    for (int i = 0; i < 3; ++i) {
        const int id = i < 2 ? wid + 8 * i : 16 + wr, kb = id / 5, blk = id % 5;
        const int r = (i < 2 || P3_MODE != 0) ? lane >> 3 : ((lane >> 3) & 3) + 4 * wc, j8 = lane & 7, f = ((r >> 1) & 1) | ((kb & 1) << 1), cp = (j8 >> 1) ^ f;
        const bool ffn = blk >= 3 || (blk < 2 && !tq);
        const int pitch = ffn ? 7168 : 6144;
        const char* col = !ffn ? wqkv + (blk == 2 ? qbx : qb0 + blk) * 128 : ((blk == 0 || blk == 3) ? w1 : w3) + ((blk < 2) ? pA : pB) * 128;
        bsrc[i] = col + (size_t)((kb * 8 + r) * pitch + cp * 32 + (j8 & 1) * 16);
        binc[i] = 32u * pitch;
    }
    const char* asrc = xnF + ((size_t)rb * 16 + 2 * wid) * 1024;
    const unsigned avoff = lane * 16;
    const unsigned adst = lds0 + wid * 2048, bdst = lds0 + 65536 + wid * 1024;
    const unsigned hdst = lds0 + 65536 + 16384 + wr * 1024 + (P3_MODE == 0 ? wc * 512 : 0);
#define G1_DMA(SL) do { \
        dma_s2(avoff, asrc, adst + (SL) * 16384); asrc += 131072; \
        dma_v(bsrc[0], bdst + (SL) * 20480); bsrc[0] += binc[0]; \
        dma_v(bsrc[1], bdst + (SL) * 20480 + 8192); bsrc[1] += binc[1]; \
        if (P3_MODE == 0) asm volatile("s_mov_b32 m0, %1\n\ts_mov_b32 exec_hi, 0\n\tglobal_load_lds_dwordx4 %0, off\n\ts_mov_b32 exec_hi, -1" ::"v"(bsrc[2]), "s"(hdst + (SL) * 20480) : "memory"); \
        else if (P3_MODE == 1 || wc == 0) dma_v(bsrc[2], hdst + (SL) * 20480); \
        bsrc[2] += binc[2]; } while (0)
    G1_DMA(0); G1_DMA(1); G1_DMA(2);
    const lds_cptr pa = (lds_cptr)smem + wr * 4096 + lane * 16;
    lds_cptr pb[4], pe0, pe1;
    {
        const int g = lane >> 4, q = (lane >> 2) & 3, p = lane & 3, f = (q >> 1) | ((g & 1) << 1);
        const lds_cptr b0 = (lds_cptr)smem + 65536 + g * 5120 + q * 128 + p * 8;
#pragma unroll
        for (int c = 0; c < 4; ++c) pb[c] = b0 + ((c ^ f) << 5) + wc * 3072;
        pe0 = b0 + ((wc ^ f) << 5) + 2048; pe1 = b0 + (((2 + wc) ^ f) << 5) + 2048;
    }
    f32x4 acc[4][10];
#pragma unroll
    for (int m = 0; m < 4; ++m)
#pragma unroll
        for (int n = 0; n < 10; ++n) { acc[m][n] = (f32x4){0.f, 0.f, 0.f, 0.f}; PIN(acc[m][n]); }
    asm volatile("s_waitcnt vmcnt(" VS(VA) ")\n\ts_barrier" ::: "memory");
    STAMP(1);
    if (wc == 1) asm volatile("s_barrier" ::: "memory");
    bf16x8 af[4], bf[10];
#define BFR(P, O) cat8(tr_read((P) + (O)), tr_read((P) + (O) + 512))
#define G1_READS(SL) do { \
        _Pragma("unroll") for (int c = 0; c < 4; ++c) { bf[c] = BFR(pb[c], (SL) * 20480); bf[4 + c] = BFR(pb[c], (SL) * 20480 + 1024); } \
        bf[8] = BFR(pe0, (SL) * 20480); bf[9] = BFR(pe1, (SL) * 20480); \
        _Pragma("unroll") for (int m = 0; m < 4; ++m) af[m] = *(const LAS bf16x8*)(pa + (SL) * 16384 + m * 1024); } while (0)
#define G1_STEP(SL, DMA, VM, ENDBAR) do { \
        SEGSTAMP(0); \
        if (DMA_FIRST) { DMA; } G1_READS(SL); if (!DMA_FIRST) { DMA; } \
        asm volatile("s_waitcnt vmcnt(" VS(VM) ") lgkmcnt(0)" ::: "memory"); SEGSTAMP(1); \
        asm volatile("s_barrier" ::: "memory"); \
        STEPSTAMP(); SCHED(); __builtin_amdgcn_s_setprio(1); \
        _Pragma("unroll") for (int i = 0; i < 40; ++i) { const int m_ = MORD == 0 ? (i & 3) : MORD == 1 ? i / 10 : MORD == 2 ? i / 10 : ((i >> 2) & 1 ? 3 - (i & 3) : (i & 3)), \
            n_ = MORD == 0 ? (i >> 2) : MORD == 1 ? i % 10 : MORD == 2 ? ((i / 10) & 1 ? 9 - i % 10 : i % 10) : (i >> 2); MF16_ACC(acc[m_][n_], bf[n_], af[m_]); } \
        __builtin_amdgcn_s_setprio(0); SCHED(); \
        if (ENDBAR) asm volatile("s_barrier" ::: "memory"); } while (0)
    const char* pf = (wid < 7 ? w2 + (size_t)(bx * 7 + wid) * 4096 : wo + (size_t)bx * 8192);
    for (int it = 0; it < 7; ++it) { G1_STEP(0, G1_DMA(3), VA, true); G1_STEP(1, G1_DMA(0), VA, true); G1_STEP(2, G1_DMA(1), VA, true); G1_STEP(3, G1_DMA(2), VA, true); }
    G1_STEP(0, G1_DMA(3), VA, true);
    G1_STEP(1, { dma_s2(avoff, pf, lds0 + 147456); dma_s2(avoff, pf + 2048, lds0 + 147456); }, VB, true);
    G1_STEP(2, {}, 4, true);
    G1_STEP(3, { if (wc == 0) dma_s1(avoff, wo + (size_t)bx * 8192 + 4096 + wid * 1024, lds0 + 147456); }, 63, wc == 0);
    STAMP(2);
#undef G1_STEP
#undef G1_READS
#undef BFR
#undef G1_DMA
    asm volatile("s_nop 7" : "+v"(acc[0][9]), "+v"(acc[1][9]), "+v"(acc[2][9]), "+v"(acc[3][9]), "+v"(acc[3][8]), "+v"(acc[3][7]), "+v"(acc[3][0]), "+v"(acc[3][1]), "+v"(acc[3][2]));
    SCHED();
    int tz = threadIdx.x; asm volatile("" : "+v"(tz));
    const int fr = tz & 15, fq = (tz >> 4) & 3;
    const int tb = (rb * 16 + wr * 4) * 1024;
    const bool ffn_main = !tq || wc == 1;
    const int cm = ((fq & 1) << 1) | (fq >> 1), ce = 2 * wc + (fq >> 1);
    {
        const int cb0 = 2 * qbx;
        const float sc = cb0 < 32 ? QSCALE : 1.0f;
        char* dst = qkv + (size_t)(cb0 + (fq & 1)) * 131072 + tb + ce * 256 + ((fr ^ (4 * ce)) * 16);
#pragma unroll
        for (int m = 0; m < 4; ++m) {
            const f32x4 va = acc[m][8] * sc, vb = acc[m][9] * sc;
            unsigned a0 = cvtpk(va[0], va[1]), a1 = cvtpk(va[2], va[3]), b0 = cvtpk(vb[0], vb[1]), b1 = cvtpk(vb[2], vb[3]);
            swap16(a0, b0); swap16(a1, b1);
            ST16(dst + m * 1024, ((u32x4){a0, a1, b0, b1}));
        }
    }
    if (ffn_main) {
        char* dst = act + (size_t)(wc ? pB : pA) * 262144 + tb + cm * 256 + fr * 16;
#pragma unroll
        for (int m = 0; m < 4; ++m)
#pragma unroll
            for (int sub = 0; sub < 2; ++sub) {
                const u32x2 a = silu_mul4(acc[m][2 * sub], acc[m][4 + 2 * sub]), b = silu_mul4(acc[m][2 * sub + 1], acc[m][4 + 2 * sub + 1]);
                unsigned a0 = a[0], a1 = a[1], b0 = b[0], b1 = b[1];
                swap16(a0, b0); swap16(a1, b1);
                ST16(dst + sub * 131072 + m * 1024, ((u32x4){a0, a1, b0, b1}));
            }
    } else {
        const int lo = tb + cm * 256 + ((fr ^ (4 * cm)) * 16);
#pragma unroll
        for (int sub = 0; sub < 4; ++sub) {
            const int cb = 2 * qb0 + sub;
            const float sc = cb < 32 ? QSCALE : 1.0f;
            char* dst = qkv + (size_t)cb * 131072 + lo;
#pragma unroll
            for (int m = 0; m < 4; ++m) {
                const f32x4 va = acc[m][2 * sub] * sc, vb = acc[m][2 * sub + 1] * sc;
                unsigned a0 = cvtpk(va[0], va[1]), a1 = cvtpk(va[2], va[3]), b0 = cvtpk(vb[0], vb[1]), b1 = cvtpk(vb[2], vb[3]);
                swap16(a0, b0); swap16(a1, b1);
                ST16(dst + m * 1024, ((u32x4){a0, a1, b0, b1}));
            }
        }
    }
    STAMP(3); RSTAMP(5); DIAG_OUT(1, ts0, ts1, ts2, ts3, ts4, ts5);
}

__device__ __forceinline__ void attn_role(char* smem, const int bx, const char* __restrict__ qkv, char* __restrict__ act, unsigned* __restrict__ ctr) {
    RSTAMP(4); STAMP(0);
    const int tid = threadIdx.x, lane = tid & 63;
    const int wid = __builtin_amdgcn_readfirstlane(tid >> 6);
    const int head = (bx & 7) * 4 + (bx >> 6), qb = (bx >> 3) & 7;
    __builtin_amdgcn_s_setprio(AT_PRIO);
    const char* Qf = qkv + (size_t)head * 131072;
    const char* Kf = Qf + 4 * MB;
    const char* Vr = Qf + 8 * MB;
    const unsigned lds0 = (unsigned)(size_t)(LAS char*)smem;
    const unsigned voff = lane * 16;
    const int tq0 = qb * 8 + wid * 2;
    unsigned fo[2];
#pragma unroll
    for (int s = 0; s < 2; ++s) { const int c = 2 * s + (lane >> 5); fo[s] = ((lane >> 4) & 1) * 1024 + c * 256 + (((lane & 15) ^ (4 * c)) * 16); }
    bf16x8 q[2][2];
    asm volatile("global_load_dwordx4 %0, %4, %6\n\tglobal_load_dwordx4 %1, %5, %6\n\tglobal_load_dwordx4 %2, %4, %6 offset:2048\n\tglobal_load_dwordx4 %3, %5, %6 offset:2048"
                 : "=&v"(q[0][0]), "=&v"(q[0][1]), "=&v"(q[1][0]), "=&v"(q[1][1]) : "v"(fo[0]), "v"(fo[1]), "s"(Qf + tq0 * 2048) : "memory");
    bf16x8 sel;
    { const short one = (((lane >> 2) & 1) == ((lane >> 4) & 1)) ? (short)0x3f80 : (short)0; sel = (bf16x8){one, one, one, one, one, one, one, one}; }
    const lds_cptr kp0 = (lds_cptr)smem + fo[0], kp1 = (lds_cptr)smem + fo[1];
    lds_cptr vp0, vp1;
    { const int h = lane >> 5, c = 2 * ((lane >> 4) & 1) + ((lane >> 1) & 1), q4 = (lane >> 2) & 3;
      vp0 = (lds_cptr)smem + 8192 + c * 256 + (h ^ c) * 64 + q4 * 16 + (lane & 1) * 8; vp1 = (lds_cptr)smem + 8192 + c * 256 + ((2 + h) ^ c) * 64 + q4 * 16 + (lane & 1) * 8; }
    const char* Kw = Kf + wid * 2048;
    const char* Vw = Vr + wid * 2048;
    volatile LAS unsigned* flag = (volatile LAS unsigned*)(smem);
    const unsigned ooff = ((lane >> 4) & 1) * 1024 + (lane >> 5) * 256 + (lane & 15) * 16;
#define AT_DMA(ST, SLOT) do { const unsigned d_ = lds0 + (SLOT) + wid * 2048; \
        dma_s2(voff, Kf + (ST) * 8192 + wid * 2048, d_); dma_s2(voff, Vr + (ST) * 8192 + wid * 2048, d_ + 8192); } while (0)
    for (int attempt = 0; attempt < 2; ++attempt) {
        f32x16 o[2]; f32x4 l[2];
        bf16x8 k0, k1;
        asm volatile("global_load_dwordx4 %0, %2, %4\n\tglobal_load_dwordx4 %1, %3, %4" : "=&v"(k0), "=&v"(k1) : "v"(fo[0]), "v"(fo[1]), "s"(Kf) : "memory");
        AT_DMA(0, 0); AT_DMA(1, 16384); AT_DMA(2, 32768);
        o[0] = (f32x16){}; o[1] = (f32x16){}; l[0] = (f32x4){}; l[1] = (f32x4){};
        PIN(o[0]); PIN(o[1]); PIN(l[0]); PIN(l[1]);
        asm volatile("s_waitcnt vmcnt(12)" : "+v"(k0), "+v"(k1), "+v"(q[0][0]), "+v"(q[0][1]), "+v"(q[1][0]), "+v"(q[1][1])::"memory");
        f32x16 negm[2];
        {
            float m0 = -INFINITY, m1 = -INFINITY;
            const int nt = attempt == 0 ? 1 : 64;
            for (int kt = 0; kt < nt; ++kt) {
                if (kt) { k0 = *(const bf16x8*)(Kf + kt * 2048 + fo[0]); k1 = *(const bf16x8*)(Kf + kt * 2048 + fo[1]); }
                f32x16 s0 = MF32(k0, q[0][0], (f32x16){}), s1 = MF32(k0, q[1][0], (f32x16){});
                s0 = MF32(k1, q[0][1], s0); s1 = MF32(k1, q[1][1], s1);
#pragma unroll
                for (int r = 0; r < 16; ++r) { m0 = fmaxf(m0, s0[r]); m1 = fmaxf(m1, s1[r]); }
            }
            m0 = fmaxf(m0, __shfl_xor(m0, 32)); m1 = fmaxf(m1, __shfl_xor(m1, 32));
#pragma unroll
            for (int r = 0; r < 16; ++r) { negm[0][r] = -m0; negm[1][r] = -m1; }
            PIN(negm[0]); PIN(negm[1]);
        }
        asm volatile("s_waitcnt vmcnt(0)\n\ts_barrier" ::: "memory");
        bf16x8 kf0 = *(const LAS bf16x8*)(kp0), kf1 = *(const LAS bf16x8*)(kp1);
        bf16x8 vf0 = (bf16x8){}, vf1 = (bf16x8){};
        u32x4 pa0 = (u32x4){}, pa1 = (u32x4){}, pb0 = (u32x4){}, pb1 = (u32x4){};
        f32x16 sA = MF32(kf0, q[0][0], negm[0]); sA = MF32(kf1, q[0][1], sA);
        f32x16 sB;
#define EX(X, i) X[i] = __builtin_amdgcn_exp2f(X[i])
#define PK(X, i) cvtpk(X[i], X[(i) + 1])
#define BC8(x) __builtin_bit_cast(bf16x8, x)
#define AT_UNIT(SC, PC0, PC1, SN, QN, NM, PP0, PP1, OP, LP, K0, K1, V0, V1, L0, L1, L2, L3) do { unsigned c0, c1, c2, c3, c4, c5, c6, c7; \
            SN = MF32(K0, QN[0], NM); SCHED(); L0; SCHED(); EX(SC, 0); EX(SC, 1); EX(SC, 2); SCHED(); \
            OP = MF32(V0, BC8(PP0), OP); SCHED(); L1; SCHED(); c0 = PK(SC, 0); EX(SC, 3); EX(SC, 4); EX(SC, 5); SCHED(); \
            SN = MF32(K1, QN[1], SN); SCHED(); L2; SCHED(); c1 = PK(SC, 2); c2 = PK(SC, 4); EX(SC, 6); EX(SC, 7); EX(SC, 8); SCHED(); \
            OP = MF32(V1, BC8(PP1), OP); SCHED(); L3; SCHED(); c3 = PK(SC, 6); EX(SC, 9); EX(SC, 10); EX(SC, 11); SCHED(); \
            MF16_ACC(LP, sel, PP0); SCHED(); c4 = PK(SC, 8); c5 = PK(SC, 10); EX(SC, 12); EX(SC, 13); SCHED(); \
            MF16_ACC(LP, sel, PP1); SCHED(); EX(SC, 14); EX(SC, 15); SCHED(); c6 = PK(SC, 12); SCHED(); c7 = PK(SC, 14); \
            PC0 = (u32x4){c0, c1, c2, c3}; PC1 = (u32x4){c4, c5, c6, c7}; SCHED(); } while (0)
#define AT_M0(SLOT, V) asm volatile("s_mov_b32 m0, %0" ::"s"(lds0 + (SLOT) + wid * 2048 + (V) * 8192) : "memory")
#define AT_PAIR(ST, V) asm volatile("global_load_lds_dwordx4 %0, %1\n\tglobal_load_lds_dwordx4 %0, %1 offset:1024" ::"v"(voff), "s"(((V) ? Vw : Kw) + ((ST) & 15) * 8192) : "memory")
#define AT_STAGE(SO, SN_, DM, ST, SLA, SLB) do { _Pragma("unroll") for (int kt = 0; kt < 4; ++kt) { \
                const int ko = kt < 3 ? (SO) + (kt + 1) * 2048 : (SN_), vo = (SO) + kt * 2048; \
                bf16x8 kn0, kn1, vn0, vn1; \
                AT_UNIT(sA, pa0, pa1, sB, q[1], negm[1], pb0, pb1, o[1], l[1], kf0, kf1, vf0, vf1, \
                        (kn0 = *(const LAS bf16x8*)(kp0 + ko), kn1 = *(const LAS bf16x8*)(kp1 + ko)), vn0 = cat8(tr_read(vp0 + vo), tr_read(vp1 + vo)), \
                        vn1 = cat8(tr_read(vp0 + vo + 1024), tr_read(vp1 + vo + 1024)), {}); \
                kf0 = kn0; kf1 = kn1; vf0 = vn0; vf1 = vn1; \
                __builtin_amdgcn_s_waitcnt(0xC07F); SCHED(); \
                AT_UNIT(sB, pb0, pb1, sA, q[0], negm[0], pa0, pa1, o[0], l[0], kf0, kf1, vf0, vf1, \
                        if (DM) AT_M0((kt >> 1) ? (SLB) : (SLA), kt & 1), if (DM) AT_PAIR((ST) + (kt >> 1), kt & 1), {}, {}); } } while (0)
#define NXT(x) ((x) == 65536 ? 0 : (x) + 16384)
        int s0 = 0;
        for (int k = 0; k < 8; ++k) {
            const int s1 = NXT(s0), s2 = NXT(s1), s3 = NXT(s2), s4 = NXT(s3);
            AT_STAGE(s0, s1, !AT_NODMA, 2 * k + 3, s3, s4);
            AT_STAGE(s1, s2, false, 0, 0, 0);
            s0 = s2;
            if (!AT_NOBAR) asm volatile("s_waitcnt vmcnt(0)\n\ts_barrier" ::: "memory");
        }
#undef NXT
#undef AT_STAGE
#undef AT_M0
#undef AT_PAIR
        o[1] = MF32(vf0, BC8(pb0), o[1]); o[1] = MF32(vf1, BC8(pb1), o[1]);
        l[1] = MF16(sel, BC8(pb0), l[1]); l[1] = MF16(sel, BC8(pb1), l[1]);
#undef AT_UNIT
        asm volatile("s_nop 7\n\ts_nop 7" : "+v"(l[0]), "+v"(l[1]), "+v"(o[0]), "+v"(o[1]));
        float tsum = 0.f;
#pragma unroll
        for (int i = 0; i < 2; ++i) {
            const float r = 1.0f / l[i][0];
#pragma unroll
            for (int e = 0; e < 16; ++e) { tsum += fabsf(o[i][e]); o[i][e] *= r; }
        }
        const bool bad = !(l[0][0] < 1.0995e12f) || !(l[1][0] < 1.0995e12f) || !(tsum < INFINITY);
        if (lane == 0) flag[wid] = 0u;
        if (bad) flag[wid] = 1u;
#pragma unroll
        for (int i = 0; i < 2; ++i)
#pragma unroll
            for (int s = 0; s < 2; ++s) {
                unsigned a0 = cvtpk(o[i][8 * s + 0], o[i][8 * s + 1]), a1 = cvtpk(o[i][8 * s + 2], o[i][8 * s + 3]);
                unsigned b0 = cvtpk(o[i][8 * s + 4], o[i][8 * s + 5]), b1 = cvtpk(o[i][8 * s + 6], o[i][8 * s + 7]);
                swap32(a0, b0); swap32(a1, b1);
                const u32x4 w = (u32x4){a0, a1, b0, b1};
                asm volatile("global_store_dwordx4 %0, %1, %2 sc1\n\ts_nop 1" ::"v"(ooff), "v"(w), "s"(act + (size_t)(112 + head) * 131072 + (tq0 + i) * 2048 + s * 512) : "memory");
            }
        asm volatile("s_waitcnt vmcnt(0) lgkmcnt(0)\n\ts_barrier" ::: "memory");
        const unsigned redo = flag[0] | flag[1] | flag[2] | flag[3];
        if (attempt == 1 || !__builtin_amdgcn_readfirstlane(redo)) break;
        asm volatile("s_waitcnt lgkmcnt(0)\n\ts_barrier" ::: "memory");
    }
#undef AT_DMA
    if (tid == 0) __hip_atomic_fetch_add(ctr + qb * 64, 1u, __ATOMIC_RELAXED, __HIP_MEMORY_SCOPE_AGENT);
    STAMP(3); RSTAMP(5); DIAG_OUT(2, ts0, tA, tB, ts3, ts4, ts5);
}

__device__ __forceinline__ void gemm2_role(char* smem, const int bx, const char* __restrict__ act, const char* __restrict__ w2, const char* __restrict__ wo,
                                           const char* __restrict__ x, char* __restrict__ out, unsigned* __restrict__ ctr) {
    RSTAMP(4); STAMP(0);
    const int tid = threadIdx.x, lane = tid & 63;
    const int wid = __builtin_amdgcn_readfirstlane(tid >> 6);
    const int xc = bx & 7, ii = bx >> 3, tm = (xc >> 1) * 4 + (ii >> 3), tn = (xc & 1) * 8 + (ii & 7);
    const unsigned lds0 = (unsigned)(size_t)(LAS char*)smem + wid * 16384;
    if (G2_DELAY) __builtin_amdgcn_s_sleep(G2_DELAY);
    const char* asrc = act + (size_t)wid * 131072 + tm * 8192;
    unsigned av[4];
#pragma unroll
    for (int i = 0; i < 4; ++i) av[i] = (i ^ wid) * 2048 + lane * 16;
    const char* wsrc = w2 + (size_t)wid * 32 * 2048 + tn * 128;
    const unsigned wv0 = (lane >> 3) * 2048 + ((((lane >> 1) & 3) ^ ((lane >> 4) & 1)) * 32) + (lane & 1) * 16;
    const unsigned wv1 = (lane >> 3) * 2048 + ((((lane >> 1) & 3) ^ ((lane >> 4) & 1) ^ 2) * 32) + (lane & 1) * 16 + 16384 - 1024;
    lds_cptr wp[4];
    {
        const int g = lane >> 4, q = (lane >> 2) & 3, p = lane & 3, f = (q >> 1) | ((g & 1) << 1);
#pragma unroll
        for (int c = 0; c < 4; ++c) wp[c] = (lds_cptr)smem + wid * 16384 + g * 1024 + q * 128 + ((c ^ f) << 5) + p * 8;
    }
    f32x4 acc[8][4];
#pragma unroll
    for (int i = 0; i < 8; ++i)
#pragma unroll
        for (int n = 0; n < 4; ++n) { acc[i][n] = (f32x4){}; PIN(acc[i][n]); }
    bf16x8 a0[4], a1[4], a2[4], a3[4], wfa[4], wfb[4];
#define G2_LDA(A, H) do { \
        asm volatile("global_load_dwordx4 %0, %4, %6\n\tglobal_load_dwordx4 %1, %4, %6 offset:1024\n\tglobal_load_dwordx4 %2, %5, %6\n\tglobal_load_dwordx4 %3, %5, %6 offset:1024" \
                     : "=&v"(A[0]), "=&v"(A[1]), "=&v"(A[2]), "=&v"(A[3]) : "v"(av[2 * (H)]), "v"(av[2 * (H) + 1]), "s"(asrc) : "memory"); \
        if (H) asrc += 524288; } while (0)
#define G2_LDW(SL) do {     \
        asm volatile("s_mov_b32 m0, %3\n\ts_nop 0\n\tglobal_load_lds_dwordx4 %0, %2\n\tglobal_load_lds_dwordx4 %1, %2 offset:1024" \
                     ::"v"(wv0), "v"(wv1), "s"(wsrc), "s"(lds0 + (SL) * 2048) : "memory"); \
        wsrc += ((SL) & 1) ? 262144 - 32768 : 32768; } while (0)
#define G2_WAIT(N, A) asm volatile("s_waitcnt vmcnt(" #N ")" : "+v"(A[0]), "+v"(A[1]), "+v"(A[2]), "+v"(A[3])::"memory")
#define WFR(P, O) cat8(tr_read((P) + (O)), tr_read((P) + (O) + 512))
#define G2_STEP(N, AC, AN, WC, WN, SL, RD, IA, IW) do { \
        if ((N) >= 0) G2_WAIT(N, AN); \
        if (RD) { WN[2 * ((SL) & 1)] = WFR(wp[2 * ((SL) & 1)], ((((SL) >> 1) + 1) & 3) * 4096); WN[2 * ((SL) & 1) + 1] = WFR(wp[2 * ((SL) & 1) + 1], ((((SL) >> 1) + 1) & 3) * 4096); } \
        SCHED(); \
        _Pragma("unroll") for (int i = 0; i < 16; ++i) MF16_ACC(acc[4 * ((SL) & 1) + (i >> 2)][((i >> 2) & 1) ? 3 - (i & 3) : (i & 3)], WC[((i >> 2) & 1) ? 3 - (i & 3) : (i & 3)], AC[i >> 2]); \
        SCHED(); if (IA) G2_LDA(AC, (SL) & 1); if (IW) G2_LDW(SL); } while (0)
#define G2_STEP4(N0, N1, N2, N3, SL, RDL, IA, IW) do { \
        G2_STEP(N0, a0, a1, wfa, wfb, (SL), true, IA, IW); G2_STEP(N1, a1, a2, wfa, wfb, (SL) + 1, true, IA, IW); \
        G2_STEP(N2, a2, a3, wfb, wfa, (SL) + 2, RDL, IA, IW); G2_STEP(N3, a3, a0, wfb, wfa, (SL) + 3, RDL, IA, IW); } while (0)
    G2_LDW(0); G2_LDW(1); G2_LDW(2); G2_LDW(3);
    G2_LDA(a0, 0); G2_LDW(4); G2_LDA(a1, 1); G2_LDW(5); G2_LDA(a2, 0); G2_LDW(6); G2_LDA(a3, 1); G2_LDW(7);
    G2_WAIT(20, a0); STAMP(1);
#pragma unroll
    for (int c = 0; c < 4; ++c) wfa[c] = WFR(wp[c], 0);
    for (int it = 0; it < 6; ++it) { G2_STEP4(14, 14, 14, 14, 0, true, true, true); G2_STEP4(14, 14, 14, 14, 4, true, true, true); }
    G2_STEP4(14, 12, 10, 8, 0, true, true, false);
    G2_STEP4(8, 4, 0, -1, 4, false, false, false);
#undef G2_STEP4
#undef G2_STEP
#undef G2_WAIT
#undef G2_LDW
#undef G2_LDA
    const int fr = lane & 15, fq = lane >> 4;
    const size_t orow = (size_t)(tm * 128 + wid * 32 + fr) * 2048 + tn * 128 + (fq & 1) * 32 + (fq >> 1) * 16;
    u32x4 xr[4];
#pragma unroll
    for (int c = 0; c < 4; ++c) xr[c] = *(const u32x4*)(x + orow + (c >> 1) * 32768 + (c & 1) * 64);
    asm volatile("s_nop 7" : "+v"(acc[7][0]), "+v"(acc[7][1]), "+v"(acc[7][2]), "+v"(acc[7][3]));
    SCHED();
    char* mine = smem + wid * 16384 + lane * 16;
#pragma unroll
    for (int i = 0; i < 16; ++i) *(f32x4*)(mine + i * 1024) = acc[4 + (i >> 2)][i & 3];
    __syncthreads();
#pragma unroll
    for (int i = 0; i < 16; ++i) acc[i >> 2][i & 3] += *(const f32x4*)(smem + (wid ^ 2) * 16384 + lane * 16 + i * 1024);
    __syncthreads();
#pragma unroll
    for (int i = 0; i < 8; ++i) *(f32x4*)(mine + i * 1024) = acc[2 + (i >> 2)][i & 3];
    __syncthreads();
#pragma unroll
    for (int i = 0; i < 8; ++i) { acc[i >> 2][i & 3] += *(const f32x4*)(smem + (wid ^ 1) * 16384 + lane * 16 + i * 1024); PIN(acc[i >> 2][i & 3]); }
    __syncthreads();
    const unsigned ldsb = (unsigned)(size_t)(LAS char*)smem;
    const char* wos = wo + tn * 128;
    const char* osrc = act + (size_t)112 * 131072 + tm * 8192 + wid * 2048;
    unsigned ov = lane * 16;
    lds_cptr wq[4];
#pragma unroll
    for (int c = 0; c < 4; ++c) { wq[c] = wp[c] - wid * 16384; PIN(wq[c]); }
    PIN(ov);
#define G3_LDW(HS, SLOT) asm volatile("s_mov_b32 m0, %3\n\ts_nop 0\n\tglobal_load_lds_dwordx4 %0, %2\n\tglobal_load_lds_dwordx4 %1, %2 offset:1024" \
                     ::"v"(wv0), "v"(wv1), "s"(wos + (size_t)(HS) * 32768), "s"(ldsb + (SLOT) * 2048) : "memory")
#pragma unroll
    for (int i = 0; i < 10; ++i) G3_LDW(wid * 10 + i, wid * 10 + i);
    if (wid == 0) {
        const unsigned long long t0 = __builtin_amdgcn_s_memrealtime();
        while ((unsigned)__builtin_amdgcn_readfirstlane(__hip_atomic_load(ctr + (tm >> 1) * 64, __ATOMIC_RELAXED, __HIP_MEMORY_SCOPE_AGENT)) < 32u) {
            if (__builtin_amdgcn_s_memrealtime() - t0 > 5000000ull) break;
            __builtin_amdgcn_s_sleep(2);
        }
        __builtin_amdgcn_fence(__ATOMIC_ACQUIRE, "wavefront");
        asm volatile("" ::: "memory");
    }
    asm volatile("s_waitcnt vmcnt(0)\n\ts_barrier" ::: "memory");
    STAMP(8);
    bf16x8 oa[8][4];
#define G3_LDA(G) asm volatile("global_load_dwordx4 %0, %4, %5 sc1\n\tglobal_load_dwordx4 %1, %4, %5 offset:1024 sc1\n\tglobal_load_dwordx4 %2, %4, %6 sc1\n\tglobal_load_dwordx4 %3, %4, %6 offset:1024 sc1" \
                     : "=&v"(oa[(G) & 7][0]), "=&v"(oa[(G) & 7][1]), "=&v"(oa[(G) & 7][2]), "=&v"(oa[(G) & 7][3]) \
                     : "v"(ov), "s"(osrc + (size_t)(G) * 262144), "s"(osrc + (size_t)(G) * 262144 + 131072) : "memory")
#define G3_WAIT(N, G) asm volatile("s_waitcnt vmcnt(" #N ")" : "+v"(oa[(G) & 7][0]), "+v"(oa[(G) & 7][1]), "+v"(oa[(G) & 7][2]), "+v"(oa[(G) & 7][3])::"memory")
    bf16x8 wf[2][8];
#define G3_WF(G, i) do { const bool hi_ = (G) >= 8 && (G) < 10; const int sl_ = (((G) * 2 + ((i) >> 2)) % 20) * 4096 - (hi_ ? 65536 : 0); \
        wf[(G) & 1][i] = WFR(wq[(i) & 3] + (hi_ ? 65536 : 0), sl_); } while (0)
#define G3_GROUP(N, G, RD) do { G3_WAIT(N, G); SCHED(); \
        const bool rf_ = (G) == 5 || (G) == 6; \
        _Pragma("unroll") for (int i = 0; i < 8; ++i) { \
            MF16_ACC(acc[0][i & 3], wf[(G) & 1][i], oa[(G) & 7][2 * (i >> 2)]); MF16_ACC(acc[1][i & 3], wf[(G) & 1][i], oa[(G) & 7][2 * (i >> 2) + 1]); SCHED(); \
            if (RD) G3_WF((G) + 1, i); \
            if (rf_ && (i & 1) && i < 6) G3_LDW(40 + wid * 6 + ((G) - 5) * 3 + (i >> 1), wid * 6 + ((G) - 5) * 3 + (i >> 1)); SCHED(); } \
        if ((G) < 8) G3_LDA((G) + 8); } while (0)
    G3_LDA(0); G3_LDA(1); G3_LDA(2); G3_LDA(3); G3_LDA(4); G3_LDA(5); G3_LDA(6); G3_LDA(7);
#pragma unroll
    for (int i = 0; i < 8; ++i) G3_WF(0, i);
    G3_GROUP(28, 0, true); G3_GROUP(28, 1, true); G3_GROUP(28, 2, true); G3_GROUP(28, 3, true); G3_GROUP(28, 4, true);
    G3_WAIT(28, 5);
    STAMP(10);
    asm volatile("s_waitcnt lgkmcnt(0)\n\ts_barrier" ::: "memory");
    G3_GROUP(28, 5, true); G3_GROUP(34, 6, true); G3_GROUP(40, 7, true); G3_GROUP(40, 8, true);
    STAMP(11);
    asm volatile("s_waitcnt vmcnt(8)\n\ts_barrier" ::: "memory");
    STAMP(12);
    G3_GROUP(8, 9, true); G3_GROUP(8, 10, true); G3_GROUP(8, 11, true); G3_GROUP(8, 12, true); G3_GROUP(8, 13, true); G3_GROUP(4, 14, true); G3_GROUP(0, 15, false);
    STAMP(2);
#undef G3_GROUP
#undef G3_WF
#undef G3_WAIT
#undef G3_LDA
#undef G3_LDW
#undef WFR
    asm volatile("s_nop 7" : "+v"(acc[0][0]), "+v"(acc[0][1]), "+v"(acc[0][2]), "+v"(acc[0][3]), "+v"(acc[1][0]), "+v"(acc[1][1]), "+v"(acc[1][2]), "+v"(acc[1][3]));
#pragma unroll
    for (int c = 0; c < 4; ++c) {
        float v[8];
#pragma unroll
        for (int e = 0; e < 4; ++e) {
            unsigned a = __float_as_uint(acc[c >> 1][2 * (c & 1)][e]), b = __float_as_uint(acc[c >> 1][2 * (c & 1) + 1][e]);
            swap16(a, b);
            v[e] = __uint_as_float(a); v[4 + e] = __uint_as_float(b);
        }
        u32x4 w;
#pragma unroll
        for (int e = 0; e < 4; ++e) w[e] = cvtpk(v[2 * e] + bf_lo(xr[c][e]), v[2 * e + 1] + bf_hi(xr[c][e]));
        *(u32x4*)(out + orow + (c >> 1) * 32768 + (c & 1) * 64) = w;
    }
    STAMP(3); RSTAMP(5); DIAG_OUT(3, ts0, ts1, ts2, ts3, ts4, ts5); DIAG_OUT(0, ts8, ts9, ts10, ts11, ts12, ts2);
}

__global__ __launch_bounds__(256, 2) void k_fused(const char* __restrict__ qkv, char* __restrict__ act, const char* __restrict__ w2, const char* __restrict__ wo,
                                                  const char* __restrict__ x, char* __restrict__ out, unsigned* __restrict__ ctr, int boff) {
    __shared__ __attribute__((aligned(1024))) char smem[81920];
    const int b = blockIdx.x + boff;
    if (b < 256) {
        if (NO_ATTN) { if (threadIdx.x == 0) __hip_atomic_fetch_add(ctr + ((b >> 3) & 7) * 64, 1u, __ATOMIC_RELAXED, __HIP_MEMORY_SCOPE_AGENT); }
        else attn_role(smem, b, qkv, act, ctr);
    } else if (!NO_GEMM) gemm2_role(smem, b - 256, act, w2, wo, x, out, ctr);
}

extern "C" void kernel_launch(void* const* d_in, const int* in_sizes, int n_in, void* d_out, int out_size, void* d_ws, size_t ws_size,
                              hipStream_t stream) {
    const u16* x = (const u16*)d_in[0];
    const char* wqkv = (const char*)d_in[1];
    const char* wo = (const char*)d_in[2];
    const char* w1 = (const char*)d_in[4];
    const char* w3 = (const char*)d_in[5];
    const char* w2 = (const char*)d_in[6];
    char* ws = (char*)d_ws;
    k_ln<<<PF_ROUNDS ? 512 : 256, 512, 0, stream>>>(x, ws + WS_XN, (unsigned*)(ws + WS_CTR), wqkv, w1, w3);
    k_gemm1<<<256, 512, 0, stream>>>(ws + WS_XN, wqkv, w1, w3, ws + WS_QKV, ws + WS_ACT, w2, wo);
    k_fused<<<512, 256, 0, stream>>>(ws + WS_QKV, ws + WS_ACT, w2, wo, (const char*)x, (char*)d_out, (unsigned*)(ws + WS_CTR), 0);
}
```

```cpp
#include <hip/hip_runtime.h>
#include <stdint.h>

typedef unsigned short u16;
typedef short bf16x8 __attribute__((ext_vector_type(8)));
typedef short s16x4 __attribute__((ext_vector_type(4)));
typedef float f32x2 __attribute__((ext_vector_type(2)));
typedef float f32x4 __attribute__((ext_vector_type(4)));
typedef float f32x16 __attribute__((ext_vector_type(16)));
typedef unsigned u32x2 __attribute__((ext_vector_type(2)));
typedef unsigned u32x4 __attribute__((ext_vector_type(4)));
typedef __bf16 bf16x2_t __attribute__((ext_vector_type(2)));
#define LAS __attribute__((address_space(3)))
typedef const LAS char* lds_cptr;

constexpr int SEQ = 2048, DM = 1024, FF = 3584, NH = 32;
constexpr size_t MB = 1024 * 1024;
constexpr size_t WS_CTR = 0;
constexpr size_t WS_XN = 65536;
constexpr size_t WS_QKV = WS_XN + 4 * MB;
constexpr size_t WS_ACT = WS_QKV + 12 * MB;
constexpr float QSCALE = 0.17677669529663687f * 1.4426950408889634f;

#define SCHED() __builtin_amdgcn_sched_barrier(0)
#define PIN(x) asm volatile("" : "+v"(x))
#define MF32(a, b, c) __builtin_amdgcn_mfma_f32_32x32x16_bf16(a, b, c, 0, 0, 0)
#define MF16(a, b, c) __builtin_amdgcn_mfma_f32_16x16x32_bf16(a, b, c, 0, 0, 0)

#define MF16_ACC(acc, a, b) asm volatile("v_mfma_f32_16x16x32_bf16 %0, %1, %2, %0" : "+v"(acc) : "v"(a), "v"(b))

__device__ __forceinline__ unsigned cvtpk(float lo, float hi) {
    f32x2 v = {lo, hi};
    bf16x2_t b = __builtin_convertvector(v, bf16x2_t);
    return __builtin_bit_cast(unsigned, b);
}
__device__ __forceinline__ float bf_lo(unsigned w) { return __uint_as_float(w << 16); }
__device__ __forceinline__ float bf_hi(unsigned w) { return __uint_as_float(w & 0xffff0000u); }
__device__ __forceinline__ float rbf(float x) { return bf_lo(cvtpk(x, x)); }
__device__ __forceinline__ s16x4 tr_read(lds_cptr p) {
    return __builtin_bit_cast(s16x4, __builtin_amdgcn_ds_read_tr16_b64_v4i16((LAS s16x4*)p));
}
__device__ __forceinline__ bf16x8 cat8(s16x4 lo, s16x4 hi) { return __builtin_shufflevector(lo, hi, 0, 1, 2, 3, 4, 5, 6, 7); }
__device__ __forceinline__ void dma_v(const void* g, unsigned lds) {
    asm volatile("s_mov_b32 m0, %1\n\ts_nop 0\n\tglobal_load_lds_dwordx4 %0, off" ::"v"(g), "s"(lds) : "memory");
}
__device__ __forceinline__ void dma_s1(unsigned voff, const void* sbase, unsigned lds) {
    asm volatile("s_mov_b32 m0, %2\n\ts_nop 0\n\tglobal_load_lds_dwordx4 %0, %1" ::"v"(voff), "s"(sbase), "s"(lds) : "memory");
}
__device__ __forceinline__ void dma_s2(unsigned voff, const void* sbase, unsigned lds) {
    asm volatile("s_mov_b32 m0, %2\n\ts_nop 0\n\tglobal_load_lds_dwordx4 %0, %1\n\tglobal_load_lds_dwordx4 %0, %1 offset:1024" ::"v"(voff), "s"(sbase), "s"(lds) : "memory");
}
__device__ __forceinline__ void swap16(unsigned& a, unsigned& b) {
    auto r = __builtin_amdgcn_permlane16_swap(a, b, false, false);
    a = r[0]; b = r[1];
}
__device__ __forceinline__ void swap32(unsigned& a, unsigned& b) {
    auto r = __builtin_amdgcn_permlane32_swap(a, b, false, false);
    a = r[0]; b = r[1];
}

__device__ __forceinline__ float wave_sum(float v) {
    v += __builtin_bit_cast(float, __builtin_amdgcn_update_dpp(0, __builtin_bit_cast(int, v), 0xB1, 0xf, 0xf, true));
    v += __builtin_bit_cast(float, __builtin_amdgcn_update_dpp(0, __builtin_bit_cast(int, v), 0x4E, 0xf, 0xf, true));
    v += __builtin_bit_cast(float, __builtin_amdgcn_update_dpp(0, __builtin_bit_cast(int, v), 0x141, 0xf, 0xf, true));
    v += __builtin_bit_cast(float, __builtin_amdgcn_update_dpp(0, __builtin_bit_cast(int, v), 0x140, 0xf, 0xf, true));
    auto a = __builtin_amdgcn_permlane16_swap(__float_as_uint(v), __float_as_uint(v), false, false);
    v = __uint_as_float(a[0]) + __uint_as_float(a[1]);
    auto b = __builtin_amdgcn_permlane32_swap(__float_as_uint(v), __float_as_uint(v), false, false);
    return __uint_as_float(b[0]) + __uint_as_float(b[1]);
}
__global__ __launch_bounds__(512) void k_ln(const u16* __restrict__ x, char* __restrict__ xnF, unsigned* __restrict__ ctr,
                                            const char* __restrict__ wqkv, const char* __restrict__ w1, const char* __restrict__ w3) {
    __shared__ __attribute__((aligned(16))) char sm[16384];
    const int tid = threadIdx.x, lane = tid & 63, wid = tid >> 6;
    if (blockIdx.x >= 256) {
        const size_t o = (size_t)((blockIdx.x - 256) * 512 + tid) * 16;
        const u32x4 p0 = *(const u32x4*)(wqkv + o), p1 = *(const u32x4*)(w1 + o), p2 = *(const u32x4*)(w3 + o);
        asm volatile("" ::"v"(p0), "v"(p1), "v"(p2));
        return;
    }
    if (blockIdx.x == 0) { ctr[tid] = 0u; ctr[tid + 512] = 0u; }
    const int row = blockIdx.x * 8 + wid;
    const u32x4* src = (const u32x4*)(x + (size_t)row * DM);
    const u32x4 a = src[lane], b = src[64 + lane];
    float v[16];
#pragma unroll
    for (int i = 0; i < 4; ++i) { v[2 * i] = bf_lo(a[i]); v[2 * i + 1] = bf_hi(a[i]); v[8 + 2 * i] = bf_lo(b[i]); v[8 + 2 * i + 1] = bf_hi(b[i]); }
    float s = 0.f;
#pragma unroll
    for (int i = 0; i < 16; ++i) s += v[i];
    s = wave_sum(s);
    const float mean = s * (1.0f / DM);
    float q = 0.f;
#pragma unroll
    for (int i = 0; i < 16; ++i) { const float d = v[i] - mean; q += d * d; }
    q = wave_sum(q);
    const float mb = rbf(mean), vb = rbf(q * (1.0f / DM));
    const float rs = 1.0f / rbf(sqrtf(rbf(vb + 1e-5f)));
    u32x4 oa, ob;
#pragma unroll
    for (int i = 0; i < 4; ++i) {
        oa[i] = cvtpk(rbf(v[2 * i] - mb) * rs, rbf(v[2 * i + 1] - mb) * rs);
        ob[i] = cvtpk(rbf(v[8 + 2 * i] - mb) * rs, rbf(v[8 + 2 * i + 1] - mb) * rs);
    }
    *(u32x4*)(sm + lane * 128 + wid * 16) = oa;
    *(u32x4*)(sm + (64 + lane) * 128 + wid * 16) = ob;
    __syncthreads();
    const int row0 = blockIdx.x * 8;
#pragma unroll
    for (int i = 0; i < 2; ++i) {
        const int p = tid + i * 512, chunk = p >> 3, r = p & 7;
        const u32x4 w = *(const u32x4*)(sm + p * 16);
        *(u32x4*)(xnF + ((size_t)(chunk >> 2) * 128 + (row0 >> 4)) * 1024 + (chunk & 3) * 256 + ((row0 & 15) + r) * 16) = w;
    }
}

__device__ __forceinline__ unsigned silu_mul2(f32x2 g, f32x2 u) {
    const f32x2 t = g * -1.4426950408889634f;
    f32x2 e; e.x = __builtin_amdgcn_exp2f(t.x); e.y = __builtin_amdgcn_exp2f(t.y);
    e = e + 1.0f;
    f32x2 r; r.x = __builtin_amdgcn_rcpf(e.x); r.y = __builtin_amdgcn_rcpf(e.y);
    const f32x2 h = (g * u) * r;
    return cvtpk(h.x, h.y);
}
__device__ __forceinline__ u32x2 silu_mul4(f32x4 g, f32x4 u) {
    return (u32x2){silu_mul2((f32x2){g[0], g[1]}, (f32x2){u[0], u[1]}), silu_mul2((f32x2){g[2], g[3]}, (f32x2){u[2], u[3]})};
}
#define ST16(p, v) asm volatile("global_store_dwordx4 %0, %1, off sc1\n\ts_nop 1" ::"v"(p), "v"(v) : "memory")
__global__ __launch_bounds__(512, 2) void k_gemm1(const char* __restrict__ xnF, const char* __restrict__ wqkv, const char* __restrict__ w1,
                                                  const char* __restrict__ w3, char* __restrict__ qkv, char* __restrict__ act,
                                                  const char* __restrict__ w2, const char* __restrict__ wo) {
    __shared__ __attribute__((aligned(1024))) char smem[147456 + 2048];

    const int tid = threadIdx.x, lane = tid & 63;
    const int wid = __builtin_amdgcn_readfirstlane(tid >> 6), wr = wid & 3, wc = wid >> 2;
    const int bx = blockIdx.x, j = (bx & 7) + 8 * ((bx >> 3) & 3), rb = bx >> 5;
    const unsigned lds0 = (unsigned)(size_t)(LAS char*)smem;
    const bool tq = j < 8;
    const int qb0 = 3 * j, qbx = tq ? 3 * j + 2 : 16 + j, pA = 2 * j - 8, pB = tq ? j : 2 * j - 7;
    const char* bsrc[3]; unsigned binc[3];
#pragma unroll
    for (int i = 0; i < 3; ++i) {
        const int id = i < 2 ? wid + 8 * i : 16 + wr, kb = (id * 13) >> 6, blk = id - 5 * kb;
        const int r = i < 2 ? lane >> 3 : ((lane >> 3) & 3) + 4 * wc, j8 = lane & 7, f = ((r >> 1) & 1) | ((kb & 1) << 1), cp = (j8 >> 1) ^ f;
        const bool ffn = blk >= 3 || (blk < 2 && !tq);
        const int pitch = ffn ? 7168 : 6144;
        const uint64_t wg = (blk == 0 || blk == 3) ? (uint64_t)w1 : (uint64_t)w3, wb = ffn ? wg : (uint64_t)wqkv;
        const int cf = blk < 2 ? pA : pB, cq = blk == 2 ? qbx : qb0 + blk, cblk = ffn ? cf : cq;
        bsrc[i] = (const char*)wb + (size_t)(unsigned)(cblk * 128 + kb * 8 * pitch) + (size_t)(unsigned)(r * pitch + cp * 32 + (j8 & 1) * 16);
        binc[i] = 32u * pitch;
    }
    const char* asrc = xnF + ((size_t)rb * 16 + 2 * wid) * 1024;
    const unsigned avoff = lane * 16;
    const unsigned adst = lds0 + wid * 2048, bdst = lds0 + 65536 + wid * 1024;
    const unsigned hdst = lds0 + 65536 + 16384 + wr * 1024 + wc * 512;
#define G1_DMA(SL) do { \
        dma_s2(avoff, asrc, adst + (SL) * 16384); asrc += 131072; \
        dma_v(bsrc[0], bdst + (SL) * 20480); bsrc[0] += binc[0]; \
        dma_v(bsrc[1], bdst + (SL) * 20480 + 8192); bsrc[1] += binc[1]; \
        asm volatile("s_mov_b32 m0, %1\n\ts_mov_b32 exec_hi, 0\n\tglobal_load_lds_dwordx4 %0, off\n\ts_mov_b32 exec_hi, -1" ::"v"(bsrc[2]), "s"(hdst + (SL) * 20480) : "memory"); \
        bsrc[2] += binc[2]; } while (0)
    G1_DMA(0); G1_DMA(1); G1_DMA(2);
    const lds_cptr pa = (lds_cptr)smem + wr * 4096 + lane * 16;
    lds_cptr pb[4], pe0, pe1;
    {
        const int g = lane >> 4, q = (lane >> 2) & 3, p = lane & 3, f = (q >> 1) | ((g & 1) << 1);
        const lds_cptr b0 = (lds_cptr)smem + 65536 + g * 5120 + q * 128 + p * 8;
#pragma unroll
        for (int c = 0; c < 4; ++c) pb[c] = b0 + ((c ^ f) << 5) + wc * 3072;
        pe0 = b0 + ((wc ^ f) << 5) + 2048; pe1 = b0 + (((2 + wc) ^ f) << 5) + 2048;
    }
    f32x4 acc[4][10];
#pragma unroll
    for (int m = 0; m < 4; ++m)
#pragma unroll
        for (int n = 0; n < 10; ++n) { acc[m][n] = (f32x4){0.f, 0.f, 0.f, 0.f}; PIN(acc[m][n]); }
    asm volatile("s_waitcnt vmcnt(10)\n\ts_barrier" ::: "memory");

    if (wc == 1) asm volatile("s_barrier" ::: "memory");
    bf16x8 af[4], bf[10];
#define BFR(P, O) cat8(tr_read((P) + (O)), tr_read((P) + (O) + 512))
#define G1_READS(SL) do { \
        _Pragma("unroll") for (int c = 0; c < 4; ++c) { bf[c] = BFR(pb[c], (SL) * 20480); bf[4 + c] = BFR(pb[c], (SL) * 20480 + 1024); } \
        bf[8] = BFR(pe0, (SL) * 20480); bf[9] = BFR(pe1, (SL) * 20480); \
        _Pragma("unroll") for (int m = 0; m < 4; ++m) af[m] = *(const LAS bf16x8*)(pa + (SL) * 16384 + m * 1024); } while (0)
#define G1_STEP(SL, DMA, VM, ENDBAR) do { \
        G1_READS(SL); DMA; \
        asm volatile("s_waitcnt vmcnt(" #VM ") lgkmcnt(0)" ::: "memory"); \
        asm volatile("s_barrier" ::: "memory"); \
        SCHED(); __builtin_amdgcn_s_setprio(1); \
        _Pragma("unroll") for (int i = 0; i < 40; ++i) { const int m_ = ((i >> 2) & 1) ? 3 - (i & 3) : (i & 3); MF16_ACC(acc[m_][i >> 2], bf[i >> 2], af[m_]); } \
        __builtin_amdgcn_s_setprio(0); SCHED(); \
        if (ENDBAR) asm volatile("s_barrier" ::: "memory"); } while (0)
    const char* pf = (wid < 7 ? w2 + (size_t)(bx * 7 + wid) * 4096 : wo + (size_t)bx * 8192);
    for (int it = 0; it < 7; ++it) { G1_STEP(0, G1_DMA(3), 10, true); G1_STEP(1, G1_DMA(0), 10, true); G1_STEP(2, G1_DMA(1), 10, true); G1_STEP(3, G1_DMA(2), 10, true); }
    G1_STEP(0, G1_DMA(3), 10, true);
    G1_STEP(1, { dma_s2(avoff, pf, lds0 + 147456); dma_s2(avoff, pf + 2048, lds0 + 147456); }, 9, true);
    G1_STEP(2, {}, 4, true);
    G1_STEP(3, { if (wc == 0) dma_s1(avoff, wo + (size_t)bx * 8192 + 4096 + wid * 1024, lds0 + 147456); }, 63, wc == 0);

#undef G1_STEP
#undef G1_READS
#undef BFR
#undef G1_DMA
    asm volatile("s_nop 7" : "+v"(acc[0][9]), "+v"(acc[1][9]), "+v"(acc[2][9]), "+v"(acc[3][9]), "+v"(acc[3][8]), "+v"(acc[2][8]));
    SCHED();
    int tz = threadIdx.x; asm volatile("" : "+v"(tz));
    const int fr = tz & 15, fq = (tz >> 4) & 3;
    const int tb = (rb * 16 + wr * 4) * 1024;
    const bool ffn_main = !tq || wc == 1;
    const int cm = ((fq & 1) << 1) | (fq >> 1), ce = 2 * wc + (fq >> 1);
    {
        const int cb0 = 2 * qbx;
        const float sc = cb0 < 32 ? QSCALE : 1.0f;
        char* dst = qkv + (size_t)(cb0 + (fq & 1)) * 131072 + tb + ce * 256 + ((fr ^ (4 * ce)) * 16);
#pragma unroll
        for (int m = 0; m < 4; ++m) {
            const f32x4 va = acc[m][8] * sc, vb = acc[m][9] * sc;
            unsigned a0 = cvtpk(va[0], va[1]), a1 = cvtpk(va[2], va[3]), b0 = cvtpk(vb[0], vb[1]), b1 = cvtpk(vb[2], vb[3]);
            swap16(a0, b0); swap16(a1, b1);
            ST16(dst + m * 1024, ((u32x4){a0, a1, b0, b1}));
        }
    }
    if (ffn_main) {
        char* dst = act + (size_t)(wc ? pB : pA) * 262144 + tb + cm * 256 + fr * 16;
#pragma unroll
        for (int m = 0; m < 4; ++m)
#pragma unroll
            for (int sub = 0; sub < 2; ++sub) {
                const u32x2 a = silu_mul4(acc[m][2 * sub], acc[m][4 + 2 * sub]), b = silu_mul4(acc[m][2 * sub + 1], acc[m][4 + 2 * sub + 1]);
                unsigned a0 = a[0], a1 = a[1], b0 = b[0], b1 = b[1];
                swap16(a0, b0); swap16(a1, b1);
                ST16(dst + sub * 131072 + m * 1024, ((u32x4){a0, a1, b0, b1}));
            }
    } else {
        const int lo = tb + cm * 256 + ((fr ^ (4 * cm)) * 16);
#pragma unroll
        for (int sub = 0; sub < 4; ++sub) {
            const int cb = 2 * qb0 + sub;
            const float sc = cb < 32 ? QSCALE : 1.0f;
            char* dst = qkv + (size_t)cb * 131072 + lo;
#pragma unroll
            for (int m = 0; m < 4; ++m) {
                const f32x4 va = acc[m][2 * sub] * sc, vb = acc[m][2 * sub + 1] * sc;
                unsigned a0 = cvtpk(va[0], va[1]), a1 = cvtpk(va[2], va[3]), b0 = cvtpk(vb[0], vb[1]), b1 = cvtpk(vb[2], vb[3]);
                swap16(a0, b0); swap16(a1, b1);
                ST16(dst + m * 1024, ((u32x4){a0, a1, b0, b1}));
            }
        }
    }

}

__device__ __forceinline__ void attn_role(char* smem, const int bx, const char* __restrict__ qkv, char* __restrict__ act, unsigned* __restrict__ ctr) {

    const int tid = threadIdx.x, lane = tid & 63;
    const int wid = __builtin_amdgcn_readfirstlane(tid >> 6);
    const int head = (bx & 7) * 4 + (bx >> 6), qb = (bx >> 3) & 7;
    __builtin_amdgcn_s_setprio(2);
    const char* Qf = qkv + (size_t)head * 131072;
    const char* Kf = Qf + 4 * MB;
    const char* Vr = Qf + 8 * MB;
    const unsigned lds0 = (unsigned)(size_t)(LAS char*)smem;
    const unsigned voff = lane * 16;
    const int tq0 = qb * 8 + wid * 2;
    unsigned fo[2];
#pragma unroll
    for (int s = 0; s < 2; ++s) { const int c = 2 * s + (lane >> 5); fo[s] = ((lane >> 4) & 1) * 1024 + c * 256 + (((lane & 15) ^ (4 * c)) * 16); }
    bf16x8 q[2][2];
    asm volatile("global_load_dwordx4 %0, %4, %6\n\tglobal_load_dwordx4 %1, %5, %6\n\tglobal_load_dwordx4 %2, %4, %6 offset:2048\n\tglobal_load_dwordx4 %3, %5, %6 offset:2048"
                 : "=&v"(q[0][0]), "=&v"(q[0][1]), "=&v"(q[1][0]), "=&v"(q[1][1]) : "v"(fo[0]), "v"(fo[1]), "s"(Qf + tq0 * 2048) : "memory");
    bf16x8 sel;
    { const short one = (((lane >> 2) & 1) == ((lane >> 4) & 1)) ? (short)0x3f80 : (short)0; sel = (bf16x8){one, one, one, one, one, one, one, one}; }
    const lds_cptr kp0 = (lds_cptr)smem + fo[0], kp1 = (lds_cptr)smem + fo[1];
    lds_cptr vp0, vp1;
    { const int h = lane >> 5, c = 2 * ((lane >> 4) & 1) + ((lane >> 1) & 1), q4 = (lane >> 2) & 3;
      vp0 = (lds_cptr)smem + 8192 + c * 256 + (h ^ c) * 64 + q4 * 16 + (lane & 1) * 8; vp1 = (lds_cptr)smem + 8192 + c * 256 + ((2 + h) ^ c) * 64 + q4 * 16 + (lane & 1) * 8; }
    const char* Kw = Kf + wid * 2048;
    const char* Vw = Vr + wid * 2048;
    volatile LAS unsigned* flag = (volatile LAS unsigned*)(smem);
    const unsigned ooff = ((lane >> 4) & 1) * 1024 + (lane >> 5) * 256 + (lane & 15) * 16;
#define AT_DMA(ST, SLOT) do { const unsigned d_ = lds0 + (SLOT) + wid * 2048; \
        dma_s2(voff, Kf + (ST) * 8192 + wid * 2048, d_); dma_s2(voff, Vr + (ST) * 8192 + wid * 2048, d_ + 8192); } while (0)
    for (int attempt = 0; attempt < 2; ++attempt) {
        f32x16 o[2]; f32x4 l[2];
        bf16x8 k0, k1;
        asm volatile("global_load_dwordx4 %0, %2, %4\n\tglobal_load_dwordx4 %1, %3, %4" : "=&v"(k0), "=&v"(k1) : "v"(fo[0]), "v"(fo[1]), "s"(Kf) : "memory");
        AT_DMA(0, 0); AT_DMA(1, 16384); AT_DMA(2, 32768);
        o[0] = (f32x16){}; o[1] = (f32x16){}; l[0] = (f32x4){}; l[1] = (f32x4){};
        PIN(o[0]); PIN(o[1]); PIN(l[0]); PIN(l[1]);
        asm volatile("s_waitcnt vmcnt(12)" : "+v"(k0), "+v"(k1), "+v"(q[0][0]), "+v"(q[0][1]), "+v"(q[1][0]), "+v"(q[1][1])::"memory");
        f32x16 negm[2];
        {
            float m0 = -INFINITY, m1 = -INFINITY;
            const int nt = attempt == 0 ? 1 : 64;
            for (int kt = 0; kt < nt; ++kt) {
                if (kt) { k0 = *(const bf16x8*)(Kf + kt * 2048 + fo[0]); k1 = *(const bf16x8*)(Kf + kt * 2048 + fo[1]); }
                f32x16 s0 = MF32(k0, q[0][0], (f32x16){}), s1 = MF32(k0, q[1][0], (f32x16){});
                s0 = MF32(k1, q[0][1], s0); s1 = MF32(k1, q[1][1], s1);
#pragma unroll
                for (int r = 0; r < 16; ++r) { m0 = fmaxf(m0, s0[r]); m1 = fmaxf(m1, s1[r]); }
            }
            m0 = fmaxf(m0, __shfl_xor(m0, 32)); m1 = fmaxf(m1, __shfl_xor(m1, 32));
#pragma unroll
            for (int r = 0; r < 16; ++r) { negm[0][r] = -m0; negm[1][r] = -m1; }
            PIN(negm[0]); PIN(negm[1]);
        }
        asm volatile("s_waitcnt vmcnt(0)\n\ts_barrier" ::: "memory");
        bf16x8 kf0 = *(const LAS bf16x8*)(kp0), kf1 = *(const LAS bf16x8*)(kp1);
        bf16x8 vf0 = (bf16x8){}, vf1 = (bf16x8){};
        u32x4 pa0 = (u32x4){}, pa1 = (u32x4){}, pb0 = (u32x4){}, pb1 = (u32x4){};
        f32x16 sA = MF32(kf0, q[0][0], negm[0]); sA = MF32(kf1, q[0][1], sA);
        f32x16 sB;
#define EX(X, i) X[i] = __builtin_amdgcn_exp2f(X[i])
#define PK(X, i) cvtpk(X[i], X[(i) + 1])
#define BC8(x) __builtin_bit_cast(bf16x8, x)
#define AT_UNIT(SC, PC0, PC1, SN, QN, NM, PP0, PP1, OP, LP, K0, K1, V0, V1, L0, L1, L2, L3) do { unsigned c0, c1, c2, c3, c4, c5, c6, c7; \
            SN = MF32(K0, QN[0], NM); SCHED(); L0; SCHED(); EX(SC, 0); EX(SC, 1); EX(SC, 2); SCHED(); \
            OP = MF32(V0, BC8(PP0), OP); SCHED(); L1; SCHED(); c0 = PK(SC, 0); EX(SC, 3); EX(SC, 4); EX(SC, 5); SCHED(); \
            SN = MF32(K1, QN[1], SN); SCHED(); L2; SCHED(); c1 = PK(SC, 2); c2 = PK(SC, 4); EX(SC, 6); EX(SC, 7); EX(SC, 8); SCHED(); \
            OP = MF32(V1, BC8(PP1), OP); SCHED(); L3; SCHED(); c3 = PK(SC, 6); EX(SC, 9); EX(SC, 10); EX(SC, 11); SCHED(); \
            MF16_ACC(LP, sel, PP0); SCHED(); c4 = PK(SC, 8); c5 = PK(SC, 10); EX(SC, 12); EX(SC, 13); SCHED(); \
            MF16_ACC(LP, sel, PP1); SCHED(); EX(SC, 14); EX(SC, 15); SCHED(); c6 = PK(SC, 12); SCHED(); c7 = PK(SC, 14); \
            PC0 = (u32x4){c0, c1, c2, c3}; PC1 = (u32x4){c4, c5, c6, c7}; SCHED(); } while (0)
#define AT_M0(SLOT, V) asm volatile("s_mov_b32 m0, %0" ::"s"(lds0 + (SLOT) + wid * 2048 + (V) * 8192) : "memory")
#define AT_PAIR(ST, V) asm volatile("global_load_lds_dwordx4 %0, %1\n\tglobal_load_lds_dwordx4 %0, %1 offset:1024" ::"v"(voff), "s"(((V) ? Vw : Kw) + ((ST) & 15) * 8192) : "memory")
#define AT_STAGE(SO, SN_, DM, ST, SLA, SLB) do { _Pragma("unroll") for (int kt = 0; kt < 4; ++kt) { \
                const int ko = kt < 3 ? (SO) + (kt + 1) * 2048 : (SN_), vo = (SO) + kt * 2048; \
                bf16x8 kn0, kn1, vn0, vn1; \
                AT_UNIT(sA, pa0, pa1, sB, q[1], negm[1], pb0, pb1, o[1], l[1], kf0, kf1, vf0, vf1, \
                        (kn0 = *(const LAS bf16x8*)(kp0 + ko), kn1 = *(const LAS bf16x8*)(kp1 + ko)), vn0 = cat8(tr_read(vp0 + vo), tr_read(vp1 + vo)), \
                        vn1 = cat8(tr_read(vp0 + vo + 1024), tr_read(vp1 + vo + 1024)), {}); \
                kf0 = kn0; kf1 = kn1; vf0 = vn0; vf1 = vn1; \
                __builtin_amdgcn_s_waitcnt(0xC07F); SCHED(); \
                AT_UNIT(sB, pb0, pb1, sA, q[0], negm[0], pa0, pa1, o[0], l[0], kf0, kf1, vf0, vf1, \
                        if (DM) AT_M0((kt >> 1) ? (SLB) : (SLA), kt & 1), if (DM) AT_PAIR((ST) + (kt >> 1), kt & 1), {}, {}); } } while (0)
#define NXT(x) ((x) == 65536 ? 0 : (x) + 16384)
        int s0 = 0;
        for (int k = 0; k < 8; ++k) {
            const int s1 = NXT(s0), s2 = NXT(s1), s3 = NXT(s2), s4 = NXT(s3);
            AT_STAGE(s0, s1, true, 2 * k + 3, s3, s4);
            AT_STAGE(s1, s2, false, 0, 0, 0);
            s0 = s2;
            asm volatile("s_waitcnt vmcnt(0)\n\ts_barrier" ::: "memory");
        }
#undef NXT
#undef AT_STAGE
#undef AT_M0
#undef AT_PAIR
        o[1] = MF32(vf0, BC8(pb0), o[1]); o[1] = MF32(vf1, BC8(pb1), o[1]);
        l[1] = MF16(sel, BC8(pb0), l[1]); l[1] = MF16(sel, BC8(pb1), l[1]);
#undef AT_UNIT
        asm volatile("s_nop 7\n\ts_nop 7" : "+v"(l[0]), "+v"(l[1]), "+v"(o[0]), "+v"(o[1]));
        float tsum = 0.f;
#pragma unroll
        for (int i = 0; i < 2; ++i) {
            const float r = 1.0f / l[i][0];
#pragma unroll
            for (int e = 0; e < 16; ++e) { tsum += fabsf(o[i][e]); o[i][e] *= r; }
        }
        const bool bad = !(l[0][0] < 1.0995e12f) || !(l[1][0] < 1.0995e12f) || !(tsum < INFINITY);
        if (lane == 0) flag[wid] = 0u;
        if (bad) flag[wid] = 1u;
#pragma unroll
        for (int i = 0; i < 2; ++i)
#pragma unroll
            for (int s = 0; s < 2; ++s) {
                unsigned a0 = cvtpk(o[i][8 * s + 0], o[i][8 * s + 1]), a1 = cvtpk(o[i][8 * s + 2], o[i][8 * s + 3]);
                unsigned b0 = cvtpk(o[i][8 * s + 4], o[i][8 * s + 5]), b1 = cvtpk(o[i][8 * s + 6], o[i][8 * s + 7]);
                swap32(a0, b0); swap32(a1, b1);
                const u32x4 w = (u32x4){a0, a1, b0, b1};
                asm volatile("global_store_dwordx4 %0, %1, %2 sc1\n\ts_nop 1" ::"v"(ooff), "v"(w), "s"(act + (size_t)(112 + head) * 131072 + (tq0 + i) * 2048 + s * 512) : "memory");
            }
        asm volatile("s_waitcnt vmcnt(0) lgkmcnt(0)\n\ts_barrier" ::: "memory");
        const unsigned redo = flag[0] | flag[1] | flag[2] | flag[3];
        if (attempt == 1 || !__builtin_amdgcn_readfirstlane(redo)) break;
        asm volatile("s_waitcnt lgkmcnt(0)\n\ts_barrier" ::: "memory");
    }
#undef AT_DMA
    if (tid == 0) __hip_atomic_fetch_add(ctr + qb * 64, 1u, __ATOMIC_RELAXED, __HIP_MEMORY_SCOPE_AGENT);

}

__device__ __forceinline__ void gemm2_role(char* smem, const int bx, const char* __restrict__ act, const char* __restrict__ w2, const char* __restrict__ wo,
                                           const char* __restrict__ x, char* __restrict__ out, unsigned* __restrict__ ctr) {

    const int tid = threadIdx.x, lane = tid & 63;
    const int wid = __builtin_amdgcn_readfirstlane(tid >> 6);
    const int xc = bx & 7, ii = bx >> 3, tm = (xc >> 1) * 4 + (ii >> 3), tn = (xc & 1) * 8 + (ii & 7);
    const unsigned lds0 = (unsigned)(size_t)(LAS char*)smem + wid * 16384;
    __builtin_amdgcn_s_sleep(16);
    const char* asrc = act + (size_t)wid * 131072 + tm * 8192;
    unsigned av[4];
#pragma unroll
    for (int i = 0; i < 4; ++i) av[i] = (i ^ wid) * 2048 + lane * 16;
    const char* wsrc = w2 + (size_t)wid * 32 * 2048 + tn * 128;
    const unsigned wv0 = (lane >> 3) * 2048 + ((((lane >> 1) & 3) ^ ((lane >> 4) & 1)) * 32) + (lane & 1) * 16;
    const unsigned wv1 = (lane >> 3) * 2048 + ((((lane >> 1) & 3) ^ ((lane >> 4) & 1) ^ 2) * 32) + (lane & 1) * 16 + 16384 - 1024;
    lds_cptr wp[4];
    {
        const int g = lane >> 4, q = (lane >> 2) & 3, p = lane & 3, f = (q >> 1) | ((g & 1) << 1);
#pragma unroll
        for (int c = 0; c < 4; ++c) wp[c] = (lds_cptr)smem + wid * 16384 + g * 1024 + q * 128 + ((c ^ f) << 5) + p * 8;
    }
    f32x4 acc[8][4];
#pragma unroll
    for (int i = 0; i < 8; ++i)
#pragma unroll
        for (int n = 0; n < 4; ++n) { acc[i][n] = (f32x4){}; PIN(acc[i][n]); }
    bf16x8 a0[4], a1[4], a2[4], a3[4], wfa[4], wfb[4];
#define G2_LDA(A, H) do { \
        asm volatile("global_load_dwordx4 %0, %4, %6\n\tglobal_load_dwordx4 %1, %4, %6 offset:1024\n\tglobal_load_dwordx4 %2, %5, %6\n\tglobal_load_dwordx4 %3, %5, %6 offset:1024" \
                     : "=&v"(A[0]), "=&v"(A[1]), "=&v"(A[2]), "=&v"(A[3]) : "v"(av[2 * (H)]), "v"(av[2 * (H) + 1]), "s"(asrc) : "memory"); \
        if (H) asrc += 524288; } while (0)
#define G2_LDW(SL) do {     \
        asm volatile("s_mov_b32 m0, %3\n\ts_nop 0\n\tglobal_load_lds_dwordx4 %0, %2\n\tglobal_load_lds_dwordx4 %1, %2 offset:1024" \
                     ::"v"(wv0), "v"(wv1), "s"(wsrc), "s"(lds0 + (SL) * 2048) : "memory"); \
        wsrc += ((SL) & 1) ? 262144 - 32768 : 32768; } while (0)
#define G2_WAIT(N, A) asm volatile("s_waitcnt vmcnt(" #N ")" : "+v"(A[0]), "+v"(A[1]), "+v"(A[2]), "+v"(A[3])::"memory")
#define WFR(P, O) cat8(tr_read((P) + (O)), tr_read((P) + (O) + 512))
#define G2_STEP(N, AC, AN, WC, WN, SL, RD, IA, IW) do { \
        if ((N) >= 0) G2_WAIT(N, AN); \
        if (RD) { WN[2 * ((SL) & 1)] = WFR(wp[2 * ((SL) & 1)], ((((SL) >> 1) + 1) & 3) * 4096); WN[2 * ((SL) & 1) + 1] = WFR(wp[2 * ((SL) & 1) + 1], ((((SL) >> 1) + 1) & 3) * 4096); } \
        SCHED(); \
        _Pragma("unroll") for (int i = 0; i < 16; ++i) MF16_ACC(acc[4 * ((SL) & 1) + (i >> 2)][((i >> 2) & 1) ? 3 - (i & 3) : (i & 3)], WC[((i >> 2) & 1) ? 3 - (i & 3) : (i & 3)], AC[i >> 2]); \
        SCHED(); if (IA) G2_LDA(AC, (SL) & 1); if (IW) G2_LDW(SL); } while (0)
#define G2_STEP4(N0, N1, N2, N3, SL, RDL, IA, IW) do { \
        G2_STEP(N0, a0, a1, wfa, wfb, (SL), true, IA, IW); G2_STEP(N1, a1, a2, wfa, wfb, (SL) + 1, true, IA, IW); \
        G2_STEP(N2, a2, a3, wfb, wfa, (SL) + 2, RDL, IA, IW); G2_STEP(N3, a3, a0, wfb, wfa, (SL) + 3, RDL, IA, IW); } while (0)
    G2_LDW(0); G2_LDW(1); G2_LDW(2); G2_LDW(3);
    G2_LDA(a0, 0); G2_LDW(4); G2_LDA(a1, 1); G2_LDW(5); G2_LDA(a2, 0); G2_LDW(6); G2_LDA(a3, 1); G2_LDW(7);
    G2_WAIT(20, a0);
#pragma unroll
    for (int c = 0; c < 4; ++c) wfa[c] = WFR(wp[c], 0);
    for (int it = 0; it < 6; ++it) { G2_STEP4(14, 14, 14, 14, 0, true, true, true); G2_STEP4(14, 14, 14, 14, 4, true, true, true); }
    G2_STEP4(14, 12, 10, 8, 0, true, true, false);
    G2_STEP4(8, 4, 0, -1, 4, false, false, false);
#undef G2_STEP4
#undef G2_STEP
#undef G2_WAIT
#undef G2_LDW
#undef G2_LDA
    const int fr = lane & 15, fq = lane >> 4;
    const size_t orow = (size_t)(tm * 128 + wid * 32 + fr) * 2048 + tn * 128 + (fq & 1) * 32 + (fq >> 1) * 16;
    u32x4 xr[4];
#pragma unroll
    for (int c = 0; c < 4; ++c) xr[c] = *(const u32x4*)(x + orow + (c >> 1) * 32768 + (c & 1) * 64);
    asm volatile("s_nop 7" : "+v"(acc[7][0]), "+v"(acc[7][1]), "+v"(acc[7][2]), "+v"(acc[7][3]));
    SCHED();
    char* mine = smem + wid * 16384 + lane * 16;
#pragma unroll
    for (int i = 0; i < 16; ++i) *(f32x4*)(mine + i * 1024) = acc[4 + (i >> 2)][i & 3];
    __syncthreads();
#pragma unroll
    for (int i = 0; i < 16; ++i) acc[i >> 2][i & 3] += *(const f32x4*)(smem + (wid ^ 2) * 16384 + lane * 16 + i * 1024);
    __syncthreads();
#pragma unroll
    for (int i = 0; i < 8; ++i) *(f32x4*)(mine + i * 1024) = acc[2 + (i >> 2)][i & 3];
    __syncthreads();
#pragma unroll
    for (int i = 0; i < 8; ++i) { acc[i >> 2][i & 3] += *(const f32x4*)(smem + (wid ^ 1) * 16384 + lane * 16 + i * 1024); PIN(acc[i >> 2][i & 3]); }
    __syncthreads();
    const unsigned ldsb = (unsigned)(size_t)(LAS char*)smem;
    const char* wos = wo + tn * 128;
    const char* osrc = act + (size_t)112 * 131072 + tm * 8192 + wid * 2048;
    unsigned ov = lane * 16;
    lds_cptr wq[4];
#pragma unroll
    for (int c = 0; c < 4; ++c) { wq[c] = wp[c] - wid * 16384; PIN(wq[c]); }
    PIN(ov);
#define G3_LDW(HS, SLOT) asm volatile("s_mov_b32 m0, %3\n\ts_nop 0\n\tglobal_load_lds_dwordx4 %0, %2\n\tglobal_load_lds_dwordx4 %1, %2 offset:1024" \
                     ::"v"(wv0), "v"(wv1), "s"(wos + (size_t)(HS) * 32768), "s"(ldsb + (SLOT) * 2048) : "memory")
#pragma unroll
    for (int i = 0; i < 10; ++i) G3_LDW(wid * 10 + i, wid * 10 + i);
    if (wid == 0) {
        const unsigned long long t0 = __builtin_amdgcn_s_memrealtime();
        while ((unsigned)__builtin_amdgcn_readfirstlane(__hip_atomic_load(ctr + (tm >> 1) * 64, __ATOMIC_RELAXED, __HIP_MEMORY_SCOPE_AGENT)) < 32u) {
            if (__builtin_amdgcn_s_memrealtime() - t0 > 5000000ull) break;
            __builtin_amdgcn_s_sleep(2);
        }
        __builtin_amdgcn_fence(__ATOMIC_ACQUIRE, "wavefront");
        asm volatile("" ::: "memory");
    }
    asm volatile("s_waitcnt vmcnt(0)\n\ts_barrier" ::: "memory");

    bf16x8 oa[8][4];
#define G3_LDA(G) asm volatile("global_load_dwordx4 %0, %4, %5 sc1\n\tglobal_load_dwordx4 %1, %4, %5 offset:1024 sc1\n\tglobal_load_dwordx4 %2, %4, %6 sc1\n\tglobal_load_dwordx4 %3, %4, %6 offset:1024 sc1" \
                     : "=&v"(oa[(G) & 7][0]), "=&v"(oa[(G) & 7][1]), "=&v"(oa[(G) & 7][2]), "=&v"(oa[(G) & 7][3]) \
                     : "v"(ov), "s"(osrc + (size_t)(G) * 262144), "s"(osrc + (size_t)(G) * 262144 + 131072) : "memory")
#define G3_WAIT(N, G) asm volatile("s_waitcnt vmcnt(" #N ")" : "+v"(oa[(G) & 7][0]), "+v"(oa[(G) & 7][1]), "+v"(oa[(G) & 7][2]), "+v"(oa[(G) & 7][3])::"memory")
    s16x4 wl[2][8], wh[2][8];
#define G3_WA(G, i) (wq[(i) & 3] + (((G) >= 8 && (G) < 10) ? 65536 : 0) + ((((G) * 2 + ((i) >> 2)) % 20) * 4096 - (((G) >= 8 && (G) < 10) ? 65536 : 0)))
#define G3_GROUP(N, G, RD) do { G3_WAIT(N, G); SCHED(); \
        const bool rf_ = (G) == 5 || (G) == 6; \
        _Pragma("unroll") for (int i = 0; i < 8; ++i) { const bf16x8 w_ = cat8(wl[(G) & 1][i], wh[(G) & 1][i]); \
            MF16_ACC(acc[0][i & 3], w_, oa[(G) & 7][2 * (i >> 2)]); SCHED(); if (RD) wl[((G) + 1) & 1][i] = tr_read(G3_WA((G) + 1, i)); SCHED(); \
            MF16_ACC(acc[1][i & 3], w_, oa[(G) & 7][2 * (i >> 2) + 1]); SCHED(); if (RD) wh[((G) + 1) & 1][i] = tr_read(G3_WA((G) + 1, i) + 512); \
            if (rf_ && (i & 1) && i < 6) G3_LDW(40 + wid * 6 + ((G) - 5) * 3 + (i >> 1), wid * 6 + ((G) - 5) * 3 + (i >> 1)); SCHED(); } \
        if ((G) < 4) { G3_LDA(2 * (G) + 4); G3_LDA(2 * (G) + 5); } else if ((G) < 8) G3_LDA((G) + 8); } while (0)
    G3_LDA(0); G3_LDA(1); G3_LDA(2); G3_LDA(3);
#pragma unroll
    for (int i = 0; i < 8; ++i) { wl[0][i] = tr_read(G3_WA(0, i)); wh[0][i] = tr_read(G3_WA(0, i) + 512); }
    G3_GROUP(12, 0, true); G3_GROUP(16, 1, true); G3_GROUP(20, 2, true); G3_GROUP(24, 3, true); G3_GROUP(28, 4, true);
    G3_WAIT(28, 5);

    asm volatile("s_waitcnt lgkmcnt(0)\n\ts_barrier" ::: "memory");
    G3_GROUP(28, 5, true); G3_GROUP(34, 6, true); G3_GROUP(40, 7, true); G3_GROUP(40, 8, true);

    asm volatile("s_waitcnt vmcnt(8)\n\ts_barrier" ::: "memory");

    G3_GROUP(8, 9, true); G3_GROUP(8, 10, true); G3_GROUP(8, 11, true); G3_GROUP(8, 12, true); G3_GROUP(8, 13, true); G3_GROUP(4, 14, true); G3_GROUP(0, 15, false);

#undef G3_GROUP
#undef G3_WA
#undef G3_WAIT
#undef G3_LDA
#undef G3_LDW
#undef WFR
    asm volatile("s_nop 7" : "+v"(acc[0][0]), "+v"(acc[0][1]), "+v"(acc[0][2]), "+v"(acc[0][3]), "+v"(acc[1][0]), "+v"(acc[1][1]), "+v"(acc[1][2]), "+v"(acc[1][3]));
#pragma unroll
    for (int c = 0; c < 4; ++c) {
        float v[8];
#pragma unroll
        for (int e = 0; e < 4; ++e) {
            unsigned a = __float_as_uint(acc[c >> 1][2 * (c & 1)][e]), b = __float_as_uint(acc[c >> 1][2 * (c & 1) + 1][e]);
            swap16(a, b);
            v[e] = __uint_as_float(a); v[4 + e] = __uint_as_float(b);
        }
        u32x4 w;
#pragma unroll
        for (int e = 0; e < 4; ++e) w[e] = cvtpk(v[2 * e] + bf_lo(xr[c][e]), v[2 * e + 1] + bf_hi(xr[c][e]));
        *(u32x4*)(out + orow + (c >> 1) * 32768 + (c & 1) * 64) = w;
    }

}

__global__ __launch_bounds__(256, 2) void k_fused(const char* __restrict__ qkv, char* __restrict__ act, const char* __restrict__ w2, const char* __restrict__ wo,
                                                  const char* __restrict__ x, char* __restrict__ out, unsigned* __restrict__ ctr) {
    __shared__ __attribute__((aligned(1024))) char smem[81920];
    asm volatile("" ::"s"(qkv), "s"(act), "s"(w2));
    if (__builtin_expect(blockIdx.x < 256, 1)) attn_role(smem, blockIdx.x, qkv, act, ctr);
    else gemm2_role(smem, blockIdx.x - 256, act, w2, wo, x, out, ctr);
}

extern "C" void kernel_launch(void* const* d_in, const int* in_sizes, int n_in, void* d_out, int out_size, void* d_ws, size_t ws_size,
                              hipStream_t stream) {
    const u16* x = (const u16*)d_in[0];
    const char* wqkv = (const char*)d_in[1];
    const char* wo = (const char*)d_in[2];
    const char* w1 = (const char*)d_in[4];
    const char* w3 = (const char*)d_in[5];
    const char* w2 = (const char*)d_in[6];
    char* ws = (char*)d_ws;
    k_ln<<<512, 512, 0, stream>>>(x, ws + WS_XN, (unsigned*)(ws + WS_CTR), wqkv, w1, w3);
    k_gemm1<<<256, 512, 0, stream>>>(ws + WS_XN, wqkv, w1, w3, ws + WS_QKV, ws + WS_ACT, w2, wo);
    k_fused<<<512, 256, 0, stream>>>(ws + WS_QKV, ws + WS_ACT, w2, wo, (const char*)x, (char*)d_out, (unsigned*)(ws + WS_CTR));
}
```

```cpp
#include <hip/hip_runtime.h>
#include <stdint.h>

typedef unsigned short u16;
typedef short bf16x8 __attribute__((ext_vector_type(8)));
typedef short s16x4 __attribute__((ext_vector_type(4)));
typedef float f32x2 __attribute__((ext_vector_type(2)));
typedef float f32x4 __attribute__((ext_vector_type(4)));
typedef float f32x16 __attribute__((ext_vector_type(16)));
typedef unsigned u32x2 __attribute__((ext_vector_type(2)));
typedef unsigned u32x4 __attribute__((ext_vector_type(4)));
typedef __bf16 bf16x2_t __attribute__((ext_vector_type(2)));
#define LAS __attribute__((address_space(3)))
typedef const LAS char* lds_cptr;

constexpr int SEQ = 2048, DM = 1024, FF = 3584, NH = 32;
constexpr size_t MB = 1024 * 1024;
constexpr size_t WS_CTR = 0;
constexpr size_t WS_XN = 65536;
constexpr size_t WS_QKV = WS_XN + 4 * MB;
constexpr size_t WS_ACT = WS_QKV + 12 * MB;
constexpr float QSCALE = 0.17677669529663687f * 1.4426950408889634f;

#define SCHED() __builtin_amdgcn_sched_barrier(0)
#define PIN(x) asm volatile("" : "+v"(x))
#define MF32(a, b, c) __builtin_amdgcn_mfma_f32_32x32x16_bf16(a, b, c, 0, 0, 0)
#define MF16(a, b, c) __builtin_amdgcn_mfma_f32_16x16x32_bf16(a, b, c, 0, 0, 0)

#define MF16_ACC(acc, a, b) asm volatile("v_mfma_f32_16x16x32_bf16 %0, %1, %2, %0" : "+v"(acc) : "v"(a), "v"(b))

__device__ __forceinline__ unsigned cvtpk(float lo, float hi) {
    f32x2 v = {lo, hi};
    bf16x2_t b = __builtin_convertvector(v, bf16x2_t);
    return __builtin_bit_cast(unsigned, b);
}
__device__ __forceinline__ float bf_lo(unsigned w) { return __uint_as_float(w << 16); }
__device__ __forceinline__ float bf_hi(unsigned w) { return __uint_as_float(w & 0xffff0000u); }
__device__ __forceinline__ float rbf(float x) { return bf_lo(cvtpk(x, x)); }
__device__ __forceinline__ s16x4 tr_read(lds_cptr p) {
    return __builtin_bit_cast(s16x4, __builtin_amdgcn_ds_read_tr16_b64_v4i16((LAS s16x4*)p));
}
__device__ __forceinline__ bf16x8 cat8(s16x4 lo, s16x4 hi) { return __builtin_shufflevector(lo, hi, 0, 1, 2, 3, 4, 5, 6, 7); }
__device__ __forceinline__ void dma_v(const void* g, unsigned lds) {
    asm volatile("s_mov_b32 m0, %1\n\ts_nop 0\n\tglobal_load_lds_dwordx4 %0, off" ::"v"(g), "s"(lds) : "memory");
}
__device__ __forceinline__ void dma_s1(unsigned voff, const void* sbase, unsigned lds) {
    asm volatile("s_mov_b32 m0, %2\n\ts_nop 0\n\tglobal_load_lds_dwordx4 %0, %1" ::"v"(voff), "s"(sbase), "s"(lds) : "memory");
}
__device__ __forceinline__ void dma_s2(unsigned voff, const void* sbase, unsigned lds) {
    asm volatile("s_mov_b32 m0, %2\n\ts_nop 0\n\tglobal_load_lds_dwordx4 %0, %1\n\tglobal_load_lds_dwordx4 %0, %1 offset:1024" ::"v"(voff), "s"(sbase), "s"(lds) : "memory");
}
__device__ __forceinline__ void swap16(unsigned& a, unsigned& b) {
    auto r = __builtin_amdgcn_permlane16_swap(a, b, false, false);
    a = r[0]; b = r[1];
}
__device__ __forceinline__ void swap32(unsigned& a, unsigned& b) {
    auto r = __builtin_amdgcn_permlane32_swap(a, b, false, false);
    a = r[0]; b = r[1];
}

__device__ __forceinline__ float wave_sum(float v) {
    v += __builtin_bit_cast(float, __builtin_amdgcn_update_dpp(0, __builtin_bit_cast(int, v), 0xB1, 0xf, 0xf, true));
    v += __builtin_bit_cast(float, __builtin_amdgcn_update_dpp(0, __builtin_bit_cast(int, v), 0x4E, 0xf, 0xf, true));
    v += __builtin_bit_cast(float, __builtin_amdgcn_update_dpp(0, __builtin_bit_cast(int, v), 0x141, 0xf, 0xf, true));
    v += __builtin_bit_cast(float, __builtin_amdgcn_update_dpp(0, __builtin_bit_cast(int, v), 0x140, 0xf, 0xf, true));
    auto a = __builtin_amdgcn_permlane16_swap(__float_as_uint(v), __float_as_uint(v), false, false);
    v = __uint_as_float(a[0]) + __uint_as_float(a[1]);
    auto b = __builtin_amdgcn_permlane32_swap(__float_as_uint(v), __float_as_uint(v), false, false);
    return __uint_as_float(b[0]) + __uint_as_float(b[1]);
}
__global__ __launch_bounds__(512) void k_ln(const u16* __restrict__ x, char* __restrict__ xnF, unsigned* __restrict__ ctr,
                                            const char* __restrict__ wqkv, const char* __restrict__ w1, const char* __restrict__ w3) {
    __shared__ __attribute__((aligned(16))) char sm[16384];
    const int tid = threadIdx.x, lane = tid & 63, wid = tid >> 6;
    if (blockIdx.x >= 256) {
        const size_t o = (size_t)((blockIdx.x - 256) * 512 + tid) * 16;
        const u32x4 p0 = *(const u32x4*)(wqkv + o), p1 = *(const u32x4*)(w1 + o), p2 = *(const u32x4*)(w3 + o);
        asm volatile("" ::"v"(p0), "v"(p1), "v"(p2));
        return;
    }
    if (blockIdx.x == 0) { ctr[tid] = 0u; ctr[tid + 512] = 0u; }
    const int row = blockIdx.x * 8 + wid;
    const u32x4* src = (const u32x4*)(x + (size_t)row * DM);
    const u32x4 a = src[lane], b = src[64 + lane];
    float v[16];
#pragma unroll
    for (int i = 0; i < 4; ++i) { v[2 * i] = bf_lo(a[i]); v[2 * i + 1] = bf_hi(a[i]); v[8 + 2 * i] = bf_lo(b[i]); v[8 + 2 * i + 1] = bf_hi(b[i]); }
    float s = 0.f;
#pragma unroll
    for (int i = 0; i < 16; ++i) s += v[i];
    s = wave_sum(s);
    const float mean = s * (1.0f / DM);
    float q = 0.f;
#pragma unroll
    for (int i = 0; i < 16; ++i) { const float d = v[i] - mean; q += d * d; }
    q = wave_sum(q);
    const float mb = rbf(mean), vb = rbf(q * (1.0f / DM));
    const float rs = 1.0f / rbf(sqrtf(rbf(vb + 1e-5f)));
    u32x4 oa, ob;
#pragma unroll
    for (int i = 0; i < 4; ++i) {
        oa[i] = cvtpk(rbf(v[2 * i] - mb) * rs, rbf(v[2 * i + 1] - mb) * rs);
        ob[i] = cvtpk(rbf(v[8 + 2 * i] - mb) * rs, rbf(v[8 + 2 * i + 1] - mb) * rs);
    }
    *(u32x4*)(sm + lane * 128 + wid * 16) = oa;
    *(u32x4*)(sm + (64 + lane) * 128 + wid * 16) = ob;
    __syncthreads();
    const int row0 = blockIdx.x * 8;
#pragma unroll
    for (int i = 0; i < 2; ++i) {
        const int p = tid + i * 512, chunk = p >> 3, r = p & 7;
        const u32x4 w = *(const u32x4*)(sm + p * 16);
        *(u32x4*)(xnF + ((size_t)(chunk >> 2) * 128 + (row0 >> 4)) * 1024 + (chunk & 3) * 256 + ((row0 & 15) + r) * 16) = w;
    }
}

__device__ __forceinline__ unsigned silu_mul2(f32x2 g, f32x2 u) {
    const f32x2 t = g * -1.4426950408889634f;
    f32x2 e; e.x = __builtin_amdgcn_exp2f(t.x); e.y = __builtin_amdgcn_exp2f(t.y);
    e = e + 1.0f;
    f32x2 r; r.x = __builtin_amdgcn_rcpf(e.x); r.y = __builtin_amdgcn_rcpf(e.y);
    const f32x2 h = (g * u) * r;
    return cvtpk(h.x, h.y);
}
__device__ __forceinline__ u32x2 silu_mul4(f32x4 g, f32x4 u) {
    return (u32x2){silu_mul2((f32x2){g[0], g[1]}, (f32x2){u[0], u[1]}), silu_mul2((f32x2){g[2], g[3]}, (f32x2){u[2], u[3]})};
}
#define ST16(p, v) asm volatile("global_store_dwordx4 %0, %1, off sc1\n\ts_nop 1" ::"v"(p), "v"(v) : "memory")
__global__ __launch_bounds__(512, 2) void k_gemm1(const char* __restrict__ xnF, const char* __restrict__ wqkv, const char* __restrict__ w1,
                                                  const char* __restrict__ w3, char* __restrict__ qkv, char* __restrict__ act,
                                                  const char* __restrict__ w2, const char* __restrict__ wo) {
    __shared__ __attribute__((aligned(1024))) char smem[147456 + 2048];

    const int tid = threadIdx.x, lane = tid & 63;
    const int wid = __builtin_amdgcn_readfirstlane(tid >> 6), wr = wid & 3, wc = wid >> 2;
    const int bx = blockIdx.x, j = (bx & 7) + 8 * ((bx >> 3) & 3), rb = bx >> 5;
    const unsigned lds0 = (unsigned)(size_t)(LAS char*)smem;
    const bool tq = j < 8;
    const int qb0 = 3 * j, qbx = tq ? 3 * j + 2 : 16 + j, pA = 2 * j - 8, pB = tq ? j : 2 * j - 7;
    const char* bsrc[3]; unsigned binc[3];
#pragma unroll
    for (int i = 0; i < 3; ++i) {
        const int id = i < 2 ? wid + 8 * i : 16 + wr, kb = (id * 13) >> 6, blk = id - 5 * kb;
        const int r = i < 2 ? lane >> 3 : ((lane >> 3) & 3) + 4 * wc, j8 = lane & 7, f = ((r >> 1) & 1) | ((kb & 1) << 1), cp = (j8 >> 1) ^ f;
        const bool ffn = blk >= 3 || (blk < 2 && !tq);
        const int pitch = ffn ? 7168 : 6144;
        const uint64_t wg = (blk == 0 || blk == 3) ? (uint64_t)w1 : (uint64_t)w3, wb = ffn ? wg : (uint64_t)wqkv;
        const int cf = blk < 2 ? pA : pB, cq = blk == 2 ? qbx : qb0 + blk, cblk = ffn ? cf : cq;
        bsrc[i] = (const char*)wb + (size_t)(unsigned)(cblk * 128 + kb * 8 * pitch) + (size_t)(unsigned)(r * pitch + cp * 32 + (j8 & 1) * 16);
        binc[i] = 32u * pitch;
    }
    const char* asrc = xnF + ((size_t)rb * 16 + 2 * wid) * 1024;
    const unsigned avoff = lane * 16;
    const unsigned adst = lds0 + wid * 2048, bdst = lds0 + 65536 + wid * 1024;
    const unsigned hdst = lds0 + 65536 + 16384 + wr * 1024 + wc * 512;
#define G1_DMA(SL) do { \
        dma_s2(avoff, asrc, adst + (SL) * 16384); asrc += 131072; \
        dma_v(bsrc[0], bdst + (SL) * 20480); bsrc[0] += binc[0]; \
        dma_v(bsrc[1], bdst + (SL) * 20480 + 8192); bsrc[1] += binc[1]; \
        asm volatile("s_mov_b32 m0, %1\n\ts_mov_b32 exec_hi, 0\n\tglobal_load_lds_dwordx4 %0, off\n\ts_mov_b32 exec_hi, -1" ::"v"(bsrc[2]), "s"(hdst + (SL) * 20480) : "memory"); \
        bsrc[2] += binc[2]; } while (0)
    G1_DMA(0); G1_DMA(1); G1_DMA(2);
    const lds_cptr pa = (lds_cptr)smem + wr * 4096 + lane * 16;
    lds_cptr pb[4], pe0, pe1;
    {
        const int g = lane >> 4, q = (lane >> 2) & 3, p = lane & 3, f = (q >> 1) | ((g & 1) << 1);
        const lds_cptr b0 = (lds_cptr)smem + 65536 + g * 5120 + q * 128 + p * 8;
#pragma unroll
        for (int c = 0; c < 4; ++c) pb[c] = b0 + ((c ^ f) << 5) + wc * 3072;
        pe0 = b0 + ((wc ^ f) << 5) + 2048; pe1 = b0 + (((2 + wc) ^ f) << 5) + 2048;
    }
    f32x4 acc[4][10];
#pragma unroll
    for (int m = 0; m < 4; ++m)
#pragma unroll
        for (int n = 0; n < 10; ++n) { acc[m][n] = (f32x4){0.f, 0.f, 0.f, 0.f}; PIN(acc[m][n]); }
    asm volatile("s_waitcnt vmcnt(10)\n\ts_barrier" ::: "memory");

    if (wc == 1) asm volatile("s_barrier" ::: "memory");
    bf16x8 af[4], bf[10];
#define BFR(P, O) cat8(tr_read((P) + (O)), tr_read((P) + (O) + 512))
#define G1_READS(SL) do { \
        _Pragma("unroll") for (int c = 0; c < 4; ++c) { bf[c] = BFR(pb[c], (SL) * 20480); bf[4 + c] = BFR(pb[c], (SL) * 20480 + 1024); } \
        bf[8] = BFR(pe0, (SL) * 20480); bf[9] = BFR(pe1, (SL) * 20480); \
        _Pragma("unroll") for (int m = 0; m < 4; ++m) af[m] = *(const LAS bf16x8*)(pa + (SL) * 16384 + m * 1024); } while (0)
#define G1_STEP(SL, DMA, VM, ENDBAR) do { \
        G1_READS(SL); DMA; \
        asm volatile("s_waitcnt vmcnt(" #VM ") lgkmcnt(0)" ::: "memory"); \
        asm volatile("s_barrier" ::: "memory"); \
        SCHED(); __builtin_amdgcn_s_setprio(1); \
        _Pragma("unroll") for (int i = 0; i < 40; ++i) { const int m_ = ((i >> 2) & 1) ? 3 - (i & 3) : (i & 3); MF16_ACC(acc[m_][i >> 2], bf[i >> 2], af[m_]); } \
        __builtin_amdgcn_s_setprio(0); SCHED(); \
        if (ENDBAR) asm volatile("s_barrier" ::: "memory"); } while (0)
    const char* pf = (wid < 7 ? w2 + (size_t)(bx * 7 + wid) * 4096 : wo + (size_t)bx * 8192);
    for (int it = 0; it < 7; ++it) { G1_STEP(0, G1_DMA(3), 10, true); G1_STEP(1, G1_DMA(0), 10, true); G1_STEP(2, G1_DMA(1), 10, true); G1_STEP(3, G1_DMA(2), 10, true); }
    G1_STEP(0, G1_DMA(3), 10, true);
    G1_STEP(1, { dma_s2(avoff, pf, lds0 + 147456); dma_s2(avoff, pf + 2048, lds0 + 147456); }, 9, true);
    G1_STEP(2, {}, 4, true);
    G1_STEP(3, { if (wc == 0) dma_s1(avoff, wo + (size_t)bx * 8192 + 4096 + wid * 1024, lds0 + 147456); }, 63, wc == 0);

#undef G1_STEP
#undef G1_READS
#undef BFR
#undef G1_DMA
    asm volatile("s_nop 7" : "+v"(acc[0][9]), "+v"(acc[1][9]), "+v"(acc[2][9]), "+v"(acc[3][9]), "+v"(acc[3][8]), "+v"(acc[2][8]));
    SCHED();
    int tz = threadIdx.x; asm volatile("" : "+v"(tz));
    const int fr = tz & 15, fq = (tz >> 4) & 3;
    const int tb = (rb * 16 + wr * 4) * 1024;
    const bool ffn_main = !tq || wc == 1;
    const int cm = ((fq & 1) << 1) | (fq >> 1), ce = 2 * wc + (fq >> 1);
    {
        const int cb0 = 2 * qbx;
        const float sc = cb0 < 32 ? QSCALE : 1.0f;
        char* dst = qkv + (size_t)(cb0 + (fq & 1)) * 131072 + tb + ce * 256 + ((fr ^ (4 * ce)) * 16);
#pragma unroll
        for (int m = 0; m < 4; ++m) {
            const f32x4 va = acc[m][8] * sc, vb = acc[m][9] * sc;
            unsigned a0 = cvtpk(va[0], va[1]), a1 = cvtpk(va[2], va[3]), b0 = cvtpk(vb[0], vb[1]), b1 = cvtpk(vb[2], vb[3]);
            swap16(a0, b0); swap16(a1, b1);
            ST16(dst + m * 1024, ((u32x4){a0, a1, b0, b1}));
        }
    }
    if (ffn_main) {
        char* dst = act + (size_t)(wc ? pB : pA) * 262144 + tb + cm * 256 + fr * 16;
#pragma unroll
        for (int m = 0; m < 4; ++m)
#pragma unroll
            for (int sub = 0; sub < 2; ++sub) {
                const u32x2 a = silu_mul4(acc[m][2 * sub], acc[m][4 + 2 * sub]), b = silu_mul4(acc[m][2 * sub + 1], acc[m][4 + 2 * sub + 1]);
                unsigned a0 = a[0], a1 = a[1], b0 = b[0], b1 = b[1];
                swap16(a0, b0); swap16(a1, b1);
                ST16(dst + sub * 131072 + m * 1024, ((u32x4){a0, a1, b0, b1}));
            }
    } else {
        const int lo = tb + cm * 256 + ((fr ^ (4 * cm)) * 16);
#pragma unroll
        for (int sub = 0; sub < 4; ++sub) {
            const int cb = 2 * qb0 + sub;
            const float sc = cb < 32 ? QSCALE : 1.0f;
            char* dst = qkv + (size_t)cb * 131072 + lo;
#pragma unroll
            for (int m = 0; m < 4; ++m) {
                const f32x4 va = acc[m][2 * sub] * sc, vb = acc[m][2 * sub + 1] * sc;
                unsigned a0 = cvtpk(va[0], va[1]), a1 = cvtpk(va[2], va[3]), b0 = cvtpk(vb[0], vb[1]), b1 = cvtpk(vb[2], vb[3]);
                swap16(a0, b0); swap16(a1, b1);
                ST16(dst + m * 1024, ((u32x4){a0, a1, b0, b1}));
            }
        }
    }

}

__device__ __forceinline__ void attn_role(char* smem, const int bx, const char* __restrict__ qkv, char* __restrict__ act, unsigned* __restrict__ ctr) {

    const int tid = threadIdx.x, lane = tid & 63;
    const int wid = __builtin_amdgcn_readfirstlane(tid >> 6);
    const int head = (bx & 7) * 4 + (bx >> 6), qb = (bx >> 3) & 7;
    __builtin_amdgcn_s_setprio(2);
    const char* Qf = qkv + (size_t)head * 131072;
    const char* Kf = Qf + 4 * MB;
    const char* Vr = Qf + 8 * MB;
    const unsigned lds0 = (unsigned)(size_t)(LAS char*)smem;
    const unsigned voff = lane * 16;
    const int tq0 = qb * 8 + wid * 2;
    unsigned fo[2];
#pragma unroll
    for (int s = 0; s < 2; ++s) { const int c = 2 * s + (lane >> 5); fo[s] = ((lane >> 4) & 1) * 1024 + c * 256 + (((lane & 15) ^ (4 * c)) * 16); }
    bf16x8 q[2][2];
    asm volatile("global_load_dwordx4 %0, %4, %6\n\tglobal_load_dwordx4 %1, %5, %6\n\tglobal_load_dwordx4 %2, %4, %6 offset:2048\n\tglobal_load_dwordx4 %3, %5, %6 offset:2048"
                 : "=&v"(q[0][0]), "=&v"(q[0][1]), "=&v"(q[1][0]), "=&v"(q[1][1]) : "v"(fo[0]), "v"(fo[1]), "s"(Qf + tq0 * 2048) : "memory");
    bf16x8 sel;
    { const short one = (((lane >> 2) & 1) == ((lane >> 4) & 1)) ? (short)0x3f80 : (short)0; sel = (bf16x8){one, one, one, one, one, one, one, one}; }
    const lds_cptr kp0 = (lds_cptr)smem + fo[0], kp1 = (lds_cptr)smem + fo[1];
    lds_cptr vp0, vp1;
    { const int h = lane >> 5, c = 2 * ((lane >> 4) & 1) + ((lane >> 1) & 1), q4 = (lane >> 2) & 3;
      vp0 = (lds_cptr)smem + 8192 + c * 256 + (h ^ c) * 64 + q4 * 16 + (lane & 1) * 8; vp1 = (lds_cptr)smem + 8192 + c * 256 + ((2 + h) ^ c) * 64 + q4 * 16 + (lane & 1) * 8; }
    const char* Kw = Kf + wid * 2048;
    const char* Vw = Vr + wid * 2048;
    volatile LAS unsigned* flag = (volatile LAS unsigned*)(smem);
    const unsigned ooff = ((lane >> 4) & 1) * 1024 + (lane >> 5) * 256 + (lane & 15) * 16;
#define AT_DMA(ST, SLOT) do { const unsigned d_ = lds0 + (SLOT) + wid * 2048; \
        dma_s2(voff, Kf + (ST) * 8192 + wid * 2048, d_); dma_s2(voff, Vr + (ST) * 8192 + wid * 2048, d_ + 8192); } while (0)
    for (int attempt = 0; attempt < 2; ++attempt) {
        f32x16 o[2]; f32x4 l[2];
        bf16x8 k0, k1;
        asm volatile("global_load_dwordx4 %0, %2, %4\n\tglobal_load_dwordx4 %1, %3, %4" : "=&v"(k0), "=&v"(k1) : "v"(fo[0]), "v"(fo[1]), "s"(Kf) : "memory");
        AT_DMA(0, 0); AT_DMA(1, 16384); AT_DMA(2, 32768);
        o[0] = (f32x16){}; o[1] = (f32x16){}; l[0] = (f32x4){}; l[1] = (f32x4){};
        PIN(o[0]); PIN(o[1]); PIN(l[0]); PIN(l[1]);
        asm volatile("s_waitcnt vmcnt(12)" : "+v"(k0), "+v"(k1), "+v"(q[0][0]), "+v"(q[0][1]), "+v"(q[1][0]), "+v"(q[1][1])::"memory");
        f32x16 negm[2];
        {
            float m0 = -INFINITY, m1 = -INFINITY;
            const int nt = attempt == 0 ? 1 : 64;
            for (int kt = 0; kt < nt; ++kt) {
                if (kt) { k0 = *(const bf16x8*)(Kf + kt * 2048 + fo[0]); k1 = *(const bf16x8*)(Kf + kt * 2048 + fo[1]); }
                f32x16 s0 = MF32(k0, q[0][0], (f32x16){}), s1 = MF32(k0, q[1][0], (f32x16){});
                s0 = MF32(k1, q[0][1], s0); s1 = MF32(k1, q[1][1], s1);
#pragma unroll
                for (int r = 0; r < 16; ++r) { m0 = fmaxf(m0, s0[r]); m1 = fmaxf(m1, s1[r]); }
            }
            m0 = fmaxf(m0, __shfl_xor(m0, 32)); m1 = fmaxf(m1, __shfl_xor(m1, 32));
#pragma unroll
            for (int r = 0; r < 16; ++r) { negm[0][r] = -m0; negm[1][r] = -m1; }
            PIN(negm[0]); PIN(negm[1]);
        }
        asm volatile("s_waitcnt vmcnt(0)\n\ts_barrier" ::: "memory");
        bf16x8 kf0 = *(const LAS bf16x8*)(kp0), kf1 = *(const LAS bf16x8*)(kp1);
        bf16x8 vf0 = (bf16x8){}, vf1 = (bf16x8){};
        u32x4 pa0 = (u32x4){}, pa1 = (u32x4){}, pb0 = (u32x4){}, pb1 = (u32x4){};
        f32x16 sA = MF32(kf0, q[0][0], negm[0]); sA = MF32(kf1, q[0][1], sA);
        f32x16 sB;
#define EX(X, i) X[i] = __builtin_amdgcn_exp2f(X[i])
#define PK(X, i) cvtpk(X[i], X[(i) + 1])
#define BC8(x) __builtin_bit_cast(bf16x8, x)
#define AT_UNIT(SC, PC0, PC1, SN, QN, NM, PP0, PP1, OP, LP, K0, K1, V0, V1, L0, L1, L2, L3) do { unsigned c0, c1, c2, c3, c4, c5, c6, c7; \
            SN = MF32(K0, QN[0], NM); SCHED(); L0; SCHED(); EX(SC, 0); EX(SC, 1); EX(SC, 2); SCHED(); \
            OP = MF32(V0, BC8(PP0), OP); SCHED(); L1; SCHED(); c0 = PK(SC, 0); EX(SC, 3); EX(SC, 4); EX(SC, 5); SCHED(); \
            SN = MF32(K1, QN[1], SN); SCHED(); L2; SCHED(); c1 = PK(SC, 2); c2 = PK(SC, 4); EX(SC, 6); EX(SC, 7); EX(SC, 8); SCHED(); \
            OP = MF32(V1, BC8(PP1), OP); SCHED(); L3; SCHED(); c3 = PK(SC, 6); EX(SC, 9); EX(SC, 10); EX(SC, 11); SCHED(); \
            MF16_ACC(LP, sel, PP0); SCHED(); c4 = PK(SC, 8); c5 = PK(SC, 10); EX(SC, 12); EX(SC, 13); SCHED(); \
            MF16_ACC(LP, sel, PP1); SCHED(); EX(SC, 14); EX(SC, 15); SCHED(); c6 = PK(SC, 12); SCHED(); c7 = PK(SC, 14); \
            PC0 = (u32x4){c0, c1, c2, c3}; PC1 = (u32x4){c4, c5, c6, c7}; SCHED(); } while (0)
#define AT_M0(SLOT, V) asm volatile("s_mov_b32 m0, %0" ::"s"(lds0 + (SLOT) + wid * 2048 + (V) * 8192) : "memory")
#define AT_PAIR(ST, V) asm volatile("global_load_lds_dwordx4 %0, %1\n\tglobal_load_lds_dwordx4 %0, %1 offset:1024" ::"v"(voff), "s"(((V) ? Vw : Kw) + ((ST) & 15) * 8192) : "memory")
#define AT_STAGE(SO, SN_, DM, ST, SLA, SLB) do { _Pragma("unroll") for (int kt = 0; kt < 4; ++kt) { \
                const int ko = kt < 3 ? (SO) + (kt + 1) * 2048 : (SN_), vo = (SO) + kt * 2048; \
                bf16x8 kn0, kn1, vn0, vn1; \
                AT_UNIT(sA, pa0, pa1, sB, q[1], negm[1], pb0, pb1, o[1], l[1], kf0, kf1, vf0, vf1, \
                        (kn0 = *(const LAS bf16x8*)(kp0 + ko), kn1 = *(const LAS bf16x8*)(kp1 + ko)), vn0 = cat8(tr_read(vp0 + vo), tr_read(vp1 + vo)), \
                        vn1 = cat8(tr_read(vp0 + vo + 1024), tr_read(vp1 + vo + 1024)), {}); \
                kf0 = kn0; kf1 = kn1; vf0 = vn0; vf1 = vn1; \
                __builtin_amdgcn_s_waitcnt(0xC07F); SCHED(); \
                AT_UNIT(sB, pb0, pb1, sA, q[0], negm[0], pa0, pa1, o[0], l[0], kf0, kf1, vf0, vf1, \
                        if (DM) AT_M0((kt >> 1) ? (SLB) : (SLA), kt & 1), if (DM) AT_PAIR((ST) + (kt >> 1), kt & 1), {}, {}); } } while (0)
#define NXT(x) ((x) == 65536 ? 0 : (x) + 16384)
        int s0 = 0;
        for (int k = 0; k < 8; ++k) {
            const int s1 = NXT(s0), s2 = NXT(s1), s3 = NXT(s2), s4 = NXT(s3);
            AT_STAGE(s0, s1, true, 2 * k + 3, s3, s4);
            AT_STAGE(s1, s2, false, 0, 0, 0);
            s0 = s2;
            asm volatile("s_waitcnt vmcnt(0)\n\ts_barrier" ::: "memory");
        }
#undef NXT
#undef AT_STAGE
#undef AT_M0
#undef AT_PAIR
        o[1] = MF32(vf0, BC8(pb0), o[1]); o[1] = MF32(vf1, BC8(pb1), o[1]);
        l[1] = MF16(sel, BC8(pb0), l[1]); l[1] = MF16(sel, BC8(pb1), l[1]);
#undef AT_UNIT
        asm volatile("s_nop 7\n\ts_nop 7" : "+v"(l[0]), "+v"(l[1]), "+v"(o[0]), "+v"(o[1]));
        float tsum = 0.f;
#pragma unroll
        for (int i = 0; i < 2; ++i) {
            const float r = 1.0f / l[i][0];
#pragma unroll
            for (int e = 0; e < 16; ++e) { tsum += fabsf(o[i][e]); o[i][e] *= r; }
        }
        const bool bad = !(l[0][0] < 1.0995e12f) || !(l[1][0] < 1.0995e12f) || !(tsum < INFINITY);
        if (lane == 0) flag[wid] = 0u;
        if (bad) flag[wid] = 1u;
#pragma unroll
        for (int i = 0; i < 2; ++i)
#pragma unroll
            for (int s = 0; s < 2; ++s) {
                unsigned a0 = cvtpk(o[i][8 * s + 0], o[i][8 * s + 1]), a1 = cvtpk(o[i][8 * s + 2], o[i][8 * s + 3]);
                unsigned b0 = cvtpk(o[i][8 * s + 4], o[i][8 * s + 5]), b1 = cvtpk(o[i][8 * s + 6], o[i][8 * s + 7]);
                swap32(a0, b0); swap32(a1, b1);
                const u32x4 w = (u32x4){a0, a1, b0, b1};
                asm volatile("global_store_dwordx4 %0, %1, %2 sc1\n\ts_nop 1" ::"v"(ooff), "v"(w), "s"(act + (size_t)(112 + head) * 131072 + (tq0 + i) * 2048 + s * 512) : "memory");
            }
        asm volatile("s_waitcnt vmcnt(0) lgkmcnt(0)\n\ts_barrier" ::: "memory");
        const unsigned redo = flag[0] | flag[1] | flag[2] | flag[3];
        if (attempt == 1 || !__builtin_amdgcn_readfirstlane(redo)) break;
        asm volatile("s_waitcnt lgkmcnt(0)\n\ts_barrier" ::: "memory");
    }
#undef AT_DMA
    if (tid == 0) __hip_atomic_fetch_add(ctr + qb * 64, 1u, __ATOMIC_RELAXED, __HIP_MEMORY_SCOPE_AGENT);

}

__device__ __forceinline__ void gemm2_role(char* smem, const int bx, const char* __restrict__ act, const char* __restrict__ w2, const char* __restrict__ wo,
                                           const char* __restrict__ x, char* __restrict__ out, unsigned* __restrict__ ctr) {

    const int tid = threadIdx.x, lane = tid & 63;
    const int wid = __builtin_amdgcn_readfirstlane(tid >> 6);
    const int xc = bx & 7, ii = bx >> 3, tm = (xc >> 1) * 4 + (ii >> 3), tn = (xc & 1) * 8 + (ii & 7);
    const unsigned lds0 = (unsigned)(size_t)(LAS char*)smem + wid * 16384;
    __builtin_amdgcn_s_sleep(16);
    const char* asrc = act + (size_t)wid * 131072 + tm * 8192;
    unsigned av[4];
#pragma unroll
    for (int i = 0; i < 4; ++i) av[i] = (i ^ wid) * 2048 + lane * 16;
    const char* wsrc = w2 + (size_t)wid * 32 * 2048 + tn * 128;
    const unsigned wv0 = (lane >> 3) * 2048 + ((((lane >> 1) & 3) ^ ((lane >> 4) & 1)) * 32) + (lane & 1) * 16;
    const unsigned wv1 = (lane >> 3) * 2048 + ((((lane >> 1) & 3) ^ ((lane >> 4) & 1) ^ 2) * 32) + (lane & 1) * 16 + 16384 - 1024;
    lds_cptr wp[4];
    {
        const int g = lane >> 4, q = (lane >> 2) & 3, p = lane & 3, f = (q >> 1) | ((g & 1) << 1);
#pragma unroll
        for (int c = 0; c < 4; ++c) wp[c] = (lds_cptr)smem + wid * 16384 + g * 1024 + q * 128 + ((c ^ f) << 5) + p * 8;
    }
    f32x4 acc[8][4];
#pragma unroll
    for (int i = 0; i < 8; ++i)
#pragma unroll
        for (int n = 0; n < 4; ++n) { acc[i][n] = (f32x4){}; PIN(acc[i][n]); }
    bf16x8 a0[4], a1[4], a2[4], a3[4], wfa[4], wfb[4];
#define G2_LDA(A, H) do { \
        asm volatile("global_load_dwordx4 %0, %4, %6\n\tglobal_load_dwordx4 %1, %4, %6 offset:1024\n\tglobal_load_dwordx4 %2, %5, %6\n\tglobal_load_dwordx4 %3, %5, %6 offset:1024" \
                     : "=&v"(A[0]), "=&v"(A[1]), "=&v"(A[2]), "=&v"(A[3]) : "v"(av[2 * (H)]), "v"(av[2 * (H) + 1]), "s"(asrc) : "memory"); \
        if (H) asrc += 524288; } while (0)
#define G2_LDW(SL) do {     \
        asm volatile("s_mov_b32 m0, %3\n\ts_nop 0\n\tglobal_load_lds_dwordx4 %0, %2\n\tglobal_load_lds_dwordx4 %1, %2 offset:1024" \
                     ::"v"(wv0), "v"(wv1), "s"(wsrc), "s"(lds0 + (SL) * 2048) : "memory"); \
        wsrc += ((SL) & 1) ? 262144 - 32768 : 32768; } while (0)
#define G2_WAIT(N, A) asm volatile("s_waitcnt vmcnt(" #N ")" : "+v"(A[0]), "+v"(A[1]), "+v"(A[2]), "+v"(A[3])::"memory")
#define WFR(P, O) cat8(tr_read((P) + (O)), tr_read((P) + (O) + 512))
#define G2_STEP(N, AC, AN, WC, WN, SL, RD, IA, IW) do { \
        if ((N) >= 0) G2_WAIT(N, AN); \
        if (RD) { WN[2 * ((SL) & 1)] = WFR(wp[2 * ((SL) & 1)], ((((SL) >> 1) + 1) & 3) * 4096); WN[2 * ((SL) & 1) + 1] = WFR(wp[2 * ((SL) & 1) + 1], ((((SL) >> 1) + 1) & 3) * 4096); } \
        SCHED(); \
        _Pragma("unroll") for (int i = 0; i < 16; ++i) MF16_ACC(acc[4 * ((SL) & 1) + (i >> 2)][((i >> 2) & 1) ? 3 - (i & 3) : (i & 3)], WC[((i >> 2) & 1) ? 3 - (i & 3) : (i & 3)], AC[i >> 2]); \
        SCHED(); if (IA) G2_LDA(AC, (SL) & 1); if (IW) G2_LDW(SL); } while (0)
#define G2_STEP4(N0, N1, N2, N3, SL, RDL, IA, IW) do { \
        G2_STEP(N0, a0, a1, wfa, wfb, (SL), true, IA, IW); G2_STEP(N1, a1, a2, wfa, wfb, (SL) + 1, true, IA, IW); \
        G2_STEP(N2, a2, a3, wfb, wfa, (SL) + 2, RDL, IA, IW); G2_STEP(N3, a3, a0, wfb, wfa, (SL) + 3, RDL, IA, IW); } while (0)
    G2_LDW(0); G2_LDW(1); G2_LDW(2); G2_LDW(3);
    G2_LDA(a0, 0); G2_LDW(4); G2_LDA(a1, 1); G2_LDW(5); G2_LDA(a2, 0); G2_LDW(6); G2_LDA(a3, 1); G2_LDW(7);
    G2_WAIT(20, a0);
#pragma unroll
    for (int c = 0; c < 4; ++c) wfa[c] = WFR(wp[c], 0);
    for (int it = 0; it < 6; ++it) { G2_STEP4(14, 14, 14, 14, 0, true, true, true); G2_STEP4(14, 14, 14, 14, 4, true, true, true); }
    G2_STEP4(14, 12, 10, 8, 0, true, true, false);
    G2_STEP4(8, 4, 0, -1, 4, false, false, false);
#undef G2_STEP4
#undef G2_STEP
#undef G2_WAIT
#undef G2_LDW
#undef G2_LDA
    const int fr = lane & 15, fq = lane >> 4;
    const size_t orow = (size_t)(tm * 128 + wid * 32 + fr) * 2048 + tn * 128 + (fq & 1) * 32 + (fq >> 1) * 16;
    u32x4 xr[4];
#pragma unroll
    for (int c = 0; c < 4; ++c) xr[c] = *(const u32x4*)(x + orow + (c >> 1) * 32768 + (c & 1) * 64);
    asm volatile("s_nop 7" : "+v"(acc[7][0]), "+v"(acc[7][1]), "+v"(acc[7][2]), "+v"(acc[7][3]));
    SCHED();
    char* mine = smem + wid * 16384 + lane * 16;
#pragma unroll
    for (int i = 0; i < 16; ++i) *(f32x4*)(mine + i * 1024) = acc[4 + (i >> 2)][i & 3];
    __syncthreads();
#pragma unroll
    for (int i = 0; i < 16; ++i) acc[i >> 2][i & 3] += *(const f32x4*)(smem + (wid ^ 2) * 16384 + lane * 16 + i * 1024);
    __syncthreads();
#pragma unroll
    for (int i = 0; i < 8; ++i) *(f32x4*)(mine + i * 1024) = acc[2 + (i >> 2)][i & 3];
    __syncthreads();
#pragma unroll
    for (int i = 0; i < 8; ++i) { acc[i >> 2][i & 3] += *(const f32x4*)(smem + (wid ^ 1) * 16384 + lane * 16 + i * 1024); PIN(acc[i >> 2][i & 3]); }
    __syncthreads();
    const unsigned ldsb = (unsigned)(size_t)(LAS char*)smem;
    const char* wos = wo + tn * 128;
    const char* osrc = act + (size_t)112 * 131072 + tm * 8192 + wid * 2048;
    unsigned ov = lane * 16;
    lds_cptr wq[4];
#pragma unroll
    for (int c = 0; c < 4; ++c) { wq[c] = wp[c] - wid * 16384; PIN(wq[c]); }
    PIN(ov);
#define G3_LDW(HS, SLOT) asm volatile("s_mov_b32 m0, %3\n\ts_nop 0\n\tglobal_load_lds_dwordx4 %0, %2\n\tglobal_load_lds_dwordx4 %1, %2 offset:1024" \
                     ::"v"(wv0), "v"(wv1), "s"(wos + (size_t)(HS) * 32768), "s"(ldsb + (SLOT) * 2048) : "memory")
#pragma unroll
    for (int i = 0; i < 10; ++i) G3_LDW(wid * 10 + i, wid * 10 + i);
    if (wid == 0) {
        const unsigned long long t0 = __builtin_amdgcn_s_memrealtime();
        while ((unsigned)__builtin_amdgcn_readfirstlane(__hip_atomic_load(ctr + (tm >> 1) * 64, __ATOMIC_RELAXED, __HIP_MEMORY_SCOPE_AGENT)) < 32u) {
            if (__builtin_amdgcn_s_memrealtime() - t0 > 5000000ull) break;
            __builtin_amdgcn_s_sleep(2);
        }
        __builtin_amdgcn_fence(__ATOMIC_ACQUIRE, "wavefront");
        asm volatile("" ::: "memory");
    }
    asm volatile("s_waitcnt vmcnt(0)\n\ts_barrier" ::: "memory");

    bf16x8 oa[8][4];
#define G3_LDA(G) asm volatile("global_load_dwordx4 %0, %4, %5 sc1\n\tglobal_load_dwordx4 %1, %4, %5 offset:1024 sc1\n\tglobal_load_dwordx4 %2, %4, %6 sc1\n\tglobal_load_dwordx4 %3, %4, %6 offset:1024 sc1" \
                     : "=&v"(oa[(G) & 7][0]), "=&v"(oa[(G) & 7][1]), "=&v"(oa[(G) & 7][2]), "=&v"(oa[(G) & 7][3]) \
                     : "v"(ov), "s"(osrc + (size_t)(G) * 262144), "s"(osrc + (size_t)(G) * 262144 + 131072) : "memory")
#define G3_WAIT(N, G) asm volatile("s_waitcnt vmcnt(" #N ")" : "+v"(oa[(G) & 7][0]), "+v"(oa[(G) & 7][1]), "+v"(oa[(G) & 7][2]), "+v"(oa[(G) & 7][3])::"memory")
    s16x4 wl[2][8], wh[2][8];
#define G3_WA(G, i) (wq[(i) & 3] + (((G) >= 8 && (G) < 10) ? 65536 : 0) + ((((G) * 2 + ((i) >> 2)) % 20) * 4096 - (((G) >= 8 && (G) < 10) ? 65536 : 0)))
#define G3_LD1(G, j) asm volatile("global_load_dwordx4 %0, %1, %2 offset:%c3 sc1" : "=&v"(oa[(G) & 7][j]) : "v"(ov), "s"(osrc + (size_t)(G) * 262144 + ((j) >> 1) * 131072), "i"(((j) & 1) * 1024) : "memory")
#define G3_GROUP(N, G, RD) do { G3_WAIT(N, G); SCHED(); \
        const bool rf_ = (G) == 5 || (G) == 6; \
        _Pragma("unroll") for (int i = 0; i < 8; ++i) { const bf16x8 w_ = cat8(wl[(G) & 1][i], wh[(G) & 1][i]); \
            MF16_ACC(acc[0][i & 3], w_, oa[(G) & 7][2 * (i >> 2)]); SCHED(); if (RD) wl[((G) + 1) & 1][i] = tr_read(G3_WA((G) + 1, i)); SCHED(); \
            MF16_ACC(acc[1][i & 3], w_, oa[(G) & 7][2 * (i >> 2) + 1]); SCHED(); if (RD) wh[((G) + 1) & 1][i] = tr_read(G3_WA((G) + 1, i) + 512); \
            if (rf_ && !(i & 1) && i < 6) G3_LDW(40 + wid * 6 + ((G) - 5) * 3 + (i >> 1), wid * 6 + ((G) - 5) * 3 + (i >> 1)); \
            if ((G) < 11 && (i & 1)) G3_LD1((G) + 5, i >> 1); SCHED(); } } while (0)
    G3_LDA(0); G3_LDA(1); G3_LDA(2); G3_LDA(3); G3_LDA(4);
#pragma unroll
    for (int i = 0; i < 8; ++i) { wl[0][i] = tr_read(G3_WA(0, i)); wh[0][i] = tr_read(G3_WA(0, i) + 512); }
    G3_GROUP(16, 0, true); G3_GROUP(16, 1, true); G3_GROUP(16, 2, true); G3_GROUP(16, 3, true); G3_GROUP(16, 4, true);

    asm volatile("s_waitcnt lgkmcnt(0)\n\ts_barrier" ::: "memory");
    G3_GROUP(16, 5, true); G3_GROUP(22, 6, true); G3_GROUP(28, 7, true); G3_GROUP(28, 8, true);

    asm volatile("s_waitcnt vmcnt(10)\n\ts_barrier" ::: "memory");

    G3_GROUP(10, 9, true); G3_GROUP(22, 10, true); G3_GROUP(16, 11, true); G3_GROUP(12, 12, true); G3_GROUP(8, 13, true); G3_GROUP(4, 14, true); G3_GROUP(0, 15, false);

#undef G3_GROUP
#undef G3_WA
#undef G3_WAIT
#undef G3_LDA
#undef G3_LD1
#undef G3_LDW
#undef WFR
    asm volatile("s_nop 7" : "+v"(acc[0][0]), "+v"(acc[0][1]), "+v"(acc[0][2]), "+v"(acc[0][3]), "+v"(acc[1][0]), "+v"(acc[1][1]), "+v"(acc[1][2]), "+v"(acc[1][3]));
#pragma unroll
    for (int c = 0; c < 4; ++c) {
        float v[8];
#pragma unroll
        for (int e = 0; e < 4; ++e) {
            unsigned a = __float_as_uint(acc[c >> 1][2 * (c & 1)][e]), b = __float_as_uint(acc[c >> 1][2 * (c & 1) + 1][e]);
            swap16(a, b);
            v[e] = __uint_as_float(a); v[4 + e] = __uint_as_float(b);
        }
        u32x4 w;
#pragma unroll
        for (int e = 0; e < 4; ++e) w[e] = cvtpk(v[2 * e] + bf_lo(xr[c][e]), v[2 * e + 1] + bf_hi(xr[c][e]));
        *(u32x4*)(out + orow + (c >> 1) * 32768 + (c & 1) * 64) = w;
    }

}

__global__ __launch_bounds__(256, 2) void k_fused(const char* __restrict__ qkv, char* __restrict__ act, const char* __restrict__ w2, const char* __restrict__ wo,
                                                  const char* __restrict__ x, char* __restrict__ out, unsigned* __restrict__ ctr) {
    __shared__ __attribute__((aligned(1024))) char smem[81920];
    asm volatile("" ::"s"(qkv), "s"(act), "s"(w2));
    if (__builtin_expect(blockIdx.x < 256, 1)) attn_role(smem, blockIdx.x, qkv, act, ctr);
    else gemm2_role(smem, blockIdx.x - 256, act, w2, wo, x, out, ctr);
}

extern "C" void kernel_launch(void* const* d_in, const int* in_sizes, int n_in, void* d_out, int out_size, void* d_ws, size_t ws_size,
                              hipStream_t stream) {
    const u16* x = (const u16*)d_in[0];
    const char* wqkv = (const char*)d_in[1];
    const char* wo = (const char*)d_in[2];
    const char* w1 = (const char*)d_in[4];
    const char* w3 = (const char*)d_in[5];
    const char* w2 = (const char*)d_in[6];
    char* ws = (char*)d_ws;
    k_ln<<<512, 512, 0, stream>>>(x, ws + WS_XN, (unsigned*)(ws + WS_CTR), wqkv, w1, w3);
    k_gemm1<<<256, 512, 0, stream>>>(ws + WS_XN, wqkv, w1, w3, ws + WS_QKV, ws + WS_ACT, w2, wo);
    k_fused<<<512, 256, 0, stream>>>(ws + WS_QKV, ws + WS_ACT, w2, wo, (const char*)x, (char*)d_out, (unsigned*)(ws + WS_CTR));
}
```

```cpp
#include <hip/hip_runtime.h>
#include <stdint.h>

typedef unsigned short u16;
typedef short bf16x8 __attribute__((ext_vector_type(8)));
typedef short s16x4 __attribute__((ext_vector_type(4)));
typedef float f32x2 __attribute__((ext_vector_type(2)));
typedef float f32x4 __attribute__((ext_vector_type(4)));
typedef float f32x16 __attribute__((ext_vector_type(16)));
typedef unsigned u32x2 __attribute__((ext_vector_type(2)));
typedef unsigned u32x4 __attribute__((ext_vector_type(4)));
typedef __bf16 bf16x2_t __attribute__((ext_vector_type(2)));
#define LAS __attribute__((address_space(3)))
typedef const LAS char* lds_cptr;

constexpr int SEQ = 2048, DM = 1024, FF = 3584, NH = 32;
constexpr size_t MB = 1024 * 1024;
constexpr size_t WS_CTR = 0;
constexpr size_t WS_XN = 65536;
constexpr size_t WS_QKV = WS_XN + 4 * MB;
constexpr size_t WS_ACT = WS_QKV + 12 * MB;
constexpr float QSCALE = 0.17677669529663687f * 1.4426950408889634f;

#define SCHED() __builtin_amdgcn_sched_barrier(0)
#define PIN(x) asm volatile("" : "+v"(x))
#define MF32(a, b, c) __builtin_amdgcn_mfma_f32_32x32x16_bf16(a, b, c, 0, 0, 0)
#define MF16(a, b, c) __builtin_amdgcn_mfma_f32_16x16x32_bf16(a, b, c, 0, 0, 0)

#define MF16_ACC(acc, a, b) asm volatile("v_mfma_f32_16x16x32_bf16 %0, %1, %2, %0" : "+v"(acc) : "v"(a), "v"(b))

__device__ __forceinline__ unsigned cvtpk(float lo, float hi) {
    f32x2 v = {lo, hi};
    bf16x2_t b = __builtin_convertvector(v, bf16x2_t);
    return __builtin_bit_cast(unsigned, b);
}
__device__ __forceinline__ float bf_lo(unsigned w) { return __uint_as_float(w << 16); }
__device__ __forceinline__ float bf_hi(unsigned w) { return __uint_as_float(w & 0xffff0000u); }
__device__ __forceinline__ float rbf(float x) { return bf_lo(cvtpk(x, x)); }
__device__ __forceinline__ s16x4 tr_read(lds_cptr p) {
    return __builtin_bit_cast(s16x4, __builtin_amdgcn_ds_read_tr16_b64_v4i16((LAS s16x4*)p));
}
__device__ __forceinline__ bf16x8 cat8(s16x4 lo, s16x4 hi) { return __builtin_shufflevector(lo, hi, 0, 1, 2, 3, 4, 5, 6, 7); }
__device__ __forceinline__ void dma_v(const void* g, unsigned lds) {
    asm volatile("s_mov_b32 m0, %1\n\ts_nop 0\n\tglobal_load_lds_dwordx4 %0, off" ::"v"(g), "s"(lds) : "memory");
}
__device__ __forceinline__ void dma_s1(unsigned voff, const void* sbase, unsigned lds) {
    asm volatile("s_mov_b32 m0, %2\n\ts_nop 0\n\tglobal_load_lds_dwordx4 %0, %1" ::"v"(voff), "s"(sbase), "s"(lds) : "memory");
}
__device__ __forceinline__ void dma_s2(unsigned voff, const void* sbase, unsigned lds) {
    asm volatile("s_mov_b32 m0, %2\n\ts_nop 0\n\tglobal_load_lds_dwordx4 %0, %1\n\tglobal_load_lds_dwordx4 %0, %1 offset:1024" ::"v"(voff), "s"(sbase), "s"(lds) : "memory");
}
__device__ __forceinline__ void swap16(unsigned& a, unsigned& b) {
    auto r = __builtin_amdgcn_permlane16_swap(a, b, false, false);
    a = r[0]; b = r[1];
}
__device__ __forceinline__ void swap32(unsigned& a, unsigned& b) {
    auto r = __builtin_amdgcn_permlane32_swap(a, b, false, false);
    a = r[0]; b = r[1];
}

__device__ __forceinline__ float wave_sum(float v) {
    v += __builtin_bit_cast(float, __builtin_amdgcn_update_dpp(0, __builtin_bit_cast(int, v), 0xB1, 0xf, 0xf, true));
    v += __builtin_bit_cast(float, __builtin_amdgcn_update_dpp(0, __builtin_bit_cast(int, v), 0x4E, 0xf, 0xf, true));
    v += __builtin_bit_cast(float, __builtin_amdgcn_update_dpp(0, __builtin_bit_cast(int, v), 0x141, 0xf, 0xf, true));
    v += __builtin_bit_cast(float, __builtin_amdgcn_update_dpp(0, __builtin_bit_cast(int, v), 0x140, 0xf, 0xf, true));
    auto a = __builtin_amdgcn_permlane16_swap(__float_as_uint(v), __float_as_uint(v), false, false);
    v = __uint_as_float(a[0]) + __uint_as_float(a[1]);
    auto b = __builtin_amdgcn_permlane32_swap(__float_as_uint(v), __float_as_uint(v), false, false);
    return __uint_as_float(b[0]) + __uint_as_float(b[1]);
}
__global__ __launch_bounds__(512) void k_ln(const u16* __restrict__ x, char* __restrict__ xnF, unsigned* __restrict__ ctr,
                                            const char* __restrict__ wqkv, const char* __restrict__ w1, const char* __restrict__ w3) {
    __shared__ __attribute__((aligned(16))) char sm[16384];
    const int tid = threadIdx.x, lane = tid & 63, wid = tid >> 6;
    if (blockIdx.x >= 256) {
        const size_t o = (size_t)((blockIdx.x - 256) * 512 + tid) * 16;
        const u32x4 p0 = *(const u32x4*)(wqkv + o), p1 = *(const u32x4*)(w1 + o), p2 = *(const u32x4*)(w3 + o);
        asm volatile("" ::"v"(p0), "v"(p1), "v"(p2));
        return;
    }
    if (blockIdx.x == 0) { ctr[tid] = 0u; ctr[tid + 512] = 0u; }
    const int row = blockIdx.x * 8 + wid;
    const u32x4* src = (const u32x4*)(x + (size_t)row * DM);
    const u32x4 a = src[lane], b = src[64 + lane];
    float v[16];
#pragma unroll
    for (int i = 0; i < 4; ++i) { v[2 * i] = bf_lo(a[i]); v[2 * i + 1] = bf_hi(a[i]); v[8 + 2 * i] = bf_lo(b[i]); v[8 + 2 * i + 1] = bf_hi(b[i]); }
    float s = 0.f;
#pragma unroll
    for (int i = 0; i < 16; ++i) s += v[i];
    s = wave_sum(s);
    const float mean = s * (1.0f / DM);
    float q = 0.f;
#pragma unroll
    for (int i = 0; i < 16; ++i) { const float d = v[i] - mean; q += d * d; }
    q = wave_sum(q);
    const float mb = rbf(mean), vb = rbf(q * (1.0f / DM));
    const float rs = 1.0f / rbf(sqrtf(rbf(vb + 1e-5f)));
    u32x4 oa, ob;
#pragma unroll
    for (int i = 0; i < 4; ++i) {
        oa[i] = cvtpk(rbf(v[2 * i] - mb) * rs, rbf(v[2 * i + 1] - mb) * rs);
        ob[i] = cvtpk(rbf(v[8 + 2 * i] - mb) * rs, rbf(v[8 + 2 * i + 1] - mb) * rs);
    }
    *(u32x4*)(sm + lane * 128 + wid * 16) = oa;
    *(u32x4*)(sm + (64 + lane) * 128 + wid * 16) = ob;
    __syncthreads();
    const int row0 = blockIdx.x * 8;
#pragma unroll
    for (int i = 0; i < 2; ++i) {
        const int p = tid + i * 512, chunk = p >> 3, r = p & 7;
        const u32x4 w = *(const u32x4*)(sm + p * 16);
        *(u32x4*)(xnF + ((size_t)(chunk >> 2) * 128 + (row0 >> 4)) * 1024 + (chunk & 3) * 256 + ((row0 & 15) + r) * 16) = w;
    }
}

__device__ __forceinline__ unsigned silu_mul2(f32x2 g, f32x2 u) {
    const f32x2 t = g * -1.4426950408889634f;
    f32x2 e; e.x = __builtin_amdgcn_exp2f(t.x); e.y = __builtin_amdgcn_exp2f(t.y);
    e = e + 1.0f;
    f32x2 r; r.x = __builtin_amdgcn_rcpf(e.x); r.y = __builtin_amdgcn_rcpf(e.y);
    const f32x2 h = (g * u) * r;
    return cvtpk(h.x, h.y);
}
__device__ __forceinline__ u32x2 silu_mul4(f32x4 g, f32x4 u) {
    return (u32x2){silu_mul2((f32x2){g[0], g[1]}, (f32x2){u[0], u[1]}), silu_mul2((f32x2){g[2], g[3]}, (f32x2){u[2], u[3]})};
}
#define ST16(p, v) asm volatile("global_store_dwordx4 %0, %1, off sc1\n\ts_nop 1" ::"v"(p), "v"(v) : "memory")
__global__ __launch_bounds__(512, 2) void k_gemm1(const char* __restrict__ xnF, const char* __restrict__ wqkv, const char* __restrict__ w1,
                                                  const char* __restrict__ w3, char* __restrict__ qkv, char* __restrict__ act,
                                                  const char* __restrict__ w2, const char* __restrict__ wo) {
    __shared__ __attribute__((aligned(1024))) char smem[147456 + 2048];

    const int tid = threadIdx.x, lane = tid & 63;
    const int wid = __builtin_amdgcn_readfirstlane(tid >> 6), wr = wid & 3, wc = wid >> 2;
    const int bx = blockIdx.x, j = (bx & 7) + 8 * ((bx >> 3) & 3), rb = bx >> 5;
    const unsigned lds0 = (unsigned)(size_t)(LAS char*)smem;
    const bool tq = j < 8;
    const int qb0 = 3 * j, qbx = tq ? 3 * j + 2 : 16 + j, pA = 2 * j - 8, pB = tq ? j : 2 * j - 7;
    const char* bsrc[5]; unsigned binc[5];
#pragma unroll
    for (int i = 0; i < 5; ++i) {
        const int kb = wr, blk = i;
        const int r = lane >> 3, j8 = lane & 7, f = ((r >> 1) & 1) | ((kb & 1) << 1), cp = (j8 >> 1) ^ f;
        const bool ffn = blk >= 3 || (blk < 2 && !tq);
        const int pitch = ffn ? 7168 : 6144;
        const uint64_t wg = (blk == 0 || blk == 3) ? (uint64_t)w1 : (uint64_t)w3, wb = ffn ? wg : (uint64_t)wqkv;
        const int cf = blk < 2 ? pA : pB, cq = blk == 2 ? qbx : qb0 + blk, cblk = ffn ? cf : cq;
        bsrc[i] = (const char*)wb + (size_t)(unsigned)(cblk * 128);
        binc[i] = 32u * pitch;
    }
    unsigned bvq, bvf;
    { const int r = lane >> 3, j8 = lane & 7, cp = (j8 >> 1) ^ (((r >> 1) & 1) | ((wr & 1) << 1));
      bvq = (wr * 8 + r) * 6144 + cp * 32 + (j8 & 1) * 16; bvf = (wr * 8 + r) * 7168 + cp * 32 + (j8 & 1) * 16; }
    const unsigned bv01 = tq ? bvq : bvf;
    const char* asrc = xnF + ((size_t)rb * 16 + 4 * wr) * 1024;
    const unsigned avoff = lane * 16;
    const unsigned adst = lds0 + wr * 4096, bdst = lds0 + 65536 + wr * 5120;
#define G1_DMA_B(SL) do { \
        dma_s1(bv01, bsrc[0], bdst + (SL) * 20480); dma_s1(bv01, bsrc[1], bdst + (SL) * 20480 + 1024); dma_s1(bvq, bsrc[2], bdst + (SL) * 20480 + 2048); \
        dma_s1(bvf, bsrc[3], bdst + (SL) * 20480 + 3072); dma_s1(bvf, bsrc[4], bdst + (SL) * 20480 + 4096); \
        _Pragma("unroll") for (int i = 0; i < 5; ++i) bsrc[i] += binc[i]; } while (0)
#define G1_DMA_A(SL) do { dma_s2(avoff, asrc, adst + (SL) * 16384); dma_s2(avoff, asrc + 2048, adst + (SL) * 16384 + 2048); asrc += 131072; } while (0)
#define G1_DMA(SL, VA) do { if (wc == 0) G1_DMA_B(SL); else { G1_DMA_A(SL); asm volatile("s_waitcnt vmcnt(" #VA ")" ::: "memory"); } } while (0)
    if (wc == 0) { G1_DMA_B(0); G1_DMA_B(1); G1_DMA_B(2); } else { G1_DMA_A(0); G1_DMA_A(1); G1_DMA_A(2); }
    const lds_cptr pa = (lds_cptr)smem + wr * 4096 + lane * 16;
    lds_cptr pb[4], pe0, pe1;
    {
        const int g = lane >> 4, q = (lane >> 2) & 3, p = lane & 3, f = (q >> 1) | ((g & 1) << 1);
        const lds_cptr b0 = (lds_cptr)smem + 65536 + g * 5120 + q * 128 + p * 8;
#pragma unroll
        for (int c = 0; c < 4; ++c) pb[c] = b0 + ((c ^ f) << 5) + wc * 3072;
        pe0 = b0 + ((wc ^ f) << 5) + 2048; pe1 = b0 + (((2 + wc) ^ f) << 5) + 2048;
    }
    f32x4 acc[4][10];
#pragma unroll
    for (int m = 0; m < 4; ++m)
#pragma unroll
        for (int n = 0; n < 10; ++n) { acc[m][n] = (f32x4){0.f, 0.f, 0.f, 0.f}; PIN(acc[m][n]); }
    asm volatile("s_waitcnt vmcnt(8)\n\ts_barrier" ::: "memory");

    if (wc == 1) asm volatile("s_barrier" ::: "memory");
    bf16x8 af[4], bf[10];
#define BFR(P, O) cat8(tr_read((P) + (O)), tr_read((P) + (O) + 512))
#define G1_READS(SL) do { \
        _Pragma("unroll") for (int c = 0; c < 4; ++c) { bf[c] = BFR(pb[c], (SL) * 20480); bf[4 + c] = BFR(pb[c], (SL) * 20480 + 1024); } \
        bf[8] = BFR(pe0, (SL) * 20480); bf[9] = BFR(pe1, (SL) * 20480); \
        _Pragma("unroll") for (int m = 0; m < 4; ++m) af[m] = *(const LAS bf16x8*)(pa + (SL) * 16384 + m * 1024); } while (0)
#define G1_STEP(SL, DMA, VB, ENDBAR) do { \
        G1_READS(SL); DMA; \
        asm volatile("s_waitcnt lgkmcnt(0)" ::: "memory"); \
        asm volatile("s_barrier" ::: "memory"); \
        SCHED(); __builtin_amdgcn_s_setprio(1); \
        _Pragma("unroll") for (int i = 0; i < 40; ++i) { const int m_ = ((i >> 2) & 1) ? 3 - (i & 3) : (i & 3); MF16_ACC(acc[m_][i >> 2], bf[i >> 2], af[m_]); } \
        __builtin_amdgcn_s_setprio(0); SCHED(); \
        if (ENDBAR) asm volatile("s_waitcnt vmcnt(" #VB ")\n\ts_barrier" ::: "memory"); } while (0)
    const char* pf = (wid < 7 ? w2 + (size_t)(bx * 7 + wid) * 4096 : wo + (size_t)bx * 8192);
    for (int it = 0; it < 7; ++it) { G1_STEP(0, G1_DMA(3, 8), 10, true); G1_STEP(1, G1_DMA(0, 8), 10, true); G1_STEP(2, G1_DMA(1, 8), 10, true); G1_STEP(3, G1_DMA(2, 8), 10, true); }
    G1_STEP(0, G1_DMA(3, 8), 10, true);
    G1_STEP(1, { dma_s2(avoff, pf, lds0 + 147456); dma_s2(avoff, pf + 2048, lds0 + 147456); if (wc == 1) asm volatile("s_waitcnt vmcnt(8)" ::: "memory"); }, 9, true);
    G1_STEP(2, { if (wc == 1) asm volatile("s_waitcnt vmcnt(4)" ::: "memory"); }, 4, true);
    G1_STEP(3, { if (wc == 0) dma_s1(avoff, wo + (size_t)bx * 8192 + 4096 + wid * 1024, lds0 + 147456); }, 63, wc == 0);

#undef G1_STEP
#undef G1_READS
#undef BFR
#undef G1_DMA
#undef G1_DMA_A
#undef G1_DMA_B
    asm volatile("s_nop 7" : "+v"(acc[0][9]), "+v"(acc[1][9]), "+v"(acc[2][9]), "+v"(acc[3][9]), "+v"(acc[3][8]), "+v"(acc[2][8]));
    SCHED();
    int tz = threadIdx.x; asm volatile("" : "+v"(tz));
    const int fr = tz & 15, fq = (tz >> 4) & 3;
    const int tb = (rb * 16 + wr * 4) * 1024;
    const bool ffn_main = !tq || wc == 1;
    const int cm = ((fq & 1) << 1) | (fq >> 1), ce = 2 * wc + (fq >> 1);
    {
        const int cb0 = 2 * qbx;
        const float sc = cb0 < 32 ? QSCALE : 1.0f;
        char* dst = qkv + (size_t)(cb0 + (fq & 1)) * 131072 + tb + ce * 256 + ((fr ^ (4 * ce)) * 16);
#pragma unroll
        for (int m = 0; m < 4; ++m) {
            const f32x4 va = acc[m][8] * sc, vb = acc[m][9] * sc;
            unsigned a0 = cvtpk(va[0], va[1]), a1 = cvtpk(va[2], va[3]), b0 = cvtpk(vb[0], vb[1]), b1 = cvtpk(vb[2], vb[3]);
            swap16(a0, b0); swap16(a1, b1);
            ST16(dst + m * 1024, ((u32x4){a0, a1, b0, b1}));
        }
    }
    if (ffn_main) {
        char* dst = act + (size_t)(wc ? pB : pA) * 262144 + tb + cm * 256 + fr * 16;
#pragma unroll
        for (int m = 0; m < 4; ++m)
#pragma unroll
            for (int sub = 0; sub < 2; ++sub) {
                const u32x2 a = silu_mul4(acc[m][2 * sub], acc[m][4 + 2 * sub]), b = silu_mul4(acc[m][2 * sub + 1], acc[m][4 + 2 * sub + 1]);
                unsigned a0 = a[0], a1 = a[1], b0 = b[0], b1 = b[1];
                swap16(a0, b0); swap16(a1, b1);
                ST16(dst + sub * 131072 + m * 1024, ((u32x4){a0, a1, b0, b1}));
            }
    } else {
        const int lo = tb + cm * 256 + ((fr ^ (4 * cm)) * 16);
#pragma unroll
        for (int sub = 0; sub < 4; ++sub) {
            const int cb = 2 * qb0 + sub;
            const float sc = cb < 32 ? QSCALE : 1.0f;
            char* dst = qkv + (size_t)cb * 131072 + lo;
#pragma unroll
            for (int m = 0; m < 4; ++m) {
                const f32x4 va = acc[m][2 * sub] * sc, vb = acc[m][2 * sub + 1] * sc;
                unsigned a0 = cvtpk(va[0], va[1]), a1 = cvtpk(va[2], va[3]), b0 = cvtpk(vb[0], vb[1]), b1 = cvtpk(vb[2], vb[3]);
                swap16(a0, b0); swap16(a1, b1);
                ST16(dst + m * 1024, ((u32x4){a0, a1, b0, b1}));
            }
        }
    }

}

__device__ __forceinline__ void attn_role(char* smem, const int bx, const char* __restrict__ qkv, char* __restrict__ act, unsigned* __restrict__ ctr) {

    const int tid = threadIdx.x, lane = tid & 63;
    const int wid = __builtin_amdgcn_readfirstlane(tid >> 6);
    const int head = (bx & 7) * 4 + (bx >> 6), qb = (bx >> 3) & 7;
    __builtin_amdgcn_s_setprio(2);
    const char* Qf = qkv + (size_t)head * 131072;
    const char* Kf = Qf + 4 * MB;
    const char* Vr = Qf + 8 * MB;
    const unsigned lds0 = (unsigned)(size_t)(LAS char*)smem;
    const unsigned voff = lane * 16;
    const int tq0 = qb * 8 + wid * 2;
    unsigned fo[2];
#pragma unroll
    for (int s = 0; s < 2; ++s) { const int c = 2 * s + (lane >> 5); fo[s] = ((lane >> 4) & 1) * 1024 + c * 256 + (((lane & 15) ^ (4 * c)) * 16); }
    bf16x8 q[2][2];
    asm volatile("global_load_dwordx4 %0, %4, %6\n\tglobal_load_dwordx4 %1, %5, %6\n\tglobal_load_dwordx4 %2, %4, %6 offset:2048\n\tglobal_load_dwordx4 %3, %5, %6 offset:2048"
                 : "=&v"(q[0][0]), "=&v"(q[0][1]), "=&v"(q[1][0]), "=&v"(q[1][1]) : "v"(fo[0]), "v"(fo[1]), "s"(Qf + tq0 * 2048) : "memory");
    bf16x8 sel;
    { const short one = (((lane >> 2) & 1) == ((lane >> 4) & 1)) ? (short)0x3f80 : (short)0; sel = (bf16x8){one, one, one, one, one, one, one, one}; }
    const lds_cptr kp0 = (lds_cptr)smem + fo[0], kp1 = (lds_cptr)smem + fo[1];
    lds_cptr vp0, vp1;
    { const int h = lane >> 5, c = 2 * ((lane >> 4) & 1) + ((lane >> 1) & 1), q4 = (lane >> 2) & 3;
      vp0 = (lds_cptr)smem + 8192 + c * 256 + (h ^ c) * 64 + q4 * 16 + (lane & 1) * 8; vp1 = (lds_cptr)smem + 8192 + c * 256 + ((2 + h) ^ c) * 64 + q4 * 16 + (lane & 1) * 8; }
    const char* Kw = Kf + wid * 2048;
    const char* Vw = Vr + wid * 2048;
    volatile LAS unsigned* flag = (volatile LAS unsigned*)(smem);
    const unsigned ooff = ((lane >> 4) & 1) * 1024 + (lane >> 5) * 256 + (lane & 15) * 16;
#define AT_DMA(ST, SLOT) do { const unsigned d_ = lds0 + (SLOT) + wid * 2048; \
        dma_s2(voff, Kf + (ST) * 8192 + wid * 2048, d_); dma_s2(voff, Vr + (ST) * 8192 + wid * 2048, d_ + 8192); } while (0)
    for (int attempt = 0; attempt < 2; ++attempt) {
        f32x16 o[2]; f32x4 l[2];
        bf16x8 k0, k1;
        asm volatile("global_load_dwordx4 %0, %2, %4\n\tglobal_load_dwordx4 %1, %3, %4" : "=&v"(k0), "=&v"(k1) : "v"(fo[0]), "v"(fo[1]), "s"(Kf) : "memory");
        AT_DMA(0, 0); AT_DMA(1, 16384); AT_DMA(2, 32768);
        o[0] = (f32x16){}; o[1] = (f32x16){}; l[0] = (f32x4){}; l[1] = (f32x4){};
        PIN(o[0]); PIN(o[1]); PIN(l[0]); PIN(l[1]);
        asm volatile("s_waitcnt vmcnt(12)" : "+v"(k0), "+v"(k1), "+v"(q[0][0]), "+v"(q[0][1]), "+v"(q[1][0]), "+v"(q[1][1])::"memory");
        f32x16 negm[2];
        {
            float m0 = -INFINITY, m1 = -INFINITY;
            const int nt = attempt == 0 ? 1 : 64;
            for (int kt = 0; kt < nt; ++kt) {
                if (kt) { k0 = *(const bf16x8*)(Kf + kt * 2048 + fo[0]); k1 = *(const bf16x8*)(Kf + kt * 2048 + fo[1]); }
                f32x16 s0 = MF32(k0, q[0][0], (f32x16){}), s1 = MF32(k0, q[1][0], (f32x16){});
                s0 = MF32(k1, q[0][1], s0); s1 = MF32(k1, q[1][1], s1);
#pragma unroll
                for (int r = 0; r < 16; ++r) { m0 = fmaxf(m0, s0[r]); m1 = fmaxf(m1, s1[r]); }
            }
            m0 = fmaxf(m0, __shfl_xor(m0, 32)); m1 = fmaxf(m1, __shfl_xor(m1, 32));
#pragma unroll
            for (int r = 0; r < 16; ++r) { negm[0][r] = -m0; negm[1][r] = -m1; }
            PIN(negm[0]); PIN(negm[1]);
        }
        asm volatile("s_waitcnt vmcnt(0)\n\ts_barrier" ::: "memory");
        bf16x8 kf0 = *(const LAS bf16x8*)(kp0), kf1 = *(const LAS bf16x8*)(kp1);
        bf16x8 vf0 = (bf16x8){}, vf1 = (bf16x8){};
        u32x4 pa0 = (u32x4){}, pa1 = (u32x4){}, pb0 = (u32x4){}, pb1 = (u32x4){};
        f32x16 sA = MF32(kf0, q[0][0], negm[0]); sA = MF32(kf1, q[0][1], sA);
        f32x16 sB;
#define EX(X, i) X[i] = __builtin_amdgcn_exp2f(X[i])
#define PK(X, i) cvtpk(X[i], X[(i) + 1])
#define BC8(x) __builtin_bit_cast(bf16x8, x)
#define AT_UNIT(SC, PC0, PC1, SN, QN, NM, PP0, PP1, OP, LP, K0, K1, V0, V1, L0, L1, L2, L3) do { unsigned c0, c1, c2, c3, c4, c5, c6, c7; \
            SN = MF32(K0, QN[0], NM); SCHED(); L0; SCHED(); EX(SC, 0); EX(SC, 1); EX(SC, 2); SCHED(); \
            OP = MF32(V0, BC8(PP0), OP); SCHED(); L1; SCHED(); c0 = PK(SC, 0); EX(SC, 3); EX(SC, 4); EX(SC, 5); SCHED(); \
            SN = MF32(K1, QN[1], SN); SCHED(); L2; SCHED(); c1 = PK(SC, 2); c2 = PK(SC, 4); EX(SC, 6); EX(SC, 7); EX(SC, 8); SCHED(); \
            OP = MF32(V1, BC8(PP1), OP); SCHED(); L3; SCHED(); c3 = PK(SC, 6); EX(SC, 9); EX(SC, 10); EX(SC, 11); SCHED(); \
            MF16_ACC(LP, sel, PP0); SCHED(); c4 = PK(SC, 8); c5 = PK(SC, 10); EX(SC, 12); EX(SC, 13); SCHED(); \
            MF16_ACC(LP, sel, PP1); SCHED(); EX(SC, 14); EX(SC, 15); SCHED(); c6 = PK(SC, 12); SCHED(); c7 = PK(SC, 14); \
            PC0 = (u32x4){c0, c1, c2, c3}; PC1 = (u32x4){c4, c5, c6, c7}; SCHED(); } while (0)
#define AT_M0(SLOT, V) asm volatile("s_mov_b32 m0, %0" ::"s"(lds0 + (SLOT) + wid * 2048 + (V) * 8192) : "memory")
#define AT_PAIR(ST, V) asm volatile("global_load_lds_dwordx4 %0, %1\n\tglobal_load_lds_dwordx4 %0, %1 offset:1024" ::"v"(voff), "s"(((V) ? Vw : Kw) + ((ST) & 15) * 8192) : "memory")
#define AT_STAGE(SO, SN_, DM, ST, SLA, SLB) do { _Pragma("unroll") for (int kt = 0; kt < 4; ++kt) { \
                const int ko = kt < 3 ? (SO) + (kt + 1) * 2048 : (SN_), vo = (SO) + kt * 2048; \
                bf16x8 kn0, kn1, vn0, vn1; \
                AT_UNIT(sA, pa0, pa1, sB, q[1], negm[1], pb0, pb1, o[1], l[1], kf0, kf1, vf0, vf1, \
                        (kn0 = *(const LAS bf16x8*)(kp0 + ko), kn1 = *(const LAS bf16x8*)(kp1 + ko)), vn0 = cat8(tr_read(vp0 + vo), tr_read(vp1 + vo)), \
                        vn1 = cat8(tr_read(vp0 + vo + 1024), tr_read(vp1 + vo + 1024)), {}); \
                kf0 = kn0; kf1 = kn1; vf0 = vn0; vf1 = vn1; \
                __builtin_amdgcn_s_waitcnt(0xC07F); SCHED(); \
                AT_UNIT(sB, pb0, pb1, sA, q[0], negm[0], pa0, pa1, o[0], l[0], kf0, kf1, vf0, vf1, \
                        if (DM) AT_M0((kt >> 1) ? (SLB) : (SLA), kt & 1), if (DM) AT_PAIR((ST) + (kt >> 1), kt & 1), {}, {}); } } while (0)
#define NXT(x) ((x) == 65536 ? 0 : (x) + 16384)
        int s0 = 0;
        for (int k = 0; k < 8; ++k) {
            const int s1 = NXT(s0), s2 = NXT(s1), s3 = NXT(s2), s4 = NXT(s3);
            AT_STAGE(s0, s1, true, 2 * k + 3, s3, s4);
            AT_STAGE(s1, s2, false, 0, 0, 0);
            s0 = s2;
            asm volatile("s_waitcnt vmcnt(0)\n\ts_barrier" ::: "memory");
        }
#undef NXT
#undef AT_STAGE
#undef AT_M0
#undef AT_PAIR
        o[1] = MF32(vf0, BC8(pb0), o[1]); o[1] = MF32(vf1, BC8(pb1), o[1]);
        l[1] = MF16(sel, BC8(pb0), l[1]); l[1] = MF16(sel, BC8(pb1), l[1]);
#undef AT_UNIT
        asm volatile("s_nop 7\n\ts_nop 7" : "+v"(l[0]), "+v"(l[1]), "+v"(o[0]), "+v"(o[1]));
        float tsum = 0.f;
#pragma unroll
        for (int i = 0; i < 2; ++i) {
            const float r = 1.0f / l[i][0];
#pragma unroll
            for (int e = 0; e < 16; ++e) { tsum += fabsf(o[i][e]); o[i][e] *= r; }
        }
        const bool bad = !(l[0][0] < 1.0995e12f) || !(l[1][0] < 1.0995e12f) || !(tsum < INFINITY);
        if (lane == 0) flag[wid] = 0u;
        if (bad) flag[wid] = 1u;
#pragma unroll
        for (int i = 0; i < 2; ++i)
#pragma unroll
            for (int s = 0; s < 2; ++s) {
                unsigned a0 = cvtpk(o[i][8 * s + 0], o[i][8 * s + 1]), a1 = cvtpk(o[i][8 * s + 2], o[i][8 * s + 3]);
                unsigned b0 = cvtpk(o[i][8 * s + 4], o[i][8 * s + 5]), b1 = cvtpk(o[i][8 * s + 6], o[i][8 * s + 7]);
                swap32(a0, b0); swap32(a1, b1);
                const u32x4 w = (u32x4){a0, a1, b0, b1};
                asm volatile("global_store_dwordx4 %0, %1, %2 sc1\n\ts_nop 1" ::"v"(ooff), "v"(w), "s"(act + (size_t)(112 + head) * 131072 + (tq0 + i) * 2048 + s * 512) : "memory");
            }
        asm volatile("s_waitcnt vmcnt(0) lgkmcnt(0)\n\ts_barrier" ::: "memory");
        const unsigned redo = flag[0] | flag[1] | flag[2] | flag[3];
        if (attempt == 1 || !__builtin_amdgcn_readfirstlane(redo)) break;
        asm volatile("s_waitcnt lgkmcnt(0)\n\ts_barrier" ::: "memory");
    }
#undef AT_DMA
    if (tid == 0) __hip_atomic_fetch_add(ctr + qb * 64, 1u, __ATOMIC_RELAXED, __HIP_MEMORY_SCOPE_AGENT);

}

__device__ __forceinline__ void gemm2_role(char* smem, const int bx, const char* __restrict__ act, const char* __restrict__ w2, const char* __restrict__ wo,
                                           const char* __restrict__ x, char* __restrict__ out, unsigned* __restrict__ ctr) {

    const int tid = threadIdx.x, lane = tid & 63;
    const int wid = __builtin_amdgcn_readfirstlane(tid >> 6);
    const int xc = bx & 7, ii = bx >> 3, tm = (xc >> 1) * 4 + (ii >> 3), tn = (xc & 1) * 8 + (ii & 7);
    const unsigned lds0 = (unsigned)(size_t)(LAS char*)smem + wid * 16384;
    __builtin_amdgcn_s_sleep(16);
    const char* asrc = act + (size_t)wid * 131072 + tm * 8192;
    unsigned av[4];
#pragma unroll
    for (int i = 0; i < 4; ++i) av[i] = (i ^ wid) * 2048 + lane * 16;
    const char* wsrc = w2 + (size_t)wid * 32 * 2048 + tn * 128;
    const unsigned wv0 = (lane >> 3) * 2048 + ((((lane >> 1) & 3) ^ ((lane >> 4) & 1)) * 32) + (lane & 1) * 16;
    const unsigned wv1 = (lane >> 3) * 2048 + ((((lane >> 1) & 3) ^ ((lane >> 4) & 1) ^ 2) * 32) + (lane & 1) * 16 + 16384 - 1024;
    lds_cptr wp[4];
    {
        const int g = lane >> 4, q = (lane >> 2) & 3, p = lane & 3, f = (q >> 1) | ((g & 1) << 1);
#pragma unroll
        for (int c = 0; c < 4; ++c) wp[c] = (lds_cptr)smem + wid * 16384 + g * 1024 + q * 128 + ((c ^ f) << 5) + p * 8;
    }
    f32x4 acc[8][4];
#pragma unroll
    for (int i = 0; i < 8; ++i)
#pragma unroll
        for (int n = 0; n < 4; ++n) { acc[i][n] = (f32x4){}; PIN(acc[i][n]); }
    bf16x8 a0[4], a1[4], a2[4], a3[4], wfa[4], wfb[4];
#define G2_LDA(A, H) do { \
        asm volatile("global_load_dwordx4 %0, %4, %6\n\tglobal_load_dwordx4 %1, %4, %6 offset:1024\n\tglobal_load_dwordx4 %2, %5, %6\n\tglobal_load_dwordx4 %3, %5, %6 offset:1024" \
                     : "=&v"(A[0]), "=&v"(A[1]), "=&v"(A[2]), "=&v"(A[3]) : "v"(av[2 * (H)]), "v"(av[2 * (H) + 1]), "s"(asrc) : "memory"); \
        if (H) asrc += 524288; } while (0)
#define G2_LDW(SL) do {     \
        asm volatile("s_mov_b32 m0, %3\n\ts_nop 0\n\tglobal_load_lds_dwordx4 %0, %2\n\tglobal_load_lds_dwordx4 %1, %2 offset:1024" \
                     ::"v"(wv0), "v"(wv1), "s"(wsrc), "s"(lds0 + (SL) * 2048) : "memory"); \
        wsrc += ((SL) & 1) ? 262144 - 32768 : 32768; } while (0)
#define G2_WAIT(N, A) asm volatile("s_waitcnt vmcnt(" #N ")" : "+v"(A[0]), "+v"(A[1]), "+v"(A[2]), "+v"(A[3])::"memory")
#define WFR(P, O) cat8(tr_read((P) + (O)), tr_read((P) + (O) + 512))
#define G2_STEP(N, AC, AN, WC, WN, SL, RD, IA, IW) do { \
        if ((N) >= 0) G2_WAIT(N, AN); \
        if (RD) { WN[2 * ((SL) & 1)] = WFR(wp[2 * ((SL) & 1)], ((((SL) >> 1) + 1) & 3) * 4096); WN[2 * ((SL) & 1) + 1] = WFR(wp[2 * ((SL) & 1) + 1], ((((SL) >> 1) + 1) & 3) * 4096); } \
        SCHED(); \
        _Pragma("unroll") for (int i = 0; i < 16; ++i) MF16_ACC(acc[4 * ((SL) & 1) + (i >> 2)][((i >> 2) & 1) ? 3 - (i & 3) : (i & 3)], WC[((i >> 2) & 1) ? 3 - (i & 3) : (i & 3)], AC[i >> 2]); \
        SCHED(); if (IA) G2_LDA(AC, (SL) & 1); if (IW) G2_LDW(SL); } while (0)
#define G2_STEP4(N0, N1, N2, N3, SL, RDL, IA, IW) do { \
        G2_STEP(N0, a0, a1, wfa, wfb, (SL), true, IA, IW); G2_STEP(N1, a1, a2, wfa, wfb, (SL) + 1, true, IA, IW); \
        G2_STEP(N2, a2, a3, wfb, wfa, (SL) + 2, RDL, IA, IW); G2_STEP(N3, a3, a0, wfb, wfa, (SL) + 3, RDL, IA, IW); } while (0)
    G2_LDW(0); G2_LDW(1); G2_LDW(2); G2_LDW(3);
    G2_LDA(a0, 0); G2_LDW(4); G2_LDA(a1, 1); G2_LDW(5); G2_LDA(a2, 0); G2_LDW(6); G2_LDA(a3, 1); G2_LDW(7);
    G2_WAIT(20, a0);
#pragma unroll
    for (int c = 0; c < 4; ++c) wfa[c] = WFR(wp[c], 0);
    for (int it = 0; it < 6; ++it) { G2_STEP4(14, 14, 14, 14, 0, true, true, true); G2_STEP4(14, 14, 14, 14, 4, true, true, true); }
    G2_STEP4(14, 12, 10, 8, 0, true, true, false);
    G2_STEP4(8, 4, 0, -1, 4, false, false, false);
#undef G2_STEP4
#undef G2_STEP
#undef G2_WAIT
#undef G2_LDW
#undef G2_LDA
    const int fr = lane & 15, fq = lane >> 4;
    const size_t orow = (size_t)(tm * 128 + wid * 32 + fr) * 2048 + tn * 128 + (fq & 1) * 32 + (fq >> 1) * 16;
    u32x4 xr[4];
#pragma unroll
    for (int c = 0; c < 4; ++c) xr[c] = *(const u32x4*)(x + orow + (c >> 1) * 32768 + (c & 1) * 64);
    asm volatile("s_nop 7" : "+v"(acc[7][0]), "+v"(acc[7][1]), "+v"(acc[7][2]), "+v"(acc[7][3]));
    SCHED();
    char* mine = smem + wid * 16384 + lane * 16;
#pragma unroll
    for (int i = 0; i < 16; ++i) *(f32x4*)(mine + i * 1024) = acc[4 + (i >> 2)][i & 3];
    __syncthreads();
#pragma unroll
    for (int i = 0; i < 16; ++i) acc[i >> 2][i & 3] += *(const f32x4*)(smem + (wid ^ 2) * 16384 + lane * 16 + i * 1024);
    __syncthreads();
#pragma unroll
    for (int i = 0; i < 8; ++i) *(f32x4*)(mine + i * 1024) = acc[2 + (i >> 2)][i & 3];
    __syncthreads();
#pragma unroll
    for (int i = 0; i < 8; ++i) { acc[i >> 2][i & 3] += *(const f32x4*)(smem + (wid ^ 1) * 16384 + lane * 16 + i * 1024); PIN(acc[i >> 2][i & 3]); }
    __syncthreads();
    const unsigned ldsb = (unsigned)(size_t)(LAS char*)smem;
    const char* wos = wo + tn * 128;
    const char* osrc = act + (size_t)112 * 131072 + tm * 8192 + wid * 2048;
    unsigned ov = lane * 16;
    lds_cptr wq[4];
#pragma unroll
    for (int c = 0; c < 4; ++c) { wq[c] = wp[c] - wid * 16384; PIN(wq[c]); }
    PIN(ov);
#define G3_LDW(HS, SLOT) asm volatile("s_mov_b32 m0, %3\n\ts_nop 0\n\tglobal_load_lds_dwordx4 %0, %2\n\tglobal_load_lds_dwordx4 %1, %2 offset:1024" \
                     ::"v"(wv0), "v"(wv1), "s"(wos + (size_t)(HS) * 32768), "s"(ldsb + (SLOT) * 2048) : "memory")
#pragma unroll
    for (int i = 0; i < 10; ++i) G3_LDW(wid * 10 + i, wid * 10 + i);
    if (wid == 0) {
        const unsigned long long t0 = __builtin_amdgcn_s_memrealtime();
        while ((unsigned)__builtin_amdgcn_readfirstlane(__hip_atomic_load(ctr + (tm >> 1) * 64, __ATOMIC_RELAXED, __HIP_MEMORY_SCOPE_AGENT)) < 32u) {
            if (__builtin_amdgcn_s_memrealtime() - t0 > 5000000ull) break;
            __builtin_amdgcn_s_sleep(2);
        }
        __builtin_amdgcn_fence(__ATOMIC_ACQUIRE, "wavefront");
        asm volatile("" ::: "memory");
    }
    asm volatile("s_waitcnt vmcnt(0)\n\ts_barrier" ::: "memory");

    bf16x8 oa[8][4];
#define G3_LDA(G) asm volatile("global_load_dwordx4 %0, %4, %5 sc1\n\tglobal_load_dwordx4 %1, %4, %5 offset:1024 sc1\n\tglobal_load_dwordx4 %2, %4, %6 sc1\n\tglobal_load_dwordx4 %3, %4, %6 offset:1024 sc1" \
                     : "=&v"(oa[(G) & 7][0]), "=&v"(oa[(G) & 7][1]), "=&v"(oa[(G) & 7][2]), "=&v"(oa[(G) & 7][3]) \
                     : "v"(ov), "s"(osrc + (size_t)(G) * 262144), "s"(osrc + (size_t)(G) * 262144 + 131072) : "memory")
#define G3_WAIT(N, G) asm volatile("s_waitcnt vmcnt(" #N ")" : "+v"(oa[(G) & 7][0]), "+v"(oa[(G) & 7][1]), "+v"(oa[(G) & 7][2]), "+v"(oa[(G) & 7][3])::"memory")
    s16x4 wl[2][8], wh[2][8];
#define G3_WA(G, i) (wq[(i) & 3] + (((G) >= 8 && (G) < 10) ? 65536 : 0) + ((((G) * 2 + ((i) >> 2)) % 20) * 4096 - (((G) >= 8 && (G) < 10) ? 65536 : 0)))
#define G3_LD1(G, j) asm volatile("global_load_dwordx4 %0, %1, %2 offset:%c3 sc1" : "=&v"(oa[(G) & 7][j]) : "v"(ov), "s"(osrc + (size_t)(G) * 262144 + ((j) >> 1) * 131072), "i"(((j) & 1) * 1024) : "memory")
#define G3_GROUP(N, G, RD) do { G3_WAIT(N, G); SCHED(); \
        const bool rf_ = (G) == 5 || (G) == 6; \
        _Pragma("unroll") for (int i = 0; i < 8; ++i) { const bf16x8 w_ = cat8(wl[(G) & 1][i], wh[(G) & 1][i]); \
            MF16_ACC(acc[0][i & 3], w_, oa[(G) & 7][2 * (i >> 2)]); SCHED(); if (RD) wl[((G) + 1) & 1][i] = tr_read(G3_WA((G) + 1, i)); SCHED(); \
            MF16_ACC(acc[1][i & 3], w_, oa[(G) & 7][2 * (i >> 2) + 1]); SCHED(); if (RD) wh[((G) + 1) & 1][i] = tr_read(G3_WA((G) + 1, i) + 512); \
            if (rf_ && !(i & 1) && i < 6) G3_LDW(40 + wid * 6 + ((G) - 5) * 3 + (i >> 1), wid * 6 + ((G) - 5) * 3 + (i >> 1)); \
            if ((G) < 11 && (i & 1)) G3_LD1((G) + 5, i >> 1); SCHED(); } } while (0)
    G3_LDA(0); G3_LDA(1); G3_LDA(2); G3_LDA(3); G3_LDA(4);
#pragma unroll
    for (int i = 0; i < 8; ++i) { wl[0][i] = tr_read(G3_WA(0, i)); wh[0][i] = tr_read(G3_WA(0, i) + 512); }
    G3_GROUP(16, 0, true); G3_GROUP(16, 1, true); G3_GROUP(16, 2, true); G3_GROUP(16, 3, true); G3_GROUP(16, 4, true);

    asm volatile("s_waitcnt lgkmcnt(0)\n\ts_barrier" ::: "memory");
    G3_GROUP(16, 5, true); G3_GROUP(22, 6, true); G3_GROUP(28, 7, true); G3_GROUP(28, 8, true);

    asm volatile("s_waitcnt vmcnt(10)\n\ts_barrier" ::: "memory");

    G3_GROUP(10, 9, true); G3_GROUP(22, 10, true); G3_GROUP(16, 11, true); G3_GROUP(12, 12, true); G3_GROUP(8, 13, true); G3_GROUP(4, 14, true); G3_GROUP(0, 15, false);

#undef G3_GROUP
#undef G3_WA
#undef G3_WAIT
#undef G3_LDA
#undef G3_LD1
#undef G3_LDW
#undef WFR
    asm volatile("s_nop 7" : "+v"(acc[0][0]), "+v"(acc[0][1]), "+v"(acc[0][2]), "+v"(acc[0][3]), "+v"(acc[1][0]), "+v"(acc[1][1]), "+v"(acc[1][2]), "+v"(acc[1][3]));
#pragma unroll
    for (int c = 0; c < 4; ++c) {
        float v[8];
#pragma unroll
        for (int e = 0; e < 4; ++e) {
            unsigned a = __float_as_uint(acc[c >> 1][2 * (c & 1)][e]), b = __float_as_uint(acc[c >> 1][2 * (c & 1) + 1][e]);
            swap16(a, b);
            v[e] = __uint_as_float(a); v[4 + e] = __uint_as_float(b);
        }
        u32x4 w;
#pragma unroll
        for (int e = 0; e < 4; ++e) w[e] = cvtpk(v[2 * e] + bf_lo(xr[c][e]), v[2 * e + 1] + bf_hi(xr[c][e]));
        *(u32x4*)(out + orow + (c >> 1) * 32768 + (c & 1) * 64) = w;
    }

}

__global__ __launch_bounds__(256, 2) void k_fused(const char* __restrict__ qkv, char* __restrict__ act, const char* __restrict__ w2, const char* __restrict__ wo,
                                                  const char* __restrict__ x, char* __restrict__ out, unsigned* __restrict__ ctr) {
    __shared__ __attribute__((aligned(1024))) char smem[81920];
    asm volatile("" ::"s"(qkv), "s"(act), "s"(w2));
    if (__builtin_expect(blockIdx.x < 256, 1)) attn_role(smem, blockIdx.x, qkv, act, ctr);
    else gemm2_role(smem, blockIdx.x - 256, act, w2, wo, x, out, ctr);
}

extern "C" void kernel_launch(void* const* d_in, const int* in_sizes, int n_in, void* d_out, int out_size, void* d_ws, size_t ws_size,
                              hipStream_t stream) {
    const u16* x = (const u16*)d_in[0];
    const char* wqkv = (const char*)d_in[1];
    const char* wo = (const char*)d_in[2];
    const char* w1 = (const char*)d_in[4];
    const char* w3 = (const char*)d_in[5];
    const char* w2 = (const char*)d_in[6];
    char* ws = (char*)d_ws;
    k_ln<<<512, 512, 0, stream>>>(x, ws + WS_XN, (unsigned*)(ws + WS_CTR), wqkv, w1, w3);
    k_gemm1<<<256, 512, 0, stream>>>(ws + WS_XN, wqkv, w1, w3, ws + WS_QKV, ws + WS_ACT, w2, wo);
    k_fused<<<512, 256, 0, stream>>>(ws + WS_QKV, ws + WS_ACT, w2, wo, (const char*)x, (char*)d_out, (unsigned*)(ws + WS_CTR));
}
```

```cpp
#include <hip/hip_runtime.h>
#include <stdint.h>

typedef unsigned short u16;
typedef short bf16x8 __attribute__((ext_vector_type(8)));
typedef short s16x4 __attribute__((ext_vector_type(4)));
typedef float f32x2 __attribute__((ext_vector_type(2)));
typedef float f32x4 __attribute__((ext_vector_type(4)));
typedef float f32x16 __attribute__((ext_vector_type(16)));
typedef unsigned u32x2 __attribute__((ext_vector_type(2)));
typedef unsigned u32x4 __attribute__((ext_vector_type(4)));
typedef __bf16 bf16x2_t __attribute__((ext_vector_type(2)));
#define LAS __attribute__((address_space(3)))
typedef const LAS char* lds_cptr;

constexpr int SEQ = 2048, DM = 1024, FF = 3584, NH = 32;
constexpr size_t MB = 1024 * 1024;
constexpr size_t WS_CTR = 0;
constexpr size_t WS_XN = 65536;
constexpr size_t WS_QKV = WS_XN + 4 * MB;
constexpr size_t WS_ACT = WS_QKV + 12 * MB;
constexpr float QSCALE = 0.17677669529663687f * 1.4426950408889634f;

#define SCHED() __builtin_amdgcn_sched_barrier(0)
#define PIN(x) asm volatile("" : "+v"(x))
#define MF32(a, b, c) __builtin_amdgcn_mfma_f32_32x32x16_bf16(a, b, c, 0, 0, 0)
#define MF16(a, b, c) __builtin_amdgcn_mfma_f32_16x16x32_bf16(a, b, c, 0, 0, 0)

#define MF16_ACC(acc, a, b) asm volatile("v_mfma_f32_16x16x32_bf16 %0, %1, %2, %0" : "+v"(acc) : "v"(a), "v"(b))

__device__ __forceinline__ unsigned cvtpk(float lo, float hi) {
    f32x2 v = {lo, hi};
    bf16x2_t b = __builtin_convertvector(v, bf16x2_t);
    return __builtin_bit_cast(unsigned, b);
}
__device__ __forceinline__ float bf_lo(unsigned w) { return __uint_as_float(w << 16); }
__device__ __forceinline__ float bf_hi(unsigned w) { return __uint_as_float(w & 0xffff0000u); }
__device__ __forceinline__ float rbf(float x) { return bf_lo(cvtpk(x, x)); }
__device__ __forceinline__ s16x4 tr_read(lds_cptr p) {
    return __builtin_bit_cast(s16x4, __builtin_amdgcn_ds_read_tr16_b64_v4i16((LAS s16x4*)p));
}
__device__ __forceinline__ bf16x8 cat8(s16x4 lo, s16x4 hi) { return __builtin_shufflevector(lo, hi, 0, 1, 2, 3, 4, 5, 6, 7); }
__device__ __forceinline__ void dma_v(const void* g, unsigned lds) {
    asm volatile("s_mov_b32 m0, %1\n\ts_nop 0\n\tglobal_load_lds_dwordx4 %0, off" ::"v"(g), "s"(lds) : "memory");
}
__device__ __forceinline__ void dma_s1(unsigned voff, const void* sbase, unsigned lds) {
    asm volatile("s_mov_b32 m0, %2\n\ts_nop 0\n\tglobal_load_lds_dwordx4 %0, %1" ::"v"(voff), "s"(sbase), "s"(lds) : "memory");
}
__device__ __forceinline__ void dma_s2(unsigned voff, const void* sbase, unsigned lds) {
    asm volatile("s_mov_b32 m0, %2\n\ts_nop 0\n\tglobal_load_lds_dwordx4 %0, %1\n\tglobal_load_lds_dwordx4 %0, %1 offset:1024" ::"v"(voff), "s"(sbase), "s"(lds) : "memory");
}
__device__ __forceinline__ void swap16(unsigned& a, unsigned& b) {
    auto r = __builtin_amdgcn_permlane16_swap(a, b, false, false);
    a = r[0]; b = r[1];
}
__device__ __forceinline__ void swap32(unsigned& a, unsigned& b) {
    auto r = __builtin_amdgcn_permlane32_swap(a, b, false, false);
    a = r[0]; b = r[1];
}

__device__ __forceinline__ float wave_sum(float v) {
    v += __builtin_bit_cast(float, __builtin_amdgcn_update_dpp(0, __builtin_bit_cast(int, v), 0xB1, 0xf, 0xf, true));
    v += __builtin_bit_cast(float, __builtin_amdgcn_update_dpp(0, __builtin_bit_cast(int, v), 0x4E, 0xf, 0xf, true));
    v += __builtin_bit_cast(float, __builtin_amdgcn_update_dpp(0, __builtin_bit_cast(int, v), 0x141, 0xf, 0xf, true));
    v += __builtin_bit_cast(float, __builtin_amdgcn_update_dpp(0, __builtin_bit_cast(int, v), 0x140, 0xf, 0xf, true));
    auto a = __builtin_amdgcn_permlane16_swap(__float_as_uint(v), __float_as_uint(v), false, false);
    v = __uint_as_float(a[0]) + __uint_as_float(a[1]);
    auto b = __builtin_amdgcn_permlane32_swap(__float_as_uint(v), __float_as_uint(v), false, false);
    return __uint_as_float(b[0]) + __uint_as_float(b[1]);
}
__global__ __launch_bounds__(512) void k_ln(const u16* __restrict__ x, char* __restrict__ xnF, unsigned* __restrict__ ctr,
                                            const char* __restrict__ wqkv, const char* __restrict__ w1, const char* __restrict__ w3) {
    __shared__ __attribute__((aligned(16))) char sm[16384];
    const int tid = threadIdx.x, lane = tid & 63, wid = tid >> 6;
    if (blockIdx.x >= 256) {
        const size_t o = (size_t)((blockIdx.x - 256) * 512 + tid) * 16;
        const u32x4 p0 = *(const u32x4*)(wqkv + o), p1 = *(const u32x4*)(w1 + o), p2 = *(const u32x4*)(w3 + o);
        asm volatile("" ::"v"(p0), "v"(p1), "v"(p2));
        return;
    }
    if (blockIdx.x == 0) { ctr[tid] = 0u; ctr[tid + 512] = 0u; }
    const int row = blockIdx.x * 8 + wid;
    const u32x4* src = (const u32x4*)(x + (size_t)row * DM);
    const u32x4 a = src[lane], b = src[64 + lane];
    float v[16];
#pragma unroll
    for (int i = 0; i < 4; ++i) { v[2 * i] = bf_lo(a[i]); v[2 * i + 1] = bf_hi(a[i]); v[8 + 2 * i] = bf_lo(b[i]); v[8 + 2 * i + 1] = bf_hi(b[i]); }
    float s = 0.f;
#pragma unroll
    for (int i = 0; i < 16; ++i) s += v[i];
    s = wave_sum(s);
    const float mean = s * (1.0f / DM);
    float q = 0.f;
#pragma unroll
    for (int i = 0; i < 16; ++i) { const float d = v[i] - mean; q += d * d; }
    q = wave_sum(q);
    const float mb = rbf(mean), vb = rbf(q * (1.0f / DM));
    const float rs = 1.0f / rbf(sqrtf(rbf(vb + 1e-5f)));
    u32x4 oa, ob;
#pragma unroll
    for (int i = 0; i < 4; ++i) {
        oa[i] = cvtpk(rbf(v[2 * i] - mb) * rs, rbf(v[2 * i + 1] - mb) * rs);
        ob[i] = cvtpk(rbf(v[8 + 2 * i] - mb) * rs, rbf(v[8 + 2 * i + 1] - mb) * rs);
    }
    *(u32x4*)(sm + lane * 128 + wid * 16) = oa;
    *(u32x4*)(sm + (64 + lane) * 128 + wid * 16) = ob;
    __syncthreads();
    const int row0 = blockIdx.x * 8;
#pragma unroll
    for (int i = 0; i < 2; ++i) {
        const int p = tid + i * 512, chunk = p >> 3, r = p & 7;
        const u32x4 w = *(const u32x4*)(sm + p * 16);
        *(u32x4*)(xnF + ((size_t)(chunk >> 2) * 128 + (row0 >> 4)) * 1024 + (chunk & 3) * 256 + ((row0 & 15) + r) * 16) = w;
    }
}

__device__ __forceinline__ unsigned silu_mul2(f32x2 g, f32x2 u) {
    const f32x2 t = g * -1.4426950408889634f;
    f32x2 e; e.x = __builtin_amdgcn_exp2f(t.x); e.y = __builtin_amdgcn_exp2f(t.y);
    e = e + 1.0f;
    f32x2 r; r.x = __builtin_amdgcn_rcpf(e.x); r.y = __builtin_amdgcn_rcpf(e.y);
    const f32x2 h = (g * u) * r;
    return cvtpk(h.x, h.y);
}
__device__ __forceinline__ u32x2 silu_mul4(f32x4 g, f32x4 u) {
    return (u32x2){silu_mul2((f32x2){g[0], g[1]}, (f32x2){u[0], u[1]}), silu_mul2((f32x2){g[2], g[3]}, (f32x2){u[2], u[3]})};
}
#define ST16(p, v) asm volatile("global_store_dwordx4 %0, %1, off sc1\n\ts_nop 1" ::"v"(p), "v"(v) : "memory")
__global__ __launch_bounds__(512, 2) void k_gemm1(const char* __restrict__ xnF, const char* __restrict__ wqkv, const char* __restrict__ w1,
                                                  const char* __restrict__ w3, char* __restrict__ qkv, char* __restrict__ act,
                                                  const char* __restrict__ w2, const char* __restrict__ wo) {
    __shared__ __attribute__((aligned(1024))) char smem[147456 + 2048];

    const int tid = threadIdx.x, lane = tid & 63;
    const int wid = __builtin_amdgcn_readfirstlane(tid >> 6), wr = wid & 3, wc = wid >> 2;
    const int bx = blockIdx.x, j = (bx & 7) + 8 * ((bx >> 3) & 3), rb = bx >> 5;
    const unsigned lds0 = (unsigned)(size_t)(LAS char*)smem;
    const bool tq = j < 8;
    const int qb0 = 3 * j, qbx = tq ? 3 * j + 2 : 16 + j, pA = 2 * j - 8, pB = tq ? j : 2 * j - 7;
    const char* bsrc[5]; unsigned binc[5];
#pragma unroll
    for (int i = 0; i < 5; ++i) {
        const int kb = wr, blk = i;
        const int r = lane >> 3, j8 = lane & 7, f = ((r >> 1) & 1) | ((kb & 1) << 1), cp = (j8 >> 1) ^ f;
        const bool ffn = blk >= 3 || (blk < 2 && !tq);
        const int pitch = ffn ? 7168 : 6144;
        const uint64_t wg = (blk == 0 || blk == 3) ? (uint64_t)w1 : (uint64_t)w3, wb = ffn ? wg : (uint64_t)wqkv;
        const int cf = blk < 2 ? pA : pB, cq = blk == 2 ? qbx : qb0 + blk, cblk = ffn ? cf : cq;
        bsrc[i] = (const char*)wb + (size_t)(unsigned)(cblk * 128);
        binc[i] = 32u * pitch;
    }
    unsigned bvq, bvf;
    { const int r = lane >> 3, j8 = lane & 7, cp = (j8 >> 1) ^ (((r >> 1) & 1) | ((wr & 1) << 1));
      bvq = (wr * 8 + r) * 6144 + cp * 32 + (j8 & 1) * 16; bvf = (wr * 8 + r) * 7168 + cp * 32 + (j8 & 1) * 16; }
    const unsigned bv01 = tq ? bvq : bvf;
    const char* asrc = xnF + ((size_t)rb * 16 + 4 * wr) * 1024;
    const unsigned avoff = lane * 16;
    const unsigned adst = lds0 + wr * 4096, bdst = lds0 + 65536 + wr * 5120;
#define G1_DMA_B(SL) do { \
        dma_s1(bv01, bsrc[0], bdst + (SL) * 20480); dma_s1(bv01, bsrc[1], bdst + (SL) * 20480 + 1024); dma_s1(bvq, bsrc[2], bdst + (SL) * 20480 + 2048); \
        dma_s1(bvf, bsrc[3], bdst + (SL) * 20480 + 3072); dma_s1(bvf, bsrc[4], bdst + (SL) * 20480 + 4096); \
        _Pragma("unroll") for (int i = 0; i < 5; ++i) bsrc[i] += binc[i]; } while (0)
#define G1_DMA_A(SL) do { dma_s2(avoff, asrc, adst + (SL) * 16384); dma_s2(avoff, asrc + 2048, adst + (SL) * 16384 + 2048); asrc += 131072; } while (0)
#define G1_DMA(SL, VA) do { if (wc == 0) G1_DMA_B(SL); else { G1_DMA_A(SL); asm volatile("s_waitcnt vmcnt(" #VA ")" ::: "memory"); } } while (0)
    if (wc == 0) { G1_DMA_B(0); G1_DMA_B(1); G1_DMA_B(2); } else { G1_DMA_A(0); G1_DMA_A(1); G1_DMA_A(2); }
    const lds_cptr pa = (lds_cptr)smem + wr * 4096 + lane * 16;
    lds_cptr pb[4], pe0, pe1;
    {
        const int g = lane >> 4, q = (lane >> 2) & 3, p = lane & 3, f = (q >> 1) | ((g & 1) << 1);
        const lds_cptr b0 = (lds_cptr)smem + 65536 + g * 5120 + q * 128 + p * 8;
#pragma unroll
        for (int c = 0; c < 4; ++c) pb[c] = b0 + ((c ^ f) << 5) + wc * 3072;
        pe0 = b0 + ((wc ^ f) << 5) + 2048; pe1 = b0 + (((2 + wc) ^ f) << 5) + 2048;
    }
    f32x4 acc[4][10];
#pragma unroll
    for (int m = 0; m < 4; ++m)
#pragma unroll
        for (int n = 0; n < 10; ++n) { acc[m][n] = (f32x4){0.f, 0.f, 0.f, 0.f}; PIN(acc[m][n]); }
    asm volatile("s_waitcnt vmcnt(8)\n\ts_barrier" ::: "memory");

    if (wc == 1) asm volatile("s_barrier" ::: "memory");
    bf16x8 af[4], bf[10];
#define BFR(P, O) cat8(tr_read((P) + (O)), tr_read((P) + (O) + 512))
#define G1_READS(SL) do { \
        _Pragma("unroll") for (int c = 0; c < 4; ++c) { bf[c] = BFR(pb[c], (SL) * 20480); bf[4 + c] = BFR(pb[c], (SL) * 20480 + 1024); } \
        bf[8] = BFR(pe0, (SL) * 20480); bf[9] = BFR(pe1, (SL) * 20480); \
        _Pragma("unroll") for (int m = 0; m < 4; ++m) af[m] = *(const LAS bf16x8*)(pa + (SL) * 16384 + m * 1024); } while (0)
#define G1_STEP(SL, DMA, VB, ENDBAR) do { \
        G1_READS(SL); DMA; \
        asm volatile("s_waitcnt lgkmcnt(0)" ::: "memory"); \
        asm volatile("s_barrier" ::: "memory"); \
        SCHED(); __builtin_amdgcn_s_setprio(1); \
        _Pragma("unroll") for (int i = 0; i < 40; ++i) { const int m_ = ((i >> 2) & 1) ? 3 - (i & 3) : (i & 3); MF16_ACC(acc[m_][i >> 2], bf[i >> 2], af[m_]); } \
        __builtin_amdgcn_s_setprio(0); SCHED(); \
        if (ENDBAR) asm volatile("s_waitcnt vmcnt(" #VB ")\n\ts_barrier" ::: "memory"); } while (0)
    const char* pf = (wid < 7 ? w2 + (size_t)(bx * 7 + wid) * 4096 : wo + (size_t)bx * 8192);
    for (int it = 0; it < 7; ++it) { G1_STEP(0, G1_DMA(3, 8), 10, true); G1_STEP(1, G1_DMA(0, 8), 10, true); G1_STEP(2, G1_DMA(1, 8), 10, true); G1_STEP(3, G1_DMA(2, 8), 10, true); }
    G1_STEP(0, G1_DMA(3, 8), 10, true);
    G1_STEP(1, { dma_s2(avoff, pf, lds0 + 147456); dma_s2(avoff, pf + 2048, lds0 + 147456); if (wc == 1) asm volatile("s_waitcnt vmcnt(8)" ::: "memory"); }, 9, true);
    G1_STEP(2, { if (wc == 1) asm volatile("s_waitcnt vmcnt(4)" ::: "memory"); }, 4, true);
    G1_STEP(3, { if (wc == 0) dma_s1(avoff, wo + (size_t)bx * 8192 + 4096 + wid * 1024, lds0 + 147456); }, 63, wc == 0);

#undef G1_STEP
#undef G1_READS
#undef BFR
#undef G1_DMA
#undef G1_DMA_A
#undef G1_DMA_B
    asm volatile("s_nop 7" : "+v"(acc[0][9]), "+v"(acc[1][9]), "+v"(acc[2][9]), "+v"(acc[3][9]), "+v"(acc[3][8]), "+v"(acc[2][8]));
    SCHED();
    int tz = threadIdx.x; asm volatile("" : "+v"(tz));
    const int fr = tz & 15, fq = (tz >> 4) & 3;
    const int tb = (rb * 16 + wr * 4) * 1024;
    const bool ffn_main = !tq || wc == 1;
    const int cm = ((fq & 1) << 1) | (fq >> 1), ce = 2 * wc + (fq >> 1);
    {
        const int cb0 = 2 * qbx;
        const float sc = cb0 < 32 ? QSCALE : 1.0f;
        char* dst = qkv + (size_t)(cb0 + (fq & 1)) * 131072 + tb + ce * 256 + ((fr ^ (4 * ce)) * 16);
#pragma unroll
        for (int m = 0; m < 4; ++m) {
            const f32x4 va = acc[m][8] * sc, vb = acc[m][9] * sc;
            unsigned a0 = cvtpk(va[0], va[1]), a1 = cvtpk(va[2], va[3]), b0 = cvtpk(vb[0], vb[1]), b1 = cvtpk(vb[2], vb[3]);
            swap16(a0, b0); swap16(a1, b1);
            ST16(dst + m * 1024, ((u32x4){a0, a1, b0, b1}));
        }
    }
    if (ffn_main) {
        char* dst = act + (size_t)(wc ? pB : pA) * 262144 + tb + cm * 256 + fr * 16;
#pragma unroll
        for (int m = 0; m < 4; ++m)
#pragma unroll
            for (int sub = 0; sub < 2; ++sub) {
                const u32x2 a = silu_mul4(acc[m][2 * sub], acc[m][4 + 2 * sub]), b = silu_mul4(acc[m][2 * sub + 1], acc[m][4 + 2 * sub + 1]);
                unsigned a0 = a[0], a1 = a[1], b0 = b[0], b1 = b[1];
                swap16(a0, b0); swap16(a1, b1);
                ST16(dst + sub * 131072 + m * 1024, ((u32x4){a0, a1, b0, b1}));
            }
    } else {
        const int lo = tb + cm * 256 + ((fr ^ (4 * cm)) * 16);
#pragma unroll
        for (int sub = 0; sub < 4; ++sub) {
            const int cb = 2 * qb0 + sub;
            const float sc = cb < 32 ? QSCALE : 1.0f;
            char* dst = qkv + (size_t)cb * 131072 + lo;
#pragma unroll
            for (int m = 0; m < 4; ++m) {
                const f32x4 va = acc[m][2 * sub] * sc, vb = acc[m][2 * sub + 1] * sc;
                unsigned a0 = cvtpk(va[0], va[1]), a1 = cvtpk(va[2], va[3]), b0 = cvtpk(vb[0], vb[1]), b1 = cvtpk(vb[2], vb[3]);
                swap16(a0, b0); swap16(a1, b1);
                ST16(dst + m * 1024, ((u32x4){a0, a1, b0, b1}));
            }
        }
    }

}

__device__ __forceinline__ void attn_role(char* smem, const int bx, const char* __restrict__ qkv, char* __restrict__ act, unsigned* __restrict__ ctr) {

    const int tid = threadIdx.x, lane = tid & 63;
    const int wid = __builtin_amdgcn_readfirstlane(tid >> 6);
    const int head = (bx & 7) * 4 + (bx >> 6), qb = (bx >> 3) & 7;
    __builtin_amdgcn_s_setprio(2);
    const char* Qf = qkv + (size_t)head * 131072;
    const char* Kf = Qf + 4 * MB;
    const char* Vr = Qf + 8 * MB;
    const unsigned lds0 = (unsigned)(size_t)(LAS char*)smem;
    const unsigned voff = lane * 16;
    const int tq0 = qb * 8 + wid * 2;
    unsigned fo[2];
#pragma unroll
    for (int s = 0; s < 2; ++s) { const int c = 2 * s + (lane >> 5); fo[s] = ((lane >> 4) & 1) * 1024 + c * 256 + (((lane & 15) ^ (4 * c)) * 16); }
    bf16x8 q[2][2];
    asm volatile("global_load_dwordx4 %0, %4, %6\n\tglobal_load_dwordx4 %1, %5, %6\n\tglobal_load_dwordx4 %2, %4, %6 offset:2048\n\tglobal_load_dwordx4 %3, %5, %6 offset:2048"
                 : "=&v"(q[0][0]), "=&v"(q[0][1]), "=&v"(q[1][0]), "=&v"(q[1][1]) : "v"(fo[0]), "v"(fo[1]), "s"(Qf + tq0 * 2048) : "memory");
    bf16x8 sel;
    { const short one = (((lane >> 2) & 1) == ((lane >> 4) & 1)) ? (short)0x3f80 : (short)0; sel = (bf16x8){one, one, one, one, one, one, one, one}; }
    const lds_cptr kp0 = (lds_cptr)smem + fo[0], kp1 = (lds_cptr)smem + fo[1];
    lds_cptr vp0, vp1;
    { const int h = lane >> 5, c = 2 * ((lane >> 4) & 1) + ((lane >> 1) & 1), q4 = (lane >> 2) & 3;
      vp0 = (lds_cptr)smem + 8192 + c * 256 + (h ^ c) * 64 + q4 * 16 + (lane & 1) * 8; vp1 = (lds_cptr)smem + 8192 + c * 256 + ((2 + h) ^ c) * 64 + q4 * 16 + (lane & 1) * 8; }
    const char* Kw = Kf + wid * 2048;
    const char* Vw = Vr + wid * 2048;
    volatile LAS unsigned* flag = (volatile LAS unsigned*)(smem);
    const unsigned ooff = ((lane >> 4) & 1) * 1024 + (lane >> 5) * 256 + (lane & 15) * 16;
#define AT_DMA(ST, SLOT) do { const unsigned d_ = lds0 + (SLOT) + wid * 2048; \
        dma_s2(voff, Kf + (ST) * 8192 + wid * 2048, d_); dma_s2(voff, Vr + (ST) * 8192 + wid * 2048, d_ + 8192); } while (0)
    for (int attempt = 0; attempt < 2; ++attempt) {
        f32x16 o[2]; f32x4 l[2];
        bf16x8 k0, k1;
        asm volatile("global_load_dwordx4 %0, %2, %4\n\tglobal_load_dwordx4 %1, %3, %4" : "=&v"(k0), "=&v"(k1) : "v"(fo[0]), "v"(fo[1]), "s"(Kf) : "memory");
        AT_DMA(0, 0); AT_DMA(1, 16384); AT_DMA(2, 32768);
        o[0] = (f32x16){}; o[1] = (f32x16){}; l[0] = (f32x4){}; l[1] = (f32x4){};
        PIN(o[0]); PIN(o[1]); PIN(l[0]); PIN(l[1]);
        asm volatile("s_waitcnt vmcnt(12)" : "+v"(k0), "+v"(k1), "+v"(q[0][0]), "+v"(q[0][1]), "+v"(q[1][0]), "+v"(q[1][1])::"memory");
        f32x16 negm[2];
        {
            float m0 = -INFINITY, m1 = -INFINITY;
            const int nt = attempt == 0 ? 1 : 64;
            for (int kt = 0; kt < nt; ++kt) {
                if (kt) { k0 = *(const bf16x8*)(Kf + kt * 2048 + fo[0]); k1 = *(const bf16x8*)(Kf + kt * 2048 + fo[1]); }
                f32x16 s0 = MF32(k0, q[0][0], (f32x16){}), s1 = MF32(k0, q[1][0], (f32x16){});
                s0 = MF32(k1, q[0][1], s0); s1 = MF32(k1, q[1][1], s1);
#pragma unroll
                for (int r = 0; r < 16; ++r) { m0 = fmaxf(m0, s0[r]); m1 = fmaxf(m1, s1[r]); }
            }
            m0 = fmaxf(m0, __shfl_xor(m0, 32)); m1 = fmaxf(m1, __shfl_xor(m1, 32));
#pragma unroll
            for (int r = 0; r < 16; ++r) { negm[0][r] = -m0; negm[1][r] = -m1; }
            PIN(negm[0]); PIN(negm[1]);
        }
        asm volatile("s_waitcnt vmcnt(0)\n\ts_barrier" ::: "memory");
        bf16x8 kf0 = *(const LAS bf16x8*)(kp0), kf1 = *(const LAS bf16x8*)(kp1);
        bf16x8 vf0 = (bf16x8){}, vf1 = (bf16x8){};
        u32x4 pa0 = (u32x4){}, pa1 = (u32x4){}, pb0 = (u32x4){}, pb1 = (u32x4){};
        f32x16 sA = MF32(kf0, q[0][0], negm[0]); sA = MF32(kf1, q[0][1], sA);
        f32x16 sB;
#define EX(X, i) X[i] = __builtin_amdgcn_exp2f(X[i])
#define PK(X, i) cvtpk(X[i], X[(i) + 1])
#define BC8(x) __builtin_bit_cast(bf16x8, x)
#define AT_UNIT(SC, PC0, PC1, SN, QN, NM, PP0, PP1, OP, LP, K0, K1, V0, V1, L0, L1, L2, L3) do { unsigned c0, c1, c2, c3, c4, c5, c6, c7; \
            SN = MF32(K0, QN[0], NM); SCHED(); L0; SCHED(); EX(SC, 0); EX(SC, 1); EX(SC, 2); SCHED(); \
            OP = MF32(V0, BC8(PP0), OP); SCHED(); L1; SCHED(); c0 = PK(SC, 0); EX(SC, 3); EX(SC, 4); EX(SC, 5); SCHED(); \
            SN = MF32(K1, QN[1], SN); SCHED(); L2; SCHED(); c1 = PK(SC, 2); c2 = PK(SC, 4); EX(SC, 6); EX(SC, 7); EX(SC, 8); SCHED(); \
            OP = MF32(V1, BC8(PP1), OP); SCHED(); L3; SCHED(); c3 = PK(SC, 6); EX(SC, 9); EX(SC, 10); EX(SC, 11); SCHED(); \
            MF16_ACC(LP, sel, PP0); SCHED(); c4 = PK(SC, 8); c5 = PK(SC, 10); EX(SC, 12); EX(SC, 13); SCHED(); \
            MF16_ACC(LP, sel, PP1); SCHED(); EX(SC, 14); EX(SC, 15); SCHED(); c6 = PK(SC, 12); SCHED(); c7 = PK(SC, 14); \
            PC0 = (u32x4){c0, c1, c2, c3}; PC1 = (u32x4){c4, c5, c6, c7}; SCHED(); } while (0)
#define AT_M0(SLOT, V) asm volatile("s_mov_b32 m0, %0" ::"s"(lds0 + (SLOT) + wid * 2048 + (V) * 8192) : "memory")
#define AT_PAIR(ST, V) asm volatile("global_load_lds_dwordx4 %0, %1\n\tglobal_load_lds_dwordx4 %0, %1 offset:1024" ::"v"(voff), "s"(((V) ? Vw : Kw) + ((ST) & 15) * 8192) : "memory")
#define AT_STAGE(SO, SN_, DM, ST, SLA, SLB) do { _Pragma("unroll") for (int kt = 0; kt < 4; ++kt) { \
                const int ko = kt < 3 ? (SO) + (kt + 1) * 2048 : (SN_), vo = (SO) + kt * 2048; \
                bf16x8 kn0, kn1, vn0, vn1; \
                AT_UNIT(sA, pa0, pa1, sB, q[1], negm[1], pb0, pb1, o[1], l[1], kf0, kf1, vf0, vf1, \
                        (kn0 = *(const LAS bf16x8*)(kp0 + ko), kn1 = *(const LAS bf16x8*)(kp1 + ko)), vn0 = cat8(tr_read(vp0 + vo), tr_read(vp1 + vo)), \
                        vn1 = cat8(tr_read(vp0 + vo + 1024), tr_read(vp1 + vo + 1024)), {}); \
                kf0 = kn0; kf1 = kn1; vf0 = vn0; vf1 = vn1; \
                __builtin_amdgcn_s_waitcnt(0xC07F); SCHED(); \
                AT_UNIT(sB, pb0, pb1, sA, q[0], negm[0], pa0, pa1, o[0], l[0], kf0, kf1, vf0, vf1, \
                        if (DM) AT_M0((kt >> 1) ? (SLB) : (SLA), kt & 1), if (DM) AT_PAIR((ST) + (kt >> 1), kt & 1), {}, {}); } } while (0)
        int s0 = 0, s1 = 16384, s2 = 32768, s3 = 49152, s4 = 65536;
        for (int k = 0; k < 8; ++k) {
            AT_STAGE(s0, s1, true, 2 * k + 3, s3, s4);
            AT_STAGE(s1, s2, false, 0, 0, 0);
            { const int t0 = s0, t1 = s1; s0 = s2; s1 = s3; s2 = s4; s3 = t0; s4 = t1; }
            asm volatile("s_waitcnt vmcnt(0)\n\ts_barrier" ::: "memory");
        }
#undef AT_STAGE
#undef AT_M0
#undef AT_PAIR
        o[1] = MF32(vf0, BC8(pb0), o[1]); o[1] = MF32(vf1, BC8(pb1), o[1]);
        l[1] = MF16(sel, BC8(pb0), l[1]); l[1] = MF16(sel, BC8(pb1), l[1]);
#undef AT_UNIT
        asm volatile("s_nop 7\n\ts_nop 7" : "+v"(l[0]), "+v"(l[1]), "+v"(o[0]), "+v"(o[1]));
        float tsum = 0.f;
#pragma unroll
        for (int i = 0; i < 2; ++i) {
            const float r = 1.0f / l[i][0];
#pragma unroll
            for (int e = 0; e < 16; ++e) { tsum += fabsf(o[i][e]); o[i][e] *= r; }
        }
        const bool bad = !(l[0][0] < 1.0995e12f) || !(l[1][0] < 1.0995e12f) || !(tsum < INFINITY);
        if (lane == 0) flag[wid] = 0u;
        if (bad) flag[wid] = 1u;
#pragma unroll
        for (int i = 0; i < 2; ++i)
#pragma unroll
            for (int s = 0; s < 2; ++s) {
                unsigned a0 = cvtpk(o[i][8 * s + 0], o[i][8 * s + 1]), a1 = cvtpk(o[i][8 * s + 2], o[i][8 * s + 3]);
                unsigned b0 = cvtpk(o[i][8 * s + 4], o[i][8 * s + 5]), b1 = cvtpk(o[i][8 * s + 6], o[i][8 * s + 7]);
                swap32(a0, b0); swap32(a1, b1);
                const u32x4 w = (u32x4){a0, a1, b0, b1};
                asm volatile("global_store_dwordx4 %0, %1, %2 sc1\n\ts_nop 1" ::"v"(ooff), "v"(w), "s"(act + (size_t)(112 + head) * 131072 + (tq0 + i) * 2048 + s * 512) : "memory");
            }
        asm volatile("s_waitcnt vmcnt(0) lgkmcnt(0)\n\ts_barrier" ::: "memory");
        const unsigned redo = flag[0] | flag[1] | flag[2] | flag[3];
        if (attempt == 1 || !__builtin_amdgcn_readfirstlane(redo)) break;
        asm volatile("s_waitcnt lgkmcnt(0)\n\ts_barrier" ::: "memory");
    }
#undef AT_DMA
    if (tid == 0) __hip_atomic_fetch_add(ctr + qb * 64, 1u, __ATOMIC_RELAXED, __HIP_MEMORY_SCOPE_AGENT);

}

__device__ __forceinline__ void gemm2_role(char* smem, const int bx, const char* __restrict__ act, const char* __restrict__ w2, const char* __restrict__ wo,
                                           const char* __restrict__ x, char* __restrict__ out, unsigned* __restrict__ ctr) {

    const int tid = threadIdx.x, lane = tid & 63;
    const int wid = __builtin_amdgcn_readfirstlane(tid >> 6);
    const int xc = bx & 7, ii = bx >> 3, tm = (xc >> 1) * 4 + (ii >> 3), tn = (xc & 1) * 8 + (ii & 7);
    const unsigned lds0 = (unsigned)(size_t)(LAS char*)smem + wid * 16384;
    __builtin_amdgcn_s_sleep(127);
    const char* asrc = act + (size_t)wid * 131072 + tm * 8192;
    unsigned av[4];
#pragma unroll
    for (int i = 0; i < 4; ++i) av[i] = (i ^ wid) * 2048 + lane * 16;
    const char* wsrc = w2 + (size_t)wid * 32 * 2048 + tn * 128;
    const unsigned wv0 = (lane >> 3) * 2048 + ((((lane >> 1) & 3) ^ ((lane >> 4) & 1)) * 32) + (lane & 1) * 16;
    const unsigned wv1 = (lane >> 3) * 2048 + ((((lane >> 1) & 3) ^ ((lane >> 4) & 1) ^ 2) * 32) + (lane & 1) * 16 + 16384 - 1024;
    lds_cptr wp[4];
    {
        const int g = lane >> 4, q = (lane >> 2) & 3, p = lane & 3, f = (q >> 1) | ((g & 1) << 1);
#pragma unroll
        for (int c = 0; c < 4; ++c) wp[c] = (lds_cptr)smem + wid * 16384 + g * 1024 + q * 128 + ((c ^ f) << 5) + p * 8;
    }
    f32x4 acc[8][4];
#pragma unroll
    for (int i = 0; i < 8; ++i)
#pragma unroll
        for (int n = 0; n < 4; ++n) { acc[i][n] = (f32x4){}; PIN(acc[i][n]); }
    bf16x8 a0[4], a1[4], a2[4], a3[4], wfa[4], wfb[4];
#define G2_LDA(A, H) do { \
        asm volatile("global_load_dwordx4 %0, %4, %6\n\tglobal_load_dwordx4 %1, %4, %6 offset:1024\n\tglobal_load_dwordx4 %2, %5, %6\n\tglobal_load_dwordx4 %3, %5, %6 offset:1024" \
                     : "=&v"(A[0]), "=&v"(A[1]), "=&v"(A[2]), "=&v"(A[3]) : "v"(av[2 * (H)]), "v"(av[2 * (H) + 1]), "s"(asrc) : "memory"); \
        if (H) asrc += 524288; } while (0)
#define G2_LDW(SL) do {     \
        asm volatile("s_mov_b32 m0, %3\n\ts_nop 0\n\tglobal_load_lds_dwordx4 %0, %2\n\tglobal_load_lds_dwordx4 %1, %2 offset:1024" \
                     ::"v"(wv0), "v"(wv1), "s"(wsrc), "s"(lds0 + (SL) * 2048) : "memory"); \
        wsrc += ((SL) & 1) ? 262144 - 32768 : 32768; } while (0)
#define G2_WAIT(N, A) asm volatile("s_waitcnt vmcnt(" #N ")" : "+v"(A[0]), "+v"(A[1]), "+v"(A[2]), "+v"(A[3])::"memory")
#define WFR(P, O) cat8(tr_read((P) + (O)), tr_read((P) + (O) + 512))
#define G2_STEP(N, AC, AN, WC, WN, SL, RD, IA, IW) do { \
        if ((N) >= 0) G2_WAIT(N, AN); \
        if (RD) { WN[2 * ((SL) & 1)] = WFR(wp[2 * ((SL) & 1)], ((((SL) >> 1) + 1) & 3) * 4096); WN[2 * ((SL) & 1) + 1] = WFR(wp[2 * ((SL) & 1) + 1], ((((SL) >> 1) + 1) & 3) * 4096); } \
        SCHED(); \
        _Pragma("unroll") for (int i = 0; i < 16; ++i) { \
            MF16_ACC(acc[4 * ((SL) & 1) + (i >> 2)][((i >> 2) & 1) ? 3 - (i & 3) : (i & 3)], WC[((i >> 2) & 1) ? 3 - (i & 3) : (i & 3)], AC[i >> 2]); \
            if ((IA) && (i & 3) == 3) asm volatile("global_load_dwordx4 %0, %1, %2 offset:%c3" : "=&v"(AC[i >> 2]) : "v"(av[2 * ((SL) & 1) + (i >> 3)]), "s"(asrc), "i"(((i >> 2) & 1) * 1024) : "memory"); } \
        SCHED(); if ((IA) && ((SL) & 1)) asrc += 524288; if (IW) G2_LDW(SL); } while (0)
#define G2_STEP4(N0, N1, N2, N3, SL, RDL, IA, IW) do { \
        G2_STEP(N0, a0, a1, wfa, wfb, (SL), true, IA, IW); G2_STEP(N1, a1, a2, wfa, wfb, (SL) + 1, true, IA, IW); \
        G2_STEP(N2, a2, a3, wfb, wfa, (SL) + 2, RDL, IA, IW); G2_STEP(N3, a3, a0, wfb, wfa, (SL) + 3, RDL, IA, IW); } while (0)
    G2_LDW(0); G2_LDW(1); G2_LDW(2); G2_LDW(3);
    G2_LDA(a0, 0); G2_LDW(4); G2_LDA(a1, 1); G2_LDW(5); G2_LDA(a2, 0); G2_LDW(6); G2_LDA(a3, 1); G2_LDW(7);
    G2_WAIT(20, a0);
#pragma unroll
    for (int c = 0; c < 4; ++c) wfa[c] = WFR(wp[c], 0);
    for (int it = 0; it < 6; ++it) { G2_STEP4(14, 14, 14, 14, 0, true, true, true); G2_STEP4(14, 14, 14, 14, 4, true, true, true); }
    G2_STEP4(14, 12, 10, 8, 0, true, true, false);
    G2_STEP4(8, 4, 0, -1, 4, false, false, false);
#undef G2_STEP4
#undef G2_STEP
#undef G2_WAIT
#undef G2_LDW
#undef G2_LDA
    const int fr = lane & 15, fq = lane >> 4;
    const size_t orow = (size_t)(tm * 128 + wid * 32 + fr) * 2048 + tn * 128 + (fq & 1) * 32 + (fq >> 1) * 16;
    u32x4 xr[4];
#pragma unroll
    for (int c = 0; c < 4; ++c) xr[c] = *(const u32x4*)(x + orow + (c >> 1) * 32768 + (c & 1) * 64);
    asm volatile("s_nop 7" : "+v"(acc[7][0]), "+v"(acc[7][1]), "+v"(acc[7][2]), "+v"(acc[7][3]));
    SCHED();
    char* mine = smem + wid * 16384 + lane * 16;
#pragma unroll
    for (int i = 0; i < 16; ++i) *(f32x4*)(mine + i * 1024) = acc[4 + (i >> 2)][i & 3];
    __syncthreads();
#pragma unroll
    for (int i = 0; i < 16; ++i) acc[i >> 2][i & 3] += *(const f32x4*)(smem + (wid ^ 2) * 16384 + lane * 16 + i * 1024);
    __syncthreads();
#pragma unroll
    for (int i = 0; i < 8; ++i) *(f32x4*)(mine + i * 1024) = acc[2 + (i >> 2)][i & 3];
    __syncthreads();
#pragma unroll
    for (int i = 0; i < 8; ++i) { acc[i >> 2][i & 3] += *(const f32x4*)(smem + (wid ^ 1) * 16384 + lane * 16 + i * 1024); PIN(acc[i >> 2][i & 3]); }
    __syncthreads();
    const unsigned ldsb = (unsigned)(size_t)(LAS char*)smem;
    const char* wos = wo + tn * 128;
    const char* osrc = act + (size_t)112 * 131072 + tm * 8192 + wid * 2048;
    unsigned ov = lane * 16;
    lds_cptr wq[4];
#pragma unroll
    for (int c = 0; c < 4; ++c) { wq[c] = wp[c] - wid * 16384; PIN(wq[c]); }
    PIN(ov);
#define G3_LDW(HS, SLOT) asm volatile("s_mov_b32 m0, %3\n\ts_nop 0\n\tglobal_load_lds_dwordx4 %0, %2\n\tglobal_load_lds_dwordx4 %1, %2 offset:1024" \
                     ::"v"(wv0), "v"(wv1), "s"(wos + (size_t)(HS) * 32768), "s"(ldsb + (SLOT) * 2048) : "memory")
#pragma unroll
    for (int i = 0; i < 10; ++i) G3_LDW(wid * 10 + i, wid * 10 + i);
    if (wid == 0) {
        const unsigned long long t0 = __builtin_amdgcn_s_memrealtime();
        while ((unsigned)__builtin_amdgcn_readfirstlane(__hip_atomic_load(ctr + (tm >> 1) * 64, __ATOMIC_RELAXED, __HIP_MEMORY_SCOPE_AGENT)) < 32u) {
            if (__builtin_amdgcn_s_memrealtime() - t0 > 5000000ull) break;
            __builtin_amdgcn_s_sleep(2);
        }
        __builtin_amdgcn_fence(__ATOMIC_ACQUIRE, "wavefront");
        asm volatile("" ::: "memory");
    }
    asm volatile("s_waitcnt vmcnt(0)\n\ts_barrier" ::: "memory");

    bf16x8 oa[8][4];
#define G3_LDA(G) asm volatile("global_load_dwordx4 %0, %4, %5 sc1\n\tglobal_load_dwordx4 %1, %4, %5 offset:1024 sc1\n\tglobal_load_dwordx4 %2, %4, %6 sc1\n\tglobal_load_dwordx4 %3, %4, %6 offset:1024 sc1" \
                     : "=&v"(oa[(G) & 7][0]), "=&v"(oa[(G) & 7][1]), "=&v"(oa[(G) & 7][2]), "=&v"(oa[(G) & 7][3]) \
                     : "v"(ov), "s"(osrc + (size_t)(G) * 262144), "s"(osrc + (size_t)(G) * 262144 + 131072) : "memory")
#define G3_WAIT(N, G) asm volatile("s_waitcnt vmcnt(" #N ")" : "+v"(oa[(G) & 7][0]), "+v"(oa[(G) & 7][1]), "+v"(oa[(G) & 7][2]), "+v"(oa[(G) & 7][3])::"memory")
    s16x4 wl[2][8], wh[2][8];
#define G3_WA(G, i) (wq[(i) & 3] + (((G) >= 8 && (G) < 10) ? 65536 : 0) + ((((G) * 2 + ((i) >> 2)) % 20) * 4096 - (((G) >= 8 && (G) < 10) ? 65536 : 0)))
#define G3_LD1(G, j) asm volatile("global_load_dwordx4 %0, %1, %2 offset:%c3 sc1" : "=&v"(oa[(G) & 7][j]) : "v"(ov), "s"(osrc + (size_t)(G) * 262144 + ((j) >> 1) * 131072), "i"(((j) & 1) * 1024) : "memory")
#define G3_GROUP(N, G, RD) do { G3_WAIT(N, G); SCHED(); \
        const bool rf_ = (G) == 5 || (G) == 6; \
        _Pragma("unroll") for (int i = 0; i < 8; ++i) { const bf16x8 w_ = cat8(wl[(G) & 1][i], wh[(G) & 1][i]); \
            MF16_ACC(acc[0][i & 3], w_, oa[(G) & 7][2 * (i >> 2)]); SCHED(); if (RD) wl[((G) + 1) & 1][i] = tr_read(G3_WA((G) + 1, i)); SCHED(); \
            MF16_ACC(acc[1][i & 3], w_, oa[(G) & 7][2 * (i >> 2) + 1]); SCHED(); if (RD) wh[((G) + 1) & 1][i] = tr_read(G3_WA((G) + 1, i) + 512); \
            if (rf_ && !(i & 1) && i < 6) G3_LDW(40 + wid * 6 + ((G) - 5) * 3 + (i >> 1), wid * 6 + ((G) - 5) * 3 + (i >> 1)); \
            if ((G) < 11 && (i & 1)) G3_LD1((G) + 5, i >> 1); SCHED(); } } while (0)
    G3_LDA(0); G3_LDA(1); G3_LDA(2); G3_LDA(3); G3_LDA(4);
#pragma unroll
    for (int i = 0; i < 8; ++i) { wl[0][i] = tr_read(G3_WA(0, i)); wh[0][i] = tr_read(G3_WA(0, i) + 512); }
    G3_GROUP(16, 0, true); G3_GROUP(16, 1, true); G3_GROUP(16, 2, true); G3_GROUP(16, 3, true); G3_GROUP(16, 4, true);

    asm volatile("s_waitcnt lgkmcnt(0)\n\ts_barrier" ::: "memory");
    G3_GROUP(16, 5, true); G3_GROUP(22, 6, true); G3_GROUP(28, 7, true); G3_GROUP(28, 8, true);

    asm volatile("s_waitcnt vmcnt(10)\n\ts_barrier" ::: "memory");

    G3_GROUP(10, 9, true); G3_GROUP(22, 10, true); G3_GROUP(16, 11, true); G3_GROUP(12, 12, true); G3_GROUP(8, 13, true); G3_GROUP(4, 14, true); G3_GROUP(0, 15, false);

#undef G3_GROUP
#undef G3_WA
#undef G3_WAIT
#undef G3_LDA
#undef G3_LD1
#undef G3_LDW
#undef WFR
    asm volatile("s_nop 7" : "+v"(acc[0][0]), "+v"(acc[0][1]), "+v"(acc[0][2]), "+v"(acc[0][3]), "+v"(acc[1][0]), "+v"(acc[1][1]), "+v"(acc[1][2]), "+v"(acc[1][3]));
#pragma unroll
    for (int c = 0; c < 4; ++c) {
        float v[8];
#pragma unroll
        for (int e = 0; e < 4; ++e) {
            unsigned a = __float_as_uint(acc[c >> 1][2 * (c & 1)][e]), b = __float_as_uint(acc[c >> 1][2 * (c & 1) + 1][e]);
            swap16(a, b);
            v[e] = __uint_as_float(a); v[4 + e] = __uint_as_float(b);
        }
        u32x4 w;
#pragma unroll
        for (int e = 0; e < 4; ++e) w[e] = cvtpk(v[2 * e] + bf_lo(xr[c][e]), v[2 * e + 1] + bf_hi(xr[c][e]));
        *(u32x4*)(out + orow + (c >> 1) * 32768 + (c & 1) * 64) = w;
    }

}

__global__ __launch_bounds__(256, 2) void k_fused(const char* __restrict__ qkv, char* __restrict__ act, const char* __restrict__ w2, const char* __restrict__ wo,
                                                  const char* __restrict__ x, char* __restrict__ out, unsigned* __restrict__ ctr) {
    __shared__ __attribute__((aligned(1024))) char smem[81920];
    asm volatile("" ::"s"(qkv), "s"(act), "s"(w2));
    if (__builtin_expect(blockIdx.x < 256, 1)) attn_role(smem, blockIdx.x, qkv, act, ctr);
    else gemm2_role(smem, blockIdx.x - 256, act, w2, wo, x, out, ctr);
}

extern "C" void kernel_launch(void* const* d_in, const int* in_sizes, int n_in, void* d_out, int out_size, void* d_ws, size_t ws_size,
                              hipStream_t stream) {
    const u16* x = (const u16*)d_in[0];
    const char* wqkv = (const char*)d_in[1];
    const char* wo = (const char*)d_in[2];
    const char* w1 = (const char*)d_in[4];
    const char* w3 = (const char*)d_in[5];
    const char* w2 = (const char*)d_in[6];
    char* ws = (char*)d_ws;
    k_ln<<<512, 512, 0, stream>>>(x, ws + WS_XN, (unsigned*)(ws + WS_CTR), wqkv, w1, w3);
    k_gemm1<<<256, 512, 0, stream>>>(ws + WS_XN, wqkv, w1, w3, ws + WS_QKV, ws + WS_ACT, w2, wo);
    k_fused<<<512, 256, 0, stream>>>(ws + WS_QKV, ws + WS_ACT, w2, wo, (const char*)x, (char*)d_out, (unsigned*)(ws + WS_CTR));
}
```

```cpp
#include <hip/hip_runtime.h>
#include <stdint.h>

typedef unsigned short u16;
typedef short bf16x8 __attribute__((ext_vector_type(8)));
typedef short s16x4 __attribute__((ext_vector_type(4)));
typedef float f32x2 __attribute__((ext_vector_type(2)));
typedef float f32x4 __attribute__((ext_vector_type(4)));
typedef float f32x16 __attribute__((ext_vector_type(16)));
typedef unsigned u32x2 __attribute__((ext_vector_type(2)));
typedef unsigned u32x4 __attribute__((ext_vector_type(4)));
typedef __bf16 bf16x2_t __attribute__((ext_vector_type(2)));
#define LAS __attribute__((address_space(3)))
typedef const LAS char* lds_cptr;

constexpr int SEQ = 2048, DM = 1024, FF = 3584, NH = 32;
constexpr size_t MB = 1024 * 1024;
constexpr size_t WS_CTR = 0;
constexpr size_t WS_XN = 65536;
constexpr size_t WS_QKV = WS_XN + 4 * MB;
constexpr size_t WS_ACT = WS_QKV + 12 * MB;
constexpr float QSCALE = 0.17677669529663687f * 1.4426950408889634f;

#define SCHED() __builtin_amdgcn_sched_barrier(0)
#define PIN(x) asm volatile("" : "+v"(x))
#define MF32(a, b, c) __builtin_amdgcn_mfma_f32_32x32x16_bf16(a, b, c, 0, 0, 0)
#define MF16(a, b, c) __builtin_amdgcn_mfma_f32_16x16x32_bf16(a, b, c, 0, 0, 0)

#define MF16_ACC(acc, a, b) asm volatile("v_mfma_f32_16x16x32_bf16 %0, %1, %2, %0" : "+v"(acc) : "v"(a), "v"(b))

__device__ __forceinline__ unsigned cvtpk(float lo, float hi) {
    f32x2 v = {lo, hi};
    bf16x2_t b = __builtin_convertvector(v, bf16x2_t);
    return __builtin_bit_cast(unsigned, b);
}
__device__ __forceinline__ float bf_lo(unsigned w) { return __uint_as_float(w << 16); }
__device__ __forceinline__ float bf_hi(unsigned w) { return __uint_as_float(w & 0xffff0000u); }
__device__ __forceinline__ float rbf(float x) { return bf_lo(cvtpk(x, x)); }
__device__ __forceinline__ s16x4 tr_read(lds_cptr p) {
    return __builtin_bit_cast(s16x4, __builtin_amdgcn_ds_read_tr16_b64_v4i16((LAS s16x4*)p));
}
__device__ __forceinline__ bf16x8 cat8(s16x4 lo, s16x4 hi) { return __builtin_shufflevector(lo, hi, 0, 1, 2, 3, 4, 5, 6, 7); }
__device__ __forceinline__ void dma_v(const void* g, unsigned lds) {
    asm volatile("s_mov_b32 m0, %1\n\ts_nop 0\n\tglobal_load_lds_dwordx4 %0, off" ::"v"(g), "s"(lds) : "memory");
}
__device__ __forceinline__ void dma_s1(unsigned voff, const void* sbase, unsigned lds) {
    asm volatile("s_mov_b32 m0, %2\n\ts_nop 0\n\tglobal_load_lds_dwordx4 %0, %1" ::"v"(voff), "s"(sbase), "s"(lds) : "memory");
}
__device__ __forceinline__ void dma_s2(unsigned voff, const void* sbase, unsigned lds) {
    asm volatile("s_mov_b32 m0, %2\n\ts_nop 0\n\tglobal_load_lds_dwordx4 %0, %1\n\tglobal_load_lds_dwordx4 %0, %1 offset:1024" ::"v"(voff), "s"(sbase), "s"(lds) : "memory");
}
__device__ __forceinline__ void swap16(unsigned& a, unsigned& b) {
    auto r = __builtin_amdgcn_permlane16_swap(a, b, false, false);
    a = r[0]; b = r[1];
}
__device__ __forceinline__ void swap32(unsigned& a, unsigned& b) {
    auto r = __builtin_amdgcn_permlane32_swap(a, b, false, false);
    a = r[0]; b = r[1];
}

__device__ __forceinline__ float wave_sum(float v) {
    v += __builtin_bit_cast(float, __builtin_amdgcn_update_dpp(0, __builtin_bit_cast(int, v), 0xB1, 0xf, 0xf, true));
    v += __builtin_bit_cast(float, __builtin_amdgcn_update_dpp(0, __builtin_bit_cast(int, v), 0x4E, 0xf, 0xf, true));
    v += __builtin_bit_cast(float, __builtin_amdgcn_update_dpp(0, __builtin_bit_cast(int, v), 0x141, 0xf, 0xf, true));
    v += __builtin_bit_cast(float, __builtin_amdgcn_update_dpp(0, __builtin_bit_cast(int, v), 0x140, 0xf, 0xf, true));
    auto a = __builtin_amdgcn_permlane16_swap(__float_as_uint(v), __float_as_uint(v), false, false);
    v = __uint_as_float(a[0]) + __uint_as_float(a[1]);
    auto b = __builtin_amdgcn_permlane32_swap(__float_as_uint(v), __float_as_uint(v), false, false);
    return __uint_as_float(b[0]) + __uint_as_float(b[1]);
}
__global__ __launch_bounds__(512) void k_ln(const u16* __restrict__ x, char* __restrict__ xnF, unsigned* __restrict__ ctr,
                                            const char* __restrict__ wqkv, const char* __restrict__ w1, const char* __restrict__ w3) {
    __shared__ __attribute__((aligned(16))) char sm[16384];
    const int tid = threadIdx.x, lane = tid & 63, wid = tid >> 6;
    if (blockIdx.x >= 256) {
        const size_t o = (size_t)((blockIdx.x - 256) * 512 + tid) * 16;
        const u32x4 p0 = *(const u32x4*)(wqkv + o), p1 = *(const u32x4*)(w1 + o), p2 = *(const u32x4*)(w3 + o);
        asm volatile("" ::"v"(p0), "v"(p1), "v"(p2));
        return;
    }
    if (blockIdx.x == 0) { ctr[tid] = 0u; ctr[tid + 512] = 0u; }
    const int row = blockIdx.x * 8 + wid;
    const u32x4* src = (const u32x4*)(x + (size_t)row * DM);
    const u32x4 a = src[lane], b = src[64 + lane];
    float v[16];
#pragma unroll
    for (int i = 0; i < 4; ++i) { v[2 * i] = bf_lo(a[i]); v[2 * i + 1] = bf_hi(a[i]); v[8 + 2 * i] = bf_lo(b[i]); v[8 + 2 * i + 1] = bf_hi(b[i]); }
    float s = 0.f;
#pragma unroll
    for (int i = 0; i < 16; ++i) s += v[i];
    s = wave_sum(s);
    const float mean = s * (1.0f / DM);
    float q = 0.f;
#pragma unroll
    for (int i = 0; i < 16; ++i) { const float d = v[i] - mean; q += d * d; }
    q = wave_sum(q);
    const float mb = rbf(mean), vb = rbf(q * (1.0f / DM));
    const float rs = 1.0f / rbf(sqrtf(rbf(vb + 1e-5f)));
    u32x4 oa, ob;
#pragma unroll
    for (int i = 0; i < 4; ++i) {
        oa[i] = cvtpk(rbf(v[2 * i] - mb) * rs, rbf(v[2 * i + 1] - mb) * rs);
        ob[i] = cvtpk(rbf(v[8 + 2 * i] - mb) * rs, rbf(v[8 + 2 * i + 1] - mb) * rs);
    }
    *(u32x4*)(sm + lane * 128 + wid * 16) = oa;
    *(u32x4*)(sm + (64 + lane) * 128 + wid * 16) = ob;
    __syncthreads();
    const int row0 = blockIdx.x * 8;
#pragma unroll
    for (int i = 0; i < 2; ++i) {
        const int p = tid + i * 512, chunk = p >> 3, r = p & 7;
        const u32x4 w = *(const u32x4*)(sm + p * 16);
        *(u32x4*)(xnF + ((size_t)(chunk >> 2) * 128 + (row0 >> 4)) * 1024 + (chunk & 3) * 256 + ((row0 & 15) + r) * 16) = w;
    }
}

__device__ __forceinline__ unsigned silu_mul2(f32x2 g, f32x2 u) {
    const f32x2 t = g * -1.4426950408889634f;
    f32x2 e; e.x = __builtin_amdgcn_exp2f(t.x); e.y = __builtin_amdgcn_exp2f(t.y);
    e = e + 1.0f;
    f32x2 r; r.x = __builtin_amdgcn_rcpf(e.x); r.y = __builtin_amdgcn_rcpf(e.y);
    const f32x2 h = (g * u) * r;
    return cvtpk(h.x, h.y);
}
__device__ __forceinline__ u32x2 silu_mul4(f32x4 g, f32x4 u) {
    return (u32x2){silu_mul2((f32x2){g[0], g[1]}, (f32x2){u[0], u[1]}), silu_mul2((f32x2){g[2], g[3]}, (f32x2){u[2], u[3]})};
}
#define ST16(p, v) asm volatile("global_store_dwordx4 %0, %1, off sc1\n\ts_nop 1" ::"v"(p), "v"(v) : "memory")
__global__ __launch_bounds__(512, 2) void k_gemm1(const char* __restrict__ xnF, const char* __restrict__ wqkv, const char* __restrict__ w1,
                                                  const char* __restrict__ w3, char* __restrict__ qkv, char* __restrict__ act,
                                                  const char* __restrict__ w2, const char* __restrict__ wo) {
    __shared__ __attribute__((aligned(1024))) char smem[147456 + 2048];

    const int tid = threadIdx.x, lane = tid & 63;
    const int wid = __builtin_amdgcn_readfirstlane(tid >> 6), wr = wid & 3, wc = wid >> 2;
    const int bx = blockIdx.x, j = (bx & 7) + 8 * ((bx >> 3) & 3), rb = bx >> 5;
    const unsigned lds0 = (unsigned)(size_t)(LAS char*)smem;
    const bool tq = j < 8;
    const int qb0 = 3 * j, qbx = tq ? 3 * j + 2 : 16 + j, pA = 2 * j - 8, pB = tq ? j : 2 * j - 7;
    const char* bsrc[5]; unsigned binc[5];
#pragma unroll
    for (int i = 0; i < 5; ++i) {
        const int kb = wr, blk = i;
        const int r = lane >> 3, j8 = lane & 7, f = ((r >> 1) & 1) | ((kb & 1) << 1), cp = (j8 >> 1) ^ f;
        const bool ffn = blk >= 3 || (blk < 2 && !tq);
        const int pitch = ffn ? 7168 : 6144;
        const uint64_t wg = (blk == 0 || blk == 3) ? (uint64_t)w1 : (uint64_t)w3, wb = ffn ? wg : (uint64_t)wqkv;
        const int cf = blk < 2 ? pA : pB, cq = blk == 2 ? qbx : qb0 + blk, cblk = ffn ? cf : cq;
        bsrc[i] = (const char*)wb + (size_t)(unsigned)(cblk * 128);
        binc[i] = 32u * pitch;
    }
    unsigned bvq, bvf;
    { const int r = lane >> 3, j8 = lane & 7, cp = (j8 >> 1) ^ (((r >> 1) & 1) | ((wr & 1) << 1));
      bvq = (wr * 8 + r) * 6144 + cp * 32 + (j8 & 1) * 16; bvf = (wr * 8 + r) * 7168 + cp * 32 + (j8 & 1) * 16; }
    const unsigned bv01 = tq ? bvq : bvf;
    const char* asrc = xnF + ((size_t)rb * 16 + 4 * wr) * 1024;
    const unsigned avoff = lane * 16;
    const unsigned adst = lds0 + wr * 4096, bdst = lds0 + 65536 + wr * 5120;
#define G1_DMA_B(SL) do { \
        dma_s1(bv01, bsrc[0], bdst + (SL) * 20480); dma_s1(bv01, bsrc[1], bdst + (SL) * 20480 + 1024); dma_s1(bvq, bsrc[2], bdst + (SL) * 20480 + 2048); \
        dma_s1(bvf, bsrc[3], bdst + (SL) * 20480 + 3072); dma_s1(bvf, bsrc[4], bdst + (SL) * 20480 + 4096); \
        _Pragma("unroll") for (int i = 0; i < 5; ++i) bsrc[i] += binc[i]; } while (0)
#define G1_DMA_A(SL) do { dma_s2(avoff, asrc, adst + (SL) * 16384); dma_s2(avoff, asrc + 2048, adst + (SL) * 16384 + 2048); asrc += 131072; } while (0)
#define G1_DMA(SL, VA) do { if (wc == 0) G1_DMA_B(SL); else { G1_DMA_A(SL); asm volatile("s_waitcnt vmcnt(" #VA ")" ::: "memory"); } } while (0)
    if (wc == 0) { G1_DMA_B(0); G1_DMA_B(1); G1_DMA_B(2); } else { G1_DMA_A(0); G1_DMA_A(1); G1_DMA_A(2); }
    const lds_cptr pa = (lds_cptr)smem + wr * 4096 + lane * 16;
    lds_cptr pb[4], pe0, pe1;
    {
        const int g = lane >> 4, q = (lane >> 2) & 3, p = lane & 3, f = (q >> 1) | ((g & 1) << 1);
        const lds_cptr b0 = (lds_cptr)smem + 65536 + g * 5120 + q * 128 + p * 8;
#pragma unroll
        for (int c = 0; c < 4; ++c) pb[c] = b0 + ((c ^ f) << 5) + wc * 3072;
        pe0 = b0 + ((wc ^ f) << 5) + 2048; pe1 = b0 + (((2 + wc) ^ f) << 5) + 2048;
    }
    f32x4 acc[4][10];
#pragma unroll
    for (int m = 0; m < 4; ++m)
#pragma unroll
        for (int n = 0; n < 10; ++n) { acc[m][n] = (f32x4){0.f, 0.f, 0.f, 0.f}; PIN(acc[m][n]); }
    asm volatile("s_waitcnt vmcnt(8)\n\ts_barrier" ::: "memory");

    if (wc == 1) asm volatile("s_barrier" ::: "memory");
    bf16x8 af[4], bf[10];
#define BFR(P, O) cat8(tr_read((P) + (O)), tr_read((P) + (O) + 512))
#define G1_READS(SL) do { \
        _Pragma("unroll") for (int c = 0; c < 4; ++c) { bf[c] = BFR(pb[c], (SL) * 20480); bf[4 + c] = BFR(pb[c], (SL) * 20480 + 1024); } \
        bf[8] = BFR(pe0, (SL) * 20480); bf[9] = BFR(pe1, (SL) * 20480); \
        _Pragma("unroll") for (int m = 0; m < 4; ++m) af[m] = *(const LAS bf16x8*)(pa + (SL) * 16384 + m * 1024); } while (0)
#define G1_STEP(SL, DMA, VB, ENDBAR) do { \
        G1_READS(SL); DMA; \
        asm volatile("s_waitcnt lgkmcnt(0)" ::: "memory"); \
        asm volatile("s_barrier" ::: "memory"); \
        SCHED(); __builtin_amdgcn_s_setprio(1); \
        _Pragma("unroll") for (int i = 0; i < 40; ++i) { const int m_ = ((i >> 2) & 1) ? 3 - (i & 3) : (i & 3); MF16_ACC(acc[m_][i >> 2], bf[i >> 2], af[m_]); } \
        __builtin_amdgcn_s_setprio(0); SCHED(); \
        if (ENDBAR) asm volatile("s_waitcnt vmcnt(" #VB ")\n\ts_barrier" ::: "memory"); } while (0)
    const char* pf = (wid < 7 ? w2 + (size_t)(bx * 7 + wid) * 4096 : wo + (size_t)bx * 8192);
    for (int it = 0; it < 7; ++it) { G1_STEP(0, G1_DMA(3, 8), 10, true); G1_STEP(1, G1_DMA(0, 8), 10, true); G1_STEP(2, G1_DMA(1, 8), 10, true); G1_STEP(3, G1_DMA(2, 8), 10, true); }
    G1_STEP(0, G1_DMA(3, 8), 10, true);
    G1_STEP(1, { dma_s2(avoff, pf, lds0 + 147456); dma_s2(avoff, pf + 2048, lds0 + 147456); if (wc == 1) asm volatile("s_waitcnt vmcnt(8)" ::: "memory"); }, 9, true);
    G1_STEP(2, { if (wc == 1) asm volatile("s_waitcnt vmcnt(4)" ::: "memory"); }, 4, true);
    G1_STEP(3, { if (wc == 0) dma_s1(avoff, wo + (size_t)bx * 8192 + 4096 + wid * 1024, lds0 + 147456); }, 63, wc == 0);

#undef G1_STEP
#undef G1_READS
#undef BFR
#undef G1_DMA
#undef G1_DMA_A
#undef G1_DMA_B
    asm volatile("s_nop 7" : "+v"(acc[0][9]), "+v"(acc[1][9]), "+v"(acc[2][9]), "+v"(acc[3][9]), "+v"(acc[3][8]), "+v"(acc[2][8]));
    SCHED();
    int tz = threadIdx.x; asm volatile("" : "+v"(tz));
    const int fr = tz & 15, fq = (tz >> 4) & 3;
    const int tb = (rb * 16 + wr * 4) * 1024;
    const bool ffn_main = !tq || wc == 1;
    const int cm = ((fq & 1) << 1) | (fq >> 1), ce = 2 * wc + (fq >> 1);
    {
        const int cb0 = 2 * qbx;
        const float sc = cb0 < 32 ? QSCALE : 1.0f;
        char* dst = qkv + (size_t)(cb0 + (fq & 1)) * 131072 + tb + ce * 256 + ((fr ^ (4 * ce)) * 16);
#pragma unroll
        for (int m = 0; m < 4; ++m) {
            const f32x4 va = acc[m][8] * sc, vb = acc[m][9] * sc;
            unsigned a0 = cvtpk(va[0], va[1]), a1 = cvtpk(va[2], va[3]), b0 = cvtpk(vb[0], vb[1]), b1 = cvtpk(vb[2], vb[3]);
            swap16(a0, b0); swap16(a1, b1);
            ST16(dst + m * 1024, ((u32x4){a0, a1, b0, b1}));
        }
    }
    if (ffn_main) {
        char* dst = act + (size_t)(wc ? pB : pA) * 262144 + tb + cm * 256 + fr * 16;
#pragma unroll
        for (int m = 0; m < 4; ++m)
#pragma unroll
            for (int sub = 0; sub < 2; ++sub) {
                const u32x2 a = silu_mul4(acc[m][2 * sub], acc[m][4 + 2 * sub]), b = silu_mul4(acc[m][2 * sub + 1], acc[m][4 + 2 * sub + 1]);
                unsigned a0 = a[0], a1 = a[1], b0 = b[0], b1 = b[1];
                swap16(a0, b0); swap16(a1, b1);
                ST16(dst + sub * 131072 + m * 1024, ((u32x4){a0, a1, b0, b1}));
            }
    } else {
        const int lo = tb + cm * 256 + ((fr ^ (4 * cm)) * 16);
#pragma unroll
        for (int sub = 0; sub < 4; ++sub) {
            const int cb = 2 * qb0 + sub;
            const float sc = cb < 32 ? QSCALE : 1.0f;
            char* dst = qkv + (size_t)cb * 131072 + lo;
#pragma unroll
            for (int m = 0; m < 4; ++m) {
                const f32x4 va = acc[m][2 * sub] * sc, vb = acc[m][2 * sub + 1] * sc;
                unsigned a0 = cvtpk(va[0], va[1]), a1 = cvtpk(va[2], va[3]), b0 = cvtpk(vb[0], vb[1]), b1 = cvtpk(vb[2], vb[3]);
                swap16(a0, b0); swap16(a1, b1);
                ST16(dst + m * 1024, ((u32x4){a0, a1, b0, b1}));
            }
        }
    }

}

__device__ __forceinline__ void attn_role(char* smem, const int bx, const char* __restrict__ qkv, char* __restrict__ act, unsigned* __restrict__ ctr) {

    const int tid = threadIdx.x, lane = tid & 63;
    const int wid = __builtin_amdgcn_readfirstlane(tid >> 6);
    const int head = (bx & 7) * 4 + (bx >> 6), qb = (bx >> 3) & 7;
    __builtin_amdgcn_s_setprio(2);
    const char* Qf = qkv + (size_t)head * 131072;
    const char* Kf = Qf + 4 * MB;
    const char* Vr = Qf + 8 * MB;
    const unsigned lds0 = (unsigned)(size_t)(LAS char*)smem;
    const unsigned voff = lane * 16;
    const int tq0 = qb * 8 + wid * 2;
    unsigned fo[2];
#pragma unroll
    for (int s = 0; s < 2; ++s) { const int c = 2 * s + (lane >> 5); fo[s] = ((lane >> 4) & 1) * 1024 + c * 256 + (((lane & 15) ^ (4 * c)) * 16); }
    bf16x8 q[2][2];
    asm volatile("global_load_dwordx4 %0, %4, %6\n\tglobal_load_dwordx4 %1, %5, %6\n\tglobal_load_dwordx4 %2, %4, %6 offset:2048\n\tglobal_load_dwordx4 %3, %5, %6 offset:2048"
                 : "=&v"(q[0][0]), "=&v"(q[0][1]), "=&v"(q[1][0]), "=&v"(q[1][1]) : "v"(fo[0]), "v"(fo[1]), "s"(Qf + tq0 * 2048) : "memory");
    bf16x8 sel;
    { const short one = (((lane >> 2) & 1) == ((lane >> 4) & 1)) ? (short)0x3f80 : (short)0; sel = (bf16x8){one, one, one, one, one, one, one, one}; }
    const lds_cptr kp0 = (lds_cptr)smem + fo[0], kp1 = (lds_cptr)smem + fo[1];
    lds_cptr vp0, vp1;
    { const int h = lane >> 5, c = 2 * ((lane >> 4) & 1) + ((lane >> 1) & 1), q4 = (lane >> 2) & 3;
      vp0 = (lds_cptr)smem + 8192 + c * 256 + (h ^ c) * 64 + q4 * 16 + (lane & 1) * 8; vp1 = (lds_cptr)smem + 8192 + c * 256 + ((2 + h) ^ c) * 64 + q4 * 16 + (lane & 1) * 8; }
    const char* Kw = Kf + wid * 2048;
    const char* Vw = Vr + wid * 2048;
    volatile LAS unsigned* flag = (volatile LAS unsigned*)(smem);
    const unsigned ooff = ((lane >> 4) & 1) * 1024 + (lane >> 5) * 256 + (lane & 15) * 16;
#define AT_DMA(ST, SLOT) do { const unsigned d_ = lds0 + (SLOT) + wid * 2048; \
        dma_s2(voff, Kf + (ST) * 8192 + wid * 2048, d_); dma_s2(voff, Vr + (ST) * 8192 + wid * 2048, d_ + 8192); } while (0)
    for (int attempt = 0; attempt < 2; ++attempt) {
        f32x16 o[2]; f32x4 l[2];
        bf16x8 k0, k1;
        asm volatile("global_load_dwordx4 %0, %2, %4\n\tglobal_load_dwordx4 %1, %3, %4" : "=&v"(k0), "=&v"(k1) : "v"(fo[0]), "v"(fo[1]), "s"(Kf) : "memory");
        AT_DMA(0, 0); AT_DMA(1, 16384); AT_DMA(2, 32768);
        o[0] = (f32x16){}; o[1] = (f32x16){}; l[0] = (f32x4){}; l[1] = (f32x4){};
        PIN(o[0]); PIN(o[1]); PIN(l[0]); PIN(l[1]);
        asm volatile("s_waitcnt vmcnt(12)" : "+v"(k0), "+v"(k1), "+v"(q[0][0]), "+v"(q[0][1]), "+v"(q[1][0]), "+v"(q[1][1])::"memory");
        f32x16 negm[2];
        {
            float m0 = -INFINITY, m1 = -INFINITY;
            const int nt = attempt == 0 ? 1 : 64;
            for (int kt = 0; kt < nt; ++kt) {
                if (kt) { k0 = *(const bf16x8*)(Kf + kt * 2048 + fo[0]); k1 = *(const bf16x8*)(Kf + kt * 2048 + fo[1]); }
                f32x16 s0 = MF32(k0, q[0][0], (f32x16){}), s1 = MF32(k0, q[1][0], (f32x16){});
                s0 = MF32(k1, q[0][1], s0); s1 = MF32(k1, q[1][1], s1);
#pragma unroll
                for (int r = 0; r < 16; ++r) { m0 = fmaxf(m0, s0[r]); m1 = fmaxf(m1, s1[r]); }
            }
            m0 = fmaxf(m0, __shfl_xor(m0, 32)); m1 = fmaxf(m1, __shfl_xor(m1, 32));
#pragma unroll
            for (int r = 0; r < 16; ++r) { negm[0][r] = -m0; negm[1][r] = -m1; }
            PIN(negm[0]); PIN(negm[1]);
        }
        asm volatile("s_waitcnt vmcnt(0)\n\ts_barrier" ::: "memory");
        bf16x8 kf0 = *(const LAS bf16x8*)(kp0), kf1 = *(const LAS bf16x8*)(kp1);
        bf16x8 vf0 = (bf16x8){}, vf1 = (bf16x8){};
        u32x4 pa0 = (u32x4){}, pa1 = (u32x4){}, pb0 = (u32x4){}, pb1 = (u32x4){};
        f32x16 sA = MF32(kf0, q[0][0], negm[0]); sA = MF32(kf1, q[0][1], sA);
        f32x16 sB;
#define EX(X, i) X[i] = __builtin_amdgcn_exp2f(X[i])
#define PK(X, i) cvtpk(X[i], X[(i) + 1])
#define BC8(x) __builtin_bit_cast(bf16x8, x)
#define AT_UNIT(SC, PC0, PC1, SN, QN, NM, PP0, PP1, OP, LP, K0, K1, V0, V1, L0, L1, L2, L3) do { unsigned c0, c1, c2, c3, c4, c5, c6, c7; \
            SN = MF32(K0, QN[0], NM); SCHED(); L0; SCHED(); EX(SC, 0); EX(SC, 1); EX(SC, 2); SCHED(); \
            OP = MF32(V0, BC8(PP0), OP); SCHED(); L1; SCHED(); c0 = PK(SC, 0); EX(SC, 3); EX(SC, 4); EX(SC, 5); SCHED(); \
            SN = MF32(K1, QN[1], SN); SCHED(); L2; SCHED(); c1 = PK(SC, 2); c2 = PK(SC, 4); EX(SC, 6); EX(SC, 7); EX(SC, 8); SCHED(); \
            OP = MF32(V1, BC8(PP1), OP); SCHED(); L3; SCHED(); c3 = PK(SC, 6); EX(SC, 9); EX(SC, 10); EX(SC, 11); SCHED(); \
            MF16_ACC(LP, sel, PP0); SCHED(); c4 = PK(SC, 8); c5 = PK(SC, 10); EX(SC, 12); EX(SC, 13); SCHED(); \
            MF16_ACC(LP, sel, PP1); SCHED(); EX(SC, 14); EX(SC, 15); SCHED(); c6 = PK(SC, 12); SCHED(); c7 = PK(SC, 14); \
            PC0 = (u32x4){c0, c1, c2, c3}; PC1 = (u32x4){c4, c5, c6, c7}; SCHED(); } while (0)
#define AT_M0(SLOT, V) asm volatile("s_mov_b32 m0, %0" ::"s"(lds0 + (SLOT) + wid * 2048 + (V) * 8192) : "memory")
#define AT_PAIR(ST, V) asm volatile("global_load_lds_dwordx4 %0, %1\n\tglobal_load_lds_dwordx4 %0, %1 offset:1024" ::"v"(voff), "s"(((V) ? Vw : Kw) + ((ST) & 15) * 8192) : "memory")
#define AT_STAGE(SO, SN_, DM, ST, SLA, SLB) do { _Pragma("unroll") for (int kt = 0; kt < 4; ++kt) { \
                const int ko = kt < 3 ? (SO) + (kt + 1) * 2048 : (SN_), vo = (SO) + kt * 2048; \
                bf16x8 kn0, kn1, vn0, vn1; \
                AT_UNIT(sA, pa0, pa1, sB, q[1], negm[1], pb0, pb1, o[1], l[1], kf0, kf1, vf0, vf1, \
                        (kn0 = *(const LAS bf16x8*)(kp0 + ko), kn1 = *(const LAS bf16x8*)(kp1 + ko)), vn0 = cat8(tr_read(vp0 + vo), tr_read(vp1 + vo)), \
                        vn1 = cat8(tr_read(vp0 + vo + 1024), tr_read(vp1 + vo + 1024)), {}); \
                kf0 = kn0; kf1 = kn1; vf0 = vn0; vf1 = vn1; \
                __builtin_amdgcn_s_waitcnt(0xC07F); SCHED(); \
                AT_UNIT(sB, pb0, pb1, sA, q[0], negm[0], pa0, pa1, o[0], l[0], kf0, kf1, vf0, vf1, \
                        if (DM) AT_M0((kt >> 1) ? (SLB) : (SLA), kt & 1), if (DM) AT_PAIR((ST) + (kt >> 1), kt & 1), {}, {}); } } while (0)
#define NXT(x) ((x) == 65536 ? 0 : (x) + 16384)
        int s0 = 0;
        for (int k = 0; k < 8; ++k) {
            const int s1 = NXT(s0), s2 = NXT(s1), s3 = NXT(s2), s4 = NXT(s3);
            AT_STAGE(s0, s1, true, 2 * k + 3, s3, s4);
            AT_STAGE(s1, s2, false, 0, 0, 0);
            s0 = s2;
            asm volatile("s_waitcnt vmcnt(0)\n\ts_barrier" ::: "memory");
        }
#undef NXT
#undef AT_STAGE
#undef AT_M0
#undef AT_PAIR
        o[1] = MF32(vf0, BC8(pb0), o[1]); o[1] = MF32(vf1, BC8(pb1), o[1]);
        l[1] = MF16(sel, BC8(pb0), l[1]); l[1] = MF16(sel, BC8(pb1), l[1]);
#undef AT_UNIT
        asm volatile("s_nop 7\n\ts_nop 7" : "+v"(l[0]), "+v"(l[1]), "+v"(o[0]), "+v"(o[1]));
        float tsum = 0.f;
#pragma unroll
        for (int i = 0; i < 2; ++i) {
            const float r = 1.0f / l[i][0];
#pragma unroll
            for (int e = 0; e < 16; ++e) { tsum += fabsf(o[i][e]); o[i][e] *= r; }
        }
        const bool bad = !(l[0][0] < 1.0995e12f) || !(l[1][0] < 1.0995e12f) || !(tsum < INFINITY);
        if (lane == 0) flag[wid] = 0u;
        if (bad) flag[wid] = 1u;
#pragma unroll
        for (int i = 0; i < 2; ++i)
#pragma unroll
            for (int s = 0; s < 2; ++s) {
                unsigned a0 = cvtpk(o[i][8 * s + 0], o[i][8 * s + 1]), a1 = cvtpk(o[i][8 * s + 2], o[i][8 * s + 3]);
                unsigned b0 = cvtpk(o[i][8 * s + 4], o[i][8 * s + 5]), b1 = cvtpk(o[i][8 * s + 6], o[i][8 * s + 7]);
                swap32(a0, b0); swap32(a1, b1);
                const u32x4 w = (u32x4){a0, a1, b0, b1};
                asm volatile("global_store_dwordx4 %0, %1, %2 sc1\n\ts_nop 1" ::"v"(ooff), "v"(w), "s"(act + (size_t)(112 + head) * 131072 + (tq0 + i) * 2048 + s * 512) : "memory");
            }
        asm volatile("s_waitcnt vmcnt(0) lgkmcnt(0)\n\ts_barrier" ::: "memory");
        const unsigned redo = flag[0] | flag[1] | flag[2] | flag[3];
        if (attempt == 1 || !__builtin_amdgcn_readfirstlane(redo)) break;
        asm volatile("s_waitcnt lgkmcnt(0)\n\ts_barrier" ::: "memory");
    }
#undef AT_DMA
    if (tid == 0) __hip_atomic_fetch_add(ctr + qb * 64, 1u, __ATOMIC_RELAXED, __HIP_MEMORY_SCOPE_AGENT);

}

__device__ __forceinline__ void gemm2_role(char* smem, const int bx, const char* __restrict__ act, const char* __restrict__ w2, const char* __restrict__ wo,
                                           const char* __restrict__ x, char* __restrict__ out, unsigned* __restrict__ ctr) {

    const int tid = threadIdx.x, lane = tid & 63;
    const int wid = __builtin_amdgcn_readfirstlane(tid >> 6);
    const int xc = bx & 7, ii = bx >> 3, tm = (xc >> 1) * 4 + (ii >> 3), tn = (xc & 1) * 8 + (ii & 7);
    const unsigned lds0 = (unsigned)(size_t)(LAS char*)smem + wid * 16384;
    __builtin_amdgcn_s_sleep(127);
    const char* asrc = act + (size_t)wid * 131072 + tm * 8192;
    unsigned av[4];
#pragma unroll
    for (int i = 0; i < 4; ++i) av[i] = (i ^ wid) * 2048 + lane * 16;
    const char* wsrc = w2 + (size_t)wid * 32 * 2048 + tn * 128;
    const unsigned wv0 = (lane >> 3) * 2048 + ((((lane >> 1) & 3) ^ ((lane >> 4) & 1)) * 32) + (lane & 1) * 16;
    const unsigned wv1 = (lane >> 3) * 2048 + ((((lane >> 1) & 3) ^ ((lane >> 4) & 1) ^ 2) * 32) + (lane & 1) * 16 + 16384 - 1024;
    lds_cptr wp[4];
    {
        const int g = lane >> 4, q = (lane >> 2) & 3, p = lane & 3, f = (q >> 1) | ((g & 1) << 1);
#pragma unroll
        for (int c = 0; c < 4; ++c) wp[c] = (lds_cptr)smem + wid * 16384 + g * 1024 + q * 128 + ((c ^ f) << 5) + p * 8;
    }
    f32x4 acc[8][4];
#pragma unroll
    for (int i = 0; i < 8; ++i)
#pragma unroll
        for (int n = 0; n < 4; ++n) { acc[i][n] = (f32x4){}; PIN(acc[i][n]); }
    bf16x8 a0[4], a1[4], a2[4], a3[4], wfa[4], wfb[4];
#define G2_LDA(A, H) do { \
        asm volatile("global_load_dwordx4 %0, %4, %6\n\tglobal_load_dwordx4 %1, %4, %6 offset:1024\n\tglobal_load_dwordx4 %2, %5, %6\n\tglobal_load_dwordx4 %3, %5, %6 offset:1024" \
                     : "=&v"(A[0]), "=&v"(A[1]), "=&v"(A[2]), "=&v"(A[3]) : "v"(av[2 * (H)]), "v"(av[2 * (H) + 1]), "s"(asrc) : "memory"); \
        if (H) asrc += 524288; } while (0)
#define G2_LDW(SL) do {     \
        asm volatile("s_mov_b32 m0, %3\n\ts_nop 0\n\tglobal_load_lds_dwordx4 %0, %2\n\tglobal_load_lds_dwordx4 %1, %2 offset:1024" \
                     ::"v"(wv0), "v"(wv1), "s"(wsrc), "s"(lds0 + (SL) * 2048) : "memory"); \
        wsrc += ((SL) & 1) ? 262144 - 32768 : 32768; } while (0)
#define G2_WAIT(N, A) asm volatile("s_waitcnt vmcnt(" #N ")" : "+v"(A[0]), "+v"(A[1]), "+v"(A[2]), "+v"(A[3])::"memory")
#define WFR(P, O) cat8(tr_read((P) + (O)), tr_read((P) + (O) + 512))
#define G2_STEP(N, AC, AN, WC, WN, SL, RD, IA, IW) do { \
        if ((N) >= 0) G2_WAIT(N, AN); \
        if (RD) { WN[2 * ((SL) & 1)] = WFR(wp[2 * ((SL) & 1)], ((((SL) >> 1) + 1) & 3) * 4096); WN[2 * ((SL) & 1) + 1] = WFR(wp[2 * ((SL) & 1) + 1], ((((SL) >> 1) + 1) & 3) * 4096); } \
        SCHED(); \
        _Pragma("unroll") for (int i = 0; i < 16; ++i) MF16_ACC(acc[4 * ((SL) & 1) + (i >> 2)][((i >> 2) & 1) ? 3 - (i & 3) : (i & 3)], WC[((i >> 2) & 1) ? 3 - (i & 3) : (i & 3)], AC[i >> 2]); \
        SCHED(); if (IA) G2_LDA(AC, (SL) & 1); if (IW) G2_LDW(SL); } while (0)
#define G2_STEP4(N0, N1, N2, N3, SL, RDL, IA, IW) do { \
        G2_STEP(N0, a0, a1, wfa, wfb, (SL), true, IA, IW); G2_STEP(N1, a1, a2, wfa, wfb, (SL) + 1, true, IA, IW); \
        G2_STEP(N2, a2, a3, wfb, wfa, (SL) + 2, RDL, IA, IW); G2_STEP(N3, a3, a0, wfb, wfa, (SL) + 3, RDL, IA, IW); } while (0)
    G2_LDW(0); G2_LDW(1); G2_LDW(2); G2_LDW(3);
    G2_LDA(a0, 0); G2_LDW(4); G2_LDA(a1, 1); G2_LDW(5); G2_LDA(a2, 0); G2_LDW(6); G2_LDA(a3, 1); G2_LDW(7);
    G2_WAIT(20, a0);
#pragma unroll
    for (int c = 0; c < 4; ++c) wfa[c] = WFR(wp[c], 0);
    for (int it = 0; it < 6; ++it) { G2_STEP4(14, 14, 14, 14, 0, true, true, true); G2_STEP4(14, 14, 14, 14, 4, true, true, true); }
    G2_STEP4(14, 12, 10, 8, 0, true, true, false);
    G2_STEP4(8, 4, 0, -1, 4, false, false, false);
#undef G2_STEP4
#undef G2_STEP
#undef G2_WAIT
#undef G2_LDW
#undef G2_LDA
    const int fr = lane & 15, fq = lane >> 4;
    const size_t orow = (size_t)(tm * 128 + wid * 32 + fr) * 2048 + tn * 128 + (fq & 1) * 32 + (fq >> 1) * 16;
    u32x4 xr[4];
#pragma unroll
    for (int c = 0; c < 4; ++c) xr[c] = *(const u32x4*)(x + orow + (c >> 1) * 32768 + (c & 1) * 64);
    asm volatile("s_nop 7" : "+v"(acc[7][0]), "+v"(acc[7][1]), "+v"(acc[7][2]), "+v"(acc[7][3]));
    SCHED();
    char* mine = smem + wid * 16384 + lane * 16;
#pragma unroll
    for (int i = 0; i < 16; ++i) *(f32x4*)(mine + i * 1024) = acc[4 + (i >> 2)][i & 3];
    __syncthreads();
#pragma unroll
    for (int i = 0; i < 16; ++i) acc[i >> 2][i & 3] += *(const f32x4*)(smem + (wid ^ 2) * 16384 + lane * 16 + i * 1024);
    __syncthreads();
#pragma unroll
    for (int i = 0; i < 8; ++i) *(f32x4*)(mine + i * 1024) = acc[2 + (i >> 2)][i & 3];
    __syncthreads();
#pragma unroll
    for (int i = 0; i < 8; ++i) { acc[i >> 2][i & 3] += *(const f32x4*)(smem + (wid ^ 1) * 16384 + lane * 16 + i * 1024); PIN(acc[i >> 2][i & 3]); }
    __syncthreads();
    const unsigned ldsb = (unsigned)(size_t)(LAS char*)smem;
    const char* wos = wo + tn * 128;
    const char* osrc = act + (size_t)112 * 131072 + tm * 8192 + wid * 2048;
    unsigned ov = lane * 16;
    lds_cptr wq[4];
#pragma unroll
    for (int c = 0; c < 4; ++c) { wq[c] = wp[c] - wid * 16384; PIN(wq[c]); }
    PIN(ov);
#define G3_LDW(HS, SLOT) asm volatile("s_mov_b32 m0, %3\n\ts_nop 0\n\tglobal_load_lds_dwordx4 %0, %2\n\tglobal_load_lds_dwordx4 %1, %2 offset:1024" \
                     ::"v"(wv0), "v"(wv1), "s"(wos + (size_t)(HS) * 32768), "s"(ldsb + (SLOT) * 2048) : "memory")
#pragma unroll
    for (int i = 0; i < 10; ++i) G3_LDW(wid * 10 + i, wid * 10 + i);
    if (wid == 0) {
        const unsigned long long t0 = __builtin_amdgcn_s_memrealtime();
        while ((unsigned)__builtin_amdgcn_readfirstlane(__hip_atomic_load(ctr + (tm >> 1) * 64, __ATOMIC_RELAXED, __HIP_MEMORY_SCOPE_AGENT)) < 32u) {
            if (__builtin_amdgcn_s_memrealtime() - t0 > 5000000ull) break;
            __builtin_amdgcn_s_sleep(2);
        }
        __builtin_amdgcn_fence(__ATOMIC_ACQUIRE, "wavefront");
        asm volatile("" ::: "memory");
    }
    asm volatile("s_waitcnt vmcnt(0)\n\ts_barrier" ::: "memory");

    bf16x8 oa[8][4];
#define G3_LDA(G) asm volatile("global_load_dwordx4 %0, %4, %5 sc1\n\tglobal_load_dwordx4 %1, %4, %5 offset:1024 sc1\n\tglobal_load_dwordx4 %2, %4, %6 sc1\n\tglobal_load_dwordx4 %3, %4, %6 offset:1024 sc1" \
                     : "=&v"(oa[(G) & 7][0]), "=&v"(oa[(G) & 7][1]), "=&v"(oa[(G) & 7][2]), "=&v"(oa[(G) & 7][3]) \
                     : "v"(ov), "s"(osrc + (size_t)(G) * 262144), "s"(osrc + (size_t)(G) * 262144 + 131072) : "memory")
#define G3_WAIT(N, G) asm volatile("s_waitcnt vmcnt(" #N ")" : "+v"(oa[(G) & 7][0]), "+v"(oa[(G) & 7][1]), "+v"(oa[(G) & 7][2]), "+v"(oa[(G) & 7][3])::"memory")
    s16x4 wl[2][8], wh[2][8];
#define G3_WA(G, i) (wq[(i) & 3] + (((G) >= 8 && (G) < 10) ? 65536 : 0) + ((((G) * 2 + ((i) >> 2)) % 20) * 4096 - (((G) >= 8 && (G) < 10) ? 65536 : 0)))
#define G3_LD1(G, j) asm volatile("global_load_dwordx4 %0, %1, %2 offset:%c3 sc1" : "=&v"(oa[(G) & 7][j]) : "v"(ov), "s"(osrc + (size_t)(G) * 262144 + ((j) >> 1) * 131072), "i"(((j) & 1) * 1024) : "memory")
#define G3_GROUP(N, G, RD) do { G3_WAIT(N, G); SCHED(); \
        const bool rf_ = (G) == 5 || (G) == 6; \
        _Pragma("unroll") for (int i = 0; i < 8; ++i) { const bf16x8 w_ = cat8(wl[(G) & 1][i], wh[(G) & 1][i]); \
            MF16_ACC(acc[0][i & 3], w_, oa[(G) & 7][2 * (i >> 2)]); SCHED(); if (RD) wl[((G) + 1) & 1][i] = tr_read(G3_WA((G) + 1, i)); SCHED(); \
            MF16_ACC(acc[1][i & 3], w_, oa[(G) & 7][2 * (i >> 2) + 1]); SCHED(); if (RD) wh[((G) + 1) & 1][i] = tr_read(G3_WA((G) + 1, i) + 512); \
            if (rf_ && !(i & 1) && i < 6) G3_LDW(40 + wid * 6 + ((G) - 5) * 3 + (i >> 1), wid * 6 + ((G) - 5) * 3 + (i >> 1)); \
            if ((G) < 11 && (i & 1)) G3_LD1((G) + 5, i >> 1); SCHED(); } } while (0)
    G3_LDA(0); G3_LDA(1); G3_LDA(2); G3_LDA(3); G3_LDA(4);
#pragma unroll
    for (int i = 0; i < 8; ++i) { wl[0][i] = tr_read(G3_WA(0, i)); wh[0][i] = tr_read(G3_WA(0, i) + 512); }
    G3_GROUP(16, 0, true); G3_GROUP(16, 1, true); G3_GROUP(16, 2, true); G3_GROUP(16, 3, true); G3_GROUP(16, 4, true);

    asm volatile("s_waitcnt lgkmcnt(0)\n\ts_barrier" ::: "memory");
    G3_GROUP(16, 5, true); G3_GROUP(22, 6, true); G3_GROUP(28, 7, true); G3_GROUP(28, 8, true);

    asm volatile("s_waitcnt vmcnt(10)\n\ts_barrier" ::: "memory");

    G3_GROUP(10, 9, true); G3_GROUP(22, 10, true); G3_GROUP(16, 11, true); G3_GROUP(12, 12, true); G3_GROUP(8, 13, true); G3_GROUP(4, 14, true); G3_GROUP(0, 15, false);

#undef G3_GROUP
#undef G3_WA
#undef G3_WAIT
#undef G3_LDA
#undef G3_LD1
#undef G3_LDW
#undef WFR
    asm volatile("s_nop 7" : "+v"(acc[0][0]), "+v"(acc[0][1]), "+v"(acc[0][2]), "+v"(acc[0][3]), "+v"(acc[1][0]), "+v"(acc[1][1]), "+v"(acc[1][2]), "+v"(acc[1][3]));
#pragma unroll
    for (int c = 0; c < 4; ++c) {
        float v[8];
#pragma unroll
        for (int e = 0; e < 4; ++e) {
            unsigned a = __float_as_uint(acc[c >> 1][2 * (c & 1)][e]), b = __float_as_uint(acc[c >> 1][2 * (c & 1) + 1][e]);
            swap16(a, b);
            v[e] = __uint_as_float(a); v[4 + e] = __uint_as_float(b);
        }
        u32x4 w;
#pragma unroll
        for (int e = 0; e < 4; ++e) w[e] = cvtpk(v[2 * e] + bf_lo(xr[c][e]), v[2 * e + 1] + bf_hi(xr[c][e]));
        *(u32x4*)(out + orow + (c >> 1) * 32768 + (c & 1) * 64) = w;
    }

}

__global__ __launch_bounds__(256, 2) void k_fused(const char* __restrict__ qkv, char* __restrict__ act, const char* __restrict__ w2, const char* __restrict__ wo,
                                                  const char* __restrict__ x, char* __restrict__ out, unsigned* __restrict__ ctr) {
    __shared__ __attribute__((aligned(1024))) char smem[81920];
    asm volatile("" ::"s"(qkv), "s"(act), "s"(w2));
    if (__builtin_expect(blockIdx.x < 256, 1)) attn_role(smem, blockIdx.x, qkv, act, ctr);
    else gemm2_role(smem, blockIdx.x - 256, act, w2, wo, x, out, ctr);
}

extern "C" void kernel_launch(void* const* d_in, const int* in_sizes, int n_in, void* d_out, int out_size, void* d_ws, size_t ws_size,
                              hipStream_t stream) {
    const u16* x = (const u16*)d_in[0];
    const char* wqkv = (const char*)d_in[1];
    const char* wo = (const char*)d_in[2];
    const char* w1 = (const char*)d_in[4];
    const char* w3 = (const char*)d_in[5];
    const char* w2 = (const char*)d_in[6];
    char* ws = (char*)d_ws;
    k_ln<<<512, 512, 0, stream>>>(x, ws + WS_XN, (unsigned*)(ws + WS_CTR), wqkv, w1, w3);
    k_gemm1<<<256, 512, 0, stream>>>(ws + WS_XN, wqkv, w1, w3, ws + WS_QKV, ws + WS_ACT, w2, wo);
    k_fused<<<512, 256, 0, stream>>>(ws + WS_QKV, ws + WS_ACT, w2, wo, (const char*)x, (char*)d_out, (unsigned*)(ws + WS_CTR));
}
```

```cpp
#include <hip/hip_runtime.h>
#include <stdint.h>

typedef unsigned short u16;
typedef short bf16x8 __attribute__((ext_vector_type(8)));
typedef short s16x4 __attribute__((ext_vector_type(4)));
typedef float f32x2 __attribute__((ext_vector_type(2)));
typedef float f32x4 __attribute__((ext_vector_type(4)));
typedef float f32x16 __attribute__((ext_vector_type(16)));
typedef unsigned u32x2 __attribute__((ext_vector_type(2)));
typedef unsigned u32x4 __attribute__((ext_vector_type(4)));
typedef __bf16 bf16x2_t __attribute__((ext_vector_type(2)));
#define LAS __attribute__((address_space(3)))
typedef const LAS char* lds_cptr;

constexpr int SEQ = 2048, DM = 1024, FF = 3584, NH = 32;
constexpr size_t MB = 1024 * 1024;
constexpr size_t WS_CTR = 0;
constexpr size_t WS_XN = 65536;
constexpr size_t WS_QKV = WS_XN + 4 * MB;
constexpr size_t WS_ACT = WS_QKV + 12 * MB;
constexpr float QSCALE = 0.17677669529663687f * 1.4426950408889634f;

#define SCHED() __builtin_amdgcn_sched_barrier(0)
#define PIN(x) asm volatile("" : "+v"(x))
#define MF32(a, b, c) __builtin_amdgcn_mfma_f32_32x32x16_bf16(a, b, c, 0, 0, 0)
#define MF16(a, b, c) __builtin_amdgcn_mfma_f32_16x16x32_bf16(a, b, c, 0, 0, 0)

#define MF16_ACC(acc, a, b) asm volatile("v_mfma_f32_16x16x32_bf16 %0, %1, %2, %0" : "+v"(acc) : "v"(a), "v"(b))

__device__ __forceinline__ unsigned cvtpk(float lo, float hi) {
    f32x2 v = {lo, hi};
    bf16x2_t b = __builtin_convertvector(v, bf16x2_t);
    return __builtin_bit_cast(unsigned, b);
}
__device__ __forceinline__ float bf_lo(unsigned w) { return __uint_as_float(w << 16); }
__device__ __forceinline__ float bf_hi(unsigned w) { return __uint_as_float(w & 0xffff0000u); }
__device__ __forceinline__ float rbf(float x) { return bf_lo(cvtpk(x, x)); }
__device__ __forceinline__ s16x4 tr_read(lds_cptr p) {
    return __builtin_bit_cast(s16x4, __builtin_amdgcn_ds_read_tr16_b64_v4i16((LAS s16x4*)p));
}
__device__ __forceinline__ bf16x8 cat8(s16x4 lo, s16x4 hi) { return __builtin_shufflevector(lo, hi, 0, 1, 2, 3, 4, 5, 6, 7); }
__device__ __forceinline__ void dma_v(const void* g, unsigned lds) {
    asm volatile("s_mov_b32 m0, %1\n\ts_nop 0\n\tglobal_load_lds_dwordx4 %0, off" ::"v"(g), "s"(lds) : "memory");
}
__device__ __forceinline__ void dma_s1(unsigned voff, const void* sbase, unsigned lds) {
    asm volatile("s_mov_b32 m0, %2\n\ts_nop 0\n\tglobal_load_lds_dwordx4 %0, %1" ::"v"(voff), "s"(sbase), "s"(lds) : "memory");
}
__device__ __forceinline__ void dma_s2(unsigned voff, const void* sbase, unsigned lds) {
    asm volatile("s_mov_b32 m0, %2\n\ts_nop 0\n\tglobal_load_lds_dwordx4 %0, %1\n\tglobal_load_lds_dwordx4 %0, %1 offset:1024" ::"v"(voff), "s"(sbase), "s"(lds) : "memory");
}
__device__ __forceinline__ void swap16(unsigned& a, unsigned& b) {
    auto r = __builtin_amdgcn_permlane16_swap(a, b, false, false);
    a = r[0]; b = r[1];
}
__device__ __forceinline__ void swap32(unsigned& a, unsigned& b) {
    auto r = __builtin_amdgcn_permlane32_swap(a, b, false, false);
    a = r[0]; b = r[1];
}

__device__ __forceinline__ float wave_sum(float v) {
    v += __builtin_bit_cast(float, __builtin_amdgcn_update_dpp(0, __builtin_bit_cast(int, v), 0xB1, 0xf, 0xf, true));
    v += __builtin_bit_cast(float, __builtin_amdgcn_update_dpp(0, __builtin_bit_cast(int, v), 0x4E, 0xf, 0xf, true));
    v += __builtin_bit_cast(float, __builtin_amdgcn_update_dpp(0, __builtin_bit_cast(int, v), 0x141, 0xf, 0xf, true));
    v += __builtin_bit_cast(float, __builtin_amdgcn_update_dpp(0, __builtin_bit_cast(int, v), 0x140, 0xf, 0xf, true));
    auto a = __builtin_amdgcn_permlane16_swap(__float_as_uint(v), __float_as_uint(v), false, false);
    v = __uint_as_float(a[0]) + __uint_as_float(a[1]);
    auto b = __builtin_amdgcn_permlane32_swap(__float_as_uint(v), __float_as_uint(v), false, false);
    return __uint_as_float(b[0]) + __uint_as_float(b[1]);
}
__device__ __forceinline__ const char* code_after_ln() {
    uint64_t p;
    asm volatile("s_getpc_b64 s[20:21]\n\ts_add_u32 s20, s20, _Z7k_gemm1PKcS0_S0_S0_PcS1_S0_S0_@rel32@lo+4\n\ts_addc_u32 s21, s21, _Z7k_gemm1PKcS0_S0_S0_PcS1_S0_S0_@rel32@hi+12\n\ts_mov_b64 %0, s[20:21]"
                 : "=s"(p)::"s20", "s21", "scc");
    return (const char*)p;
}
__global__ __launch_bounds__(512) void k_ln(const u16* __restrict__ x, char* __restrict__ xnF, unsigned* __restrict__ ctr,
                                            const char* __restrict__ wqkv, const char* __restrict__ w1, const char* __restrict__ w3) {
    __shared__ __attribute__((aligned(16))) char sm[16384];
    const int tid = threadIdx.x, lane = tid & 63, wid = tid >> 6;
    if (blockIdx.x >= 256) {
        const size_t o = (size_t)((blockIdx.x - 256) * 512 + tid) * 16;
        const u32x4 p0 = *(const u32x4*)(wqkv + o), p1 = *(const u32x4*)(w1 + o), p2 = *(const u32x4*)(w3 + o);
        u32x4 p3 = p0;
        if (tid < 64 && ((blockIdx.x - 256) >> 3) < 31) p3 = *(const __attribute__((address_space(1))) u32x4*)(uint64_t)(code_after_ln() + ((blockIdx.x - 256) >> 3) * 1024 + tid * 16);
        asm volatile("" ::"v"(p0), "v"(p1), "v"(p2), "v"(p3));
        return;
    }
    if (blockIdx.x == 0) { ctr[tid] = 0u; ctr[tid + 512] = 0u; }
    const int row = blockIdx.x * 8 + wid;
    const u32x4* src = (const u32x4*)(x + (size_t)row * DM);
    const u32x4 a = src[lane], b = src[64 + lane];
    float v[16];
#pragma unroll
    for (int i = 0; i < 4; ++i) { v[2 * i] = bf_lo(a[i]); v[2 * i + 1] = bf_hi(a[i]); v[8 + 2 * i] = bf_lo(b[i]); v[8 + 2 * i + 1] = bf_hi(b[i]); }
    float s = 0.f;
#pragma unroll
    for (int i = 0; i < 16; ++i) s += v[i];
    s = wave_sum(s);
    const float mean = s * (1.0f / DM);
    float q = 0.f;
#pragma unroll
    for (int i = 0; i < 16; ++i) { const float d = v[i] - mean; q += d * d; }
    q = wave_sum(q);
    const float mb = rbf(mean), vb = rbf(q * (1.0f / DM));
    const float rs = 1.0f / rbf(sqrtf(rbf(vb + 1e-5f)));
    u32x4 oa, ob;
#pragma unroll
    for (int i = 0; i < 4; ++i) {
        oa[i] = cvtpk(rbf(v[2 * i] - mb) * rs, rbf(v[2 * i + 1] - mb) * rs);
        ob[i] = cvtpk(rbf(v[8 + 2 * i] - mb) * rs, rbf(v[8 + 2 * i + 1] - mb) * rs);
    }
    *(u32x4*)(sm + lane * 128 + wid * 16) = oa;
    *(u32x4*)(sm + (64 + lane) * 128 + wid * 16) = ob;
    __syncthreads();
    const int row0 = blockIdx.x * 8;
#pragma unroll
    for (int i = 0; i < 2; ++i) {
        const int p = tid + i * 512, chunk = p >> 3, r = p & 7;
        const u32x4 w = *(const u32x4*)(sm + p * 16);
        *(u32x4*)(xnF + ((size_t)(chunk >> 2) * 128 + (row0 >> 4)) * 1024 + (chunk & 3) * 256 + ((row0 & 15) + r) * 16) = w;
    }
}

__device__ __forceinline__ unsigned silu_mul2(f32x2 g, f32x2 u) {
    const f32x2 t = g * -1.4426950408889634f;
    f32x2 e; e.x = __builtin_amdgcn_exp2f(t.x); e.y = __builtin_amdgcn_exp2f(t.y);
    e = e + 1.0f;
    f32x2 r; r.x = __builtin_amdgcn_rcpf(e.x); r.y = __builtin_amdgcn_rcpf(e.y);
    const f32x2 h = (g * u) * r;
    return cvtpk(h.x, h.y);
}
__device__ __forceinline__ u32x2 silu_mul4(f32x4 g, f32x4 u) {
    return (u32x2){silu_mul2((f32x2){g[0], g[1]}, (f32x2){u[0], u[1]}), silu_mul2((f32x2){g[2], g[3]}, (f32x2){u[2], u[3]})};
}
#define ST16(p, v) asm volatile("global_store_dwordx4 %0, %1, off sc1\n\ts_nop 1" ::"v"(p), "v"(v) : "memory")
__global__ __launch_bounds__(512, 2) void k_gemm1(const char* __restrict__ xnF, const char* __restrict__ wqkv, const char* __restrict__ w1,
                                                  const char* __restrict__ w3, char* __restrict__ qkv, char* __restrict__ act,
                                                  const char* __restrict__ w2, const char* __restrict__ wo) {
    __shared__ __attribute__((aligned(1024))) char smem[147456 + 2048];

    const int tid = threadIdx.x, lane = tid & 63;
    const int wid = __builtin_amdgcn_readfirstlane(tid >> 6), wr = wid & 3, wc = wid >> 2;
    const int bx = blockIdx.x, j = (bx & 7) + 8 * ((bx >> 3) & 3), rb = bx >> 5;
    const unsigned lds0 = (unsigned)(size_t)(LAS char*)smem;
    const bool tq = j < 8;
    const int qb0 = 3 * j, qbx = tq ? 3 * j + 2 : 16 + j, pA = 2 * j - 8, pB = tq ? j : 2 * j - 7;
    const char* bsrc[5]; unsigned binc[5];
#pragma unroll
    for (int i = 0; i < 5; ++i) {
        const int kb = wr, blk = i;
        const int r = lane >> 3, j8 = lane & 7, f = ((r >> 1) & 1) | ((kb & 1) << 1), cp = (j8 >> 1) ^ f;
        const bool ffn = blk >= 3 || (blk < 2 && !tq);
        const int pitch = ffn ? 7168 : 6144;
        const uint64_t wg = (blk == 0 || blk == 3) ? (uint64_t)w1 : (uint64_t)w3, wb = ffn ? wg : (uint64_t)wqkv;
        const int cf = blk < 2 ? pA : pB, cq = blk == 2 ? qbx : qb0 + blk, cblk = ffn ? cf : cq;
        bsrc[i] = (const char*)wb + (size_t)(unsigned)(cblk * 128);
        binc[i] = 32u * pitch;
    }
    unsigned bvq, bvf;
    { const int r = lane >> 3, j8 = lane & 7, cp = (j8 >> 1) ^ (((r >> 1) & 1) | ((wr & 1) << 1));
      bvq = (wr * 8 + r) * 6144 + cp * 32 + (j8 & 1) * 16; bvf = (wr * 8 + r) * 7168 + cp * 32 + (j8 & 1) * 16; }
    const unsigned bv01 = tq ? bvq : bvf;
    const char* asrc = xnF + ((size_t)rb * 16 + 4 * wr) * 1024;
    const unsigned avoff = lane * 16;
    const unsigned adst = lds0 + wr * 4096, bdst = lds0 + 65536 + wr * 5120;
#define G1_DMA_B(SL) do { \
        dma_s1(bv01, bsrc[0], bdst + (SL) * 20480); dma_s1(bv01, bsrc[1], bdst + (SL) * 20480 + 1024); dma_s1(bvq, bsrc[2], bdst + (SL) * 20480 + 2048); \
        dma_s1(bvf, bsrc[3], bdst + (SL) * 20480 + 3072); dma_s1(bvf, bsrc[4], bdst + (SL) * 20480 + 4096); \
        _Pragma("unroll") for (int i = 0; i < 5; ++i) bsrc[i] += binc[i]; } while (0)
#define G1_DMA_A(SL) do { dma_s2(avoff, asrc, adst + (SL) * 16384); dma_s2(avoff, asrc + 2048, adst + (SL) * 16384 + 2048); asrc += 131072; } while (0)
#define G1_DMA(SL, VA) do { if (wc == 0) G1_DMA_B(SL); else { G1_DMA_A(SL); asm volatile("s_waitcnt vmcnt(" #VA ")" ::: "memory"); } } while (0)
    if (wc == 0) { G1_DMA_B(0); G1_DMA_B(1); G1_DMA_B(2); } else { G1_DMA_A(0); G1_DMA_A(1); G1_DMA_A(2); }
    const lds_cptr pa = (lds_cptr)smem + wr * 4096 + lane * 16;
    lds_cptr pb[4], pe0, pe1;
    {
        const int g = lane >> 4, q = (lane >> 2) & 3, p = lane & 3, f = (q >> 1) | ((g & 1) << 1);
        const lds_cptr b0 = (lds_cptr)smem + 65536 + g * 5120 + q * 128 + p * 8;
#pragma unroll
        for (int c = 0; c < 4; ++c) pb[c] = b0 + ((c ^ f) << 5) + wc * 3072;
        pe0 = b0 + ((wc ^ f) << 5) + 2048; pe1 = b0 + (((2 + wc) ^ f) << 5) + 2048;
    }
    f32x4 acc[4][10];
#pragma unroll
    for (int m = 0; m < 4; ++m)
#pragma unroll
        for (int n = 0; n < 10; ++n) { acc[m][n] = (f32x4){0.f, 0.f, 0.f, 0.f}; PIN(acc[m][n]); }
    asm volatile("s_waitcnt vmcnt(8)\n\ts_barrier" ::: "memory");

    if (wc == 1) asm volatile("s_barrier" ::: "memory");
    bf16x8 af[4], bf[10];
#define BFR(P, O) cat8(tr_read((P) + (O)), tr_read((P) + (O) + 512))
#define G1_READS(SL) do { \
        _Pragma("unroll") for (int c = 0; c < 4; ++c) { bf[c] = BFR(pb[c], (SL) * 20480); bf[4 + c] = BFR(pb[c], (SL) * 20480 + 1024); } \
        bf[8] = BFR(pe0, (SL) * 20480); bf[9] = BFR(pe1, (SL) * 20480); \
        _Pragma("unroll") for (int m = 0; m < 4; ++m) af[m] = *(const LAS bf16x8*)(pa + (SL) * 16384 + m * 1024); } while (0)
#define G1_STEP(SL, DMA, VB, ENDBAR) do { \
        G1_READS(SL); DMA; \
        asm volatile("s_waitcnt lgkmcnt(0)" ::: "memory"); \
        asm volatile("s_barrier" ::: "memory"); \
 SCHED(); __builtin_amdgcn_s_setprio(1); \
        _Pragma("unroll") for (int i = 0; i < 40; ++i) { const int m_ = ((i >> 2) & 1) ? 3 - (i & 3) : (i & 3); MF16_ACC(acc[m_][i >> 2], bf[i >> 2], af[m_]); } \
        __builtin_amdgcn_s_setprio(0); SCHED(); \
        if (ENDBAR) asm volatile("s_waitcnt vmcnt(" #VB ")\n\ts_barrier" ::: "memory"); } while (0)
    const char* pf = (wid < 7 ? w2 + (size_t)(bx * 7 + wid) * 4096 : wo + (size_t)bx * 8192);
    for (int it = 0; it < 7; ++it) { G1_STEP(0, G1_DMA(3, 8), 10, true); G1_STEP(1, G1_DMA(0, 8), 10, true); G1_STEP(2, G1_DMA(1, 8), 10, true); G1_STEP(3, G1_DMA(2, 8), 10, true); }
    G1_STEP(0, G1_DMA(3, 8), 10, true);
    G1_STEP(1, { dma_s2(avoff, pf, lds0 + 147456); dma_s2(avoff, pf + 2048, lds0 + 147456); if (wc == 1) asm volatile("s_waitcnt vmcnt(8)" ::: "memory"); }, 9, true);
    G1_STEP(2, { if (wc == 1) asm volatile("s_waitcnt vmcnt(4)" ::: "memory"); }, 4, true);
    G1_STEP(3, { if (wc == 0) dma_s1(avoff, wo + (size_t)bx * 8192 + 4096 + wid * 1024, lds0 + 147456); }, 63, wc == 0);

#undef G1_STEP
#undef G1_READS
#undef BFR
#undef G1_DMA
#undef G1_DMA_A
#undef G1_DMA_B
    asm volatile("s_nop 7" : "+v"(acc[0][9]), "+v"(acc[1][9]), "+v"(acc[2][9]), "+v"(acc[3][9]), "+v"(acc[3][8]), "+v"(acc[2][8]));
    SCHED();
    int tz = threadIdx.x; asm volatile("" : "+v"(tz));
    const int fr = tz & 15, fq = (tz >> 4) & 3;
    const int tb = (rb * 16 + wr * 4) * 1024;
    const bool ffn_main = !tq || wc == 1;
    const int cm = ((fq & 1) << 1) | (fq >> 1), ce = 2 * wc + (fq >> 1);
    {
        const int cb0 = 2 * qbx;
        const float sc = cb0 < 32 ? QSCALE : 1.0f;
        char* dst = qkv + (size_t)(cb0 + (fq & 1)) * 131072 + tb + ce * 256 + ((fr ^ (4 * ce)) * 16);
#pragma unroll
        for (int m = 0; m < 4; ++m) {
            const f32x4 va = acc[m][8] * sc, vb = acc[m][9] * sc;
            unsigned a0 = cvtpk(va[0], va[1]), a1 = cvtpk(va[2], va[3]), b0 = cvtpk(vb[0], vb[1]), b1 = cvtpk(vb[2], vb[3]);
            swap16(a0, b0); swap16(a1, b1);
            ST16(dst + m * 1024, ((u32x4){a0, a1, b0, b1}));
        }
    }
    if (ffn_main) {
        char* dst = act + (size_t)(wc ? pB : pA) * 262144 + tb + cm * 256 + fr * 16;
#pragma unroll
        for (int m = 0; m < 4; ++m)
#pragma unroll
            for (int sub = 0; sub < 2; ++sub) {
                const u32x2 a = silu_mul4(acc[m][2 * sub], acc[m][4 + 2 * sub]), b = silu_mul4(acc[m][2 * sub + 1], acc[m][4 + 2 * sub + 1]);
                unsigned a0 = a[0], a1 = a[1], b0 = b[0], b1 = b[1];
                swap16(a0, b0); swap16(a1, b1);
                ST16(dst + sub * 131072 + m * 1024, ((u32x4){a0, a1, b0, b1}));
            }
    } else {
        const int lo = tb + cm * 256 + ((fr ^ (4 * cm)) * 16);
#pragma unroll
        for (int sub = 0; sub < 4; ++sub) {
            const int cb = 2 * qb0 + sub;
            const float sc = cb < 32 ? QSCALE : 1.0f;
            char* dst = qkv + (size_t)cb * 131072 + lo;
#pragma unroll
            for (int m = 0; m < 4; ++m) {
                const f32x4 va = acc[m][2 * sub] * sc, vb = acc[m][2 * sub + 1] * sc;
                unsigned a0 = cvtpk(va[0], va[1]), a1 = cvtpk(va[2], va[3]), b0 = cvtpk(vb[0], vb[1]), b1 = cvtpk(vb[2], vb[3]);
                swap16(a0, b0); swap16(a1, b1);
                ST16(dst + m * 1024, ((u32x4){a0, a1, b0, b1}));
            }
        }
    }

}

__device__ __forceinline__ void attn_role(char* smem, const int bx, const char* __restrict__ qkv, char* __restrict__ act, unsigned* __restrict__ ctr) {

    const int tid = threadIdx.x, lane = tid & 63;
    const int wid = __builtin_amdgcn_readfirstlane(tid >> 6);
    const int head = (bx & 7) * 4 + (bx >> 6), qb = (bx >> 3) & 7;
    __builtin_amdgcn_s_setprio(2);
    const char* Qf = qkv + (size_t)head * 131072;
    const char* Kf = Qf + 4 * MB;
    const char* Vr = Qf + 8 * MB;
    const unsigned lds0 = (unsigned)(size_t)(LAS char*)smem;
    const unsigned voff = lane * 16;
    const int tq0 = qb * 8 + wid * 2;
    unsigned fo[2];
#pragma unroll
    for (int s = 0; s < 2; ++s) { const int c = 2 * s + (lane >> 5); fo[s] = ((lane >> 4) & 1) * 1024 + c * 256 + (((lane & 15) ^ (4 * c)) * 16); }
    bf16x8 q[2][2];
    asm volatile("global_load_dwordx4 %0, %4, %6\n\tglobal_load_dwordx4 %1, %5, %6\n\tglobal_load_dwordx4 %2, %4, %6 offset:2048\n\tglobal_load_dwordx4 %3, %5, %6 offset:2048"
                 : "=&v"(q[0][0]), "=&v"(q[0][1]), "=&v"(q[1][0]), "=&v"(q[1][1]) : "v"(fo[0]), "v"(fo[1]), "s"(Qf + tq0 * 2048) : "memory");
    bf16x8 sel;
    { const short one = (((lane >> 2) & 1) == ((lane >> 4) & 1)) ? (short)0x3f80 : (short)0; sel = (bf16x8){one, one, one, one, one, one, one, one}; }
    const lds_cptr kp0 = (lds_cptr)smem + fo[0], kp1 = (lds_cptr)smem + fo[1];
    lds_cptr vp0, vp1;
    { const int h = lane >> 5, c = 2 * ((lane >> 4) & 1) + ((lane >> 1) & 1), q4 = (lane >> 2) & 3;
      vp0 = (lds_cptr)smem + 8192 + c * 256 + (h ^ c) * 64 + q4 * 16 + (lane & 1) * 8; vp1 = (lds_cptr)smem + 8192 + c * 256 + ((2 + h) ^ c) * 64 + q4 * 16 + (lane & 1) * 8; }
    const char* Kw = Kf + wid * 2048;
    const char* Vw = Vr + wid * 2048;
    volatile LAS unsigned* flag = (volatile LAS unsigned*)(smem);
    const unsigned ooff = ((lane >> 4) & 1) * 1024 + (lane >> 5) * 256 + (lane & 15) * 16;
#define AT_DMA(ST, SLOT) do { const unsigned d_ = lds0 + (SLOT) + wid * 2048; \
        dma_s2(voff, Kf + (ST) * 8192 + wid * 2048, d_); dma_s2(voff, Vr + (ST) * 8192 + wid * 2048, d_ + 8192); } while (0)
    for (int attempt = 0; attempt < 2; ++attempt) {
        f32x16 o[2]; f32x4 l[2];
        bf16x8 k0, k1;
        asm volatile("global_load_dwordx4 %0, %2, %4\n\tglobal_load_dwordx4 %1, %3, %4" : "=&v"(k0), "=&v"(k1) : "v"(fo[0]), "v"(fo[1]), "s"(Kf) : "memory");
        AT_DMA(0, 0); AT_DMA(1, 16384); AT_DMA(2, 32768);
        o[0] = (f32x16){}; o[1] = (f32x16){}; l[0] = (f32x4){}; l[1] = (f32x4){};
        PIN(o[0]); PIN(o[1]); PIN(l[0]); PIN(l[1]);
        asm volatile("s_waitcnt vmcnt(12)" : "+v"(k0), "+v"(k1), "+v"(q[0][0]), "+v"(q[0][1]), "+v"(q[1][0]), "+v"(q[1][1])::"memory");
        f32x16 negm[2];
        {
            float m0 = -INFINITY, m1 = -INFINITY;
            const int nt = attempt == 0 ? 1 : 64;
            for (int kt = 0; kt < nt; ++kt) {
                if (kt) { k0 = *(const bf16x8*)(Kf + kt * 2048 + fo[0]); k1 = *(const bf16x8*)(Kf + kt * 2048 + fo[1]); }
                f32x16 s0 = MF32(k0, q[0][0], (f32x16){}), s1 = MF32(k0, q[1][0], (f32x16){});
                s0 = MF32(k1, q[0][1], s0); s1 = MF32(k1, q[1][1], s1);
#pragma unroll
                for (int r = 0; r < 16; ++r) { m0 = fmaxf(m0, s0[r]); m1 = fmaxf(m1, s1[r]); }
            }
            m0 = fmaxf(m0, __shfl_xor(m0, 32)); m1 = fmaxf(m1, __shfl_xor(m1, 32));
#pragma unroll
            for (int r = 0; r < 16; ++r) { negm[0][r] = -m0; negm[1][r] = -m1; }
            PIN(negm[0]); PIN(negm[1]);
        }
        asm volatile("s_waitcnt vmcnt(0)\n\ts_barrier" ::: "memory");
        bf16x8 kf0 = *(const LAS bf16x8*)(kp0), kf1 = *(const LAS bf16x8*)(kp1);
        bf16x8 vf0 = (bf16x8){}, vf1 = (bf16x8){};
        u32x4 pa0 = (u32x4){}, pa1 = (u32x4){}, pb0 = (u32x4){}, pb1 = (u32x4){};
        f32x16 sA = MF32(kf0, q[0][0], negm[0]); sA = MF32(kf1, q[0][1], sA);
        f32x16 sB;
#define EX(X, i) X[i] = __builtin_amdgcn_exp2f(X[i])
#define PK(X, i) cvtpk(X[i], X[(i) + 1])
#define BC8(x) __builtin_bit_cast(bf16x8, x)
#define AT_UNIT(SC, PC0, PC1, SN, QN, NM, PP0, PP1, OP, LP, K0, K1, V0, V1, L0, L1, L2, L3) do { unsigned c0, c1, c2, c3, c4, c5, c6, c7; \
            SN = MF32(K0, QN[0], NM); SCHED(); L0; SCHED(); EX(SC, 0); EX(SC, 1); EX(SC, 2); SCHED(); \
            OP = MF32(V0, BC8(PP0), OP); SCHED(); L1; SCHED(); c0 = PK(SC, 0); EX(SC, 3); EX(SC, 4); EX(SC, 5); SCHED(); \
            SN = MF32(K1, QN[1], SN); SCHED(); L2; SCHED(); c1 = PK(SC, 2); c2 = PK(SC, 4); EX(SC, 6); EX(SC, 7); EX(SC, 8); SCHED(); \
            OP = MF32(V1, BC8(PP1), OP); SCHED(); L3; SCHED(); c3 = PK(SC, 6); EX(SC, 9); EX(SC, 10); EX(SC, 11); SCHED(); \
            MF16_ACC(LP, sel, PP0); SCHED(); c4 = PK(SC, 8); c5 = PK(SC, 10); EX(SC, 12); EX(SC, 13); SCHED(); \
            MF16_ACC(LP, sel, PP1); SCHED(); EX(SC, 14); EX(SC, 15); SCHED(); c6 = PK(SC, 12); SCHED(); c7 = PK(SC, 14); \
            PC0 = (u32x4){c0, c1, c2, c3}; PC1 = (u32x4){c4, c5, c6, c7}; SCHED(); } while (0)
#define AT_M0(SLOT, V) asm volatile("s_mov_b32 m0, %0" ::"s"(lds0 + (SLOT) + wid * 2048 + (V) * 8192) : "memory")
#define AT_PAIR(ST, V) asm volatile("global_load_lds_dwordx4 %0, %1\n\tglobal_load_lds_dwordx4 %0, %1 offset:1024" ::"v"(voff), "s"(((V) ? Vw : Kw) + ((ST) & 15) * 8192) : "memory")
#define AT_STAGE(SO, SN_, DM, ST, SLA, SLB) do { _Pragma("unroll") for (int kt = 0; kt < 4; ++kt) { \
                const int ko = kt < 3 ? (SO) + (kt + 1) * 2048 : (SN_), vo = (SO) + kt * 2048; \
                bf16x8 kn0, kn1, vn0, vn1; \
                AT_UNIT(sA, pa0, pa1, sB, q[1], negm[1], pb0, pb1, o[1], l[1], kf0, kf1, vf0, vf1, \
                        (kn0 = *(const LAS bf16x8*)(kp0 + ko), kn1 = *(const LAS bf16x8*)(kp1 + ko)), vn0 = cat8(tr_read(vp0 + vo), tr_read(vp1 + vo)), \
                        vn1 = cat8(tr_read(vp0 + vo + 1024), tr_read(vp1 + vo + 1024)), {}); \
                kf0 = kn0; kf1 = kn1; vf0 = vn0; vf1 = vn1; \
                __builtin_amdgcn_s_waitcnt(0xC07F); SCHED(); \
                AT_UNIT(sB, pb0, pb1, sA, q[0], negm[0], pa0, pa1, o[0], l[0], kf0, kf1, vf0, vf1, \
                        if (DM) AT_M0((kt >> 1) ? (SLB) : (SLA), kt & 1), if (DM) AT_PAIR((ST) + (kt >> 1), kt & 1), {}, {}); } } while (0)
        int s0 = 0, s1 = 16384, s2 = 32768, s3 = 49152, s4 = 65536;
        for (int k = 0; k < 8; ++k) {
            AT_STAGE(s0, s1, true, 2 * k + 3, s3, s4);
            AT_STAGE(s1, s2, false, 0, 0, 0);
            { const int t0 = s0, t1 = s1; s0 = s2; s1 = s3; s2 = s4; s3 = t0; s4 = t1; }
            asm volatile("s_waitcnt vmcnt(0)\n\ts_barrier" ::: "memory");
        }
#undef AT_STAGE
#undef AT_M0
#undef AT_PAIR
        o[1] = MF32(vf0, BC8(pb0), o[1]); o[1] = MF32(vf1, BC8(pb1), o[1]);
        l[1] = MF16(sel, BC8(pb0), l[1]); l[1] = MF16(sel, BC8(pb1), l[1]);
#undef AT_UNIT
        asm volatile("s_nop 7\n\ts_nop 7" : "+v"(l[0]), "+v"(l[1]), "+v"(o[0]), "+v"(o[1]));
        float tsum = 0.f;
#pragma unroll
        for (int i = 0; i < 2; ++i) {
            const float r = 1.0f / l[i][0];
#pragma unroll
            for (int e = 0; e < 16; ++e) { tsum += fabsf(o[i][e]); o[i][e] *= r; }
        }
        const bool bad = !(l[0][0] < 1.0995e12f) || !(l[1][0] < 1.0995e12f) || !(tsum < INFINITY);
        if (lane == 0) flag[wid] = 0u;
        if (bad) flag[wid] = 1u;
#pragma unroll
        for (int i = 0; i < 2; ++i)
#pragma unroll
            for (int s = 0; s < 2; ++s) {
                unsigned a0 = cvtpk(o[i][8 * s + 0], o[i][8 * s + 1]), a1 = cvtpk(o[i][8 * s + 2], o[i][8 * s + 3]);
                unsigned b0 = cvtpk(o[i][8 * s + 4], o[i][8 * s + 5]), b1 = cvtpk(o[i][8 * s + 6], o[i][8 * s + 7]);
                swap32(a0, b0); swap32(a1, b1);
                const u32x4 w = (u32x4){a0, a1, b0, b1};
                asm volatile("global_store_dwordx4 %0, %1, %2 sc1\n\ts_nop 1" ::"v"(ooff), "v"(w), "s"(act + (size_t)(112 + head) * 131072 + (tq0 + i) * 2048 + s * 512) : "memory");
            }
        asm volatile("s_waitcnt vmcnt(0) lgkmcnt(0)\n\ts_barrier" ::: "memory");
        const unsigned redo = flag[0] | flag[1] | flag[2] | flag[3];
        if (attempt == 1 || !__builtin_amdgcn_readfirstlane(redo)) break;
        asm volatile("s_waitcnt lgkmcnt(0)\n\ts_barrier" ::: "memory");
    }
#undef AT_DMA
    if (tid == 0) __hip_atomic_fetch_add(ctr + qb * 64, 1u, __ATOMIC_RELAXED, __HIP_MEMORY_SCOPE_AGENT);

}

__device__ __forceinline__ void gemm2_role(char* smem, const int bx, const char* __restrict__ act, const char* __restrict__ w2, const char* __restrict__ wo,
                                           const char* __restrict__ x, char* __restrict__ out, unsigned* __restrict__ ctr) {

    const int tid = threadIdx.x, lane = tid & 63;
    const int wid = __builtin_amdgcn_readfirstlane(tid >> 6);
    const int xc = bx & 7, ii = bx >> 3, tm = (xc >> 1) * 4 + (ii >> 3), tn = (xc & 1) * 8 + (ii & 7);
    const unsigned lds0 = (unsigned)(size_t)(LAS char*)smem + wid * 16384;
    __builtin_amdgcn_s_sleep(127);
    const char* asrc = act + (size_t)wid * 131072 + tm * 8192;
    unsigned av[4];
#pragma unroll
    for (int i = 0; i < 4; ++i) av[i] = (i ^ wid) * 2048 + lane * 16;
    const char* wsrc = w2 + (size_t)wid * 32 * 2048 + tn * 128;
    const unsigned wv0 = (lane >> 3) * 2048 + ((((lane >> 1) & 3) ^ ((lane >> 4) & 1)) * 32) + (lane & 1) * 16;
    const unsigned wv1 = (lane >> 3) * 2048 + ((((lane >> 1) & 3) ^ ((lane >> 4) & 1) ^ 2) * 32) + (lane & 1) * 16 + 16384 - 1024;
    lds_cptr wp[4];
    {
        const int g = lane >> 4, q = (lane >> 2) & 3, p = lane & 3, f = (q >> 1) | ((g & 1) << 1);
#pragma unroll
        for (int c = 0; c < 4; ++c) wp[c] = (lds_cptr)smem + wid * 16384 + g * 1024 + q * 128 + ((c ^ f) << 5) + p * 8;
    }
    f32x4 acc[8][4];
#pragma unroll
    for (int i = 0; i < 8; ++i)
#pragma unroll
        for (int n = 0; n < 4; ++n) { acc[i][n] = (f32x4){}; PIN(acc[i][n]); }
    bf16x8 a0[4], a1[4], a2[4], a3[4], wfa[4], wfb[4];
#define G2_LDA(A, H) do { \
        asm volatile("global_load_dwordx4 %0, %4, %6\n\tglobal_load_dwordx4 %1, %4, %6 offset:1024\n\tglobal_load_dwordx4 %2, %5, %6\n\tglobal_load_dwordx4 %3, %5, %6 offset:1024" \
                     : "=&v"(A[0]), "=&v"(A[1]), "=&v"(A[2]), "=&v"(A[3]) : "v"(av[2 * (H)]), "v"(av[2 * (H) + 1]), "s"(asrc) : "memory"); \
        if (H) asrc += 524288; } while (0)
#define G2_LDW(SL) do {     \
        asm volatile("s_mov_b32 m0, %3\n\ts_nop 0\n\tglobal_load_lds_dwordx4 %0, %2\n\tglobal_load_lds_dwordx4 %1, %2 offset:1024" \
                     ::"v"(wv0), "v"(wv1), "s"(wsrc), "s"(lds0 + (SL) * 2048) : "memory"); \
        wsrc += ((SL) & 1) ? 262144 - 32768 : 32768; } while (0)
#define G2_WAIT(N, A) asm volatile("s_waitcnt vmcnt(" #N ")" : "+v"(A[0]), "+v"(A[1]), "+v"(A[2]), "+v"(A[3])::"memory")
#define WFR(P, O) cat8(tr_read((P) + (O)), tr_read((P) + (O) + 512))
#define G2_STEP(N, AC, AN, WC, WN, SL, RD, IA, IW) do { \
        if ((N) >= 0) G2_WAIT(N, AN); \
        if (RD) { WN[2 * ((SL) & 1)] = WFR(wp[2 * ((SL) & 1)], ((((SL) >> 1) + 1) & 3) * 4096); WN[2 * ((SL) & 1) + 1] = WFR(wp[2 * ((SL) & 1) + 1], ((((SL) >> 1) + 1) & 3) * 4096); } \
        SCHED(); \
        _Pragma("unroll") for (int i = 0; i < 16; ++i) { \
            MF16_ACC(acc[4 * ((SL) & 1) + (i >> 2)][((i >> 2) & 1) ? 3 - (i & 3) : (i & 3)], WC[((i >> 2) & 1) ? 3 - (i & 3) : (i & 3)], AC[i >> 2]); \
            if ((IA) && (i & 3) == 3) asm volatile("global_load_dwordx4 %0, %1, %2 offset:%c3" : "=&v"(AC[i >> 2]) : "v"(av[2 * ((SL) & 1) + (i >> 3)]), "s"(asrc), "i"(((i >> 2) & 1) * 1024) : "memory"); } \
        SCHED(); if ((IA) && ((SL) & 1)) asrc += 524288; if (IW) G2_LDW(SL); } while (0)
#define G2_STEP4(N0, N1, N2, N3, SL, RDL, IA, IW) do { \
        G2_STEP(N0, a0, a1, wfa, wfb, (SL), true, IA, IW); G2_STEP(N1, a1, a2, wfa, wfb, (SL) + 1, true, IA, IW); \
        G2_STEP(N2, a2, a3, wfb, wfa, (SL) + 2, RDL, IA, IW); G2_STEP(N3, a3, a0, wfb, wfa, (SL) + 3, RDL, IA, IW); } while (0)
    G2_LDW(0); G2_LDW(1); G2_LDW(2); G2_LDW(3);
    G2_LDA(a0, 0); G2_LDW(4); G2_LDA(a1, 1); G2_LDW(5); G2_LDA(a2, 0); G2_LDW(6); G2_LDA(a3, 1); G2_LDW(7);
    G2_WAIT(20, a0);
#pragma unroll
    for (int c = 0; c < 4; ++c) wfa[c] = WFR(wp[c], 0);
    for (int it = 0; it < 6; ++it) { G2_STEP4(14, 14, 14, 14, 0, true, true, true); G2_STEP4(14, 14, 14, 14, 4, true, true, true); }
    G2_STEP4(14, 12, 10, 8, 0, true, true, false);
    G2_STEP4(8, 4, 0, -1, 4, false, false, false);
#undef G2_STEP4
#undef G2_STEP
#undef G2_WAIT
#undef G2_LDW
#undef G2_LDA
    const int fr = lane & 15, fq = lane >> 4;
    const size_t orow = (size_t)(tm * 128 + wid * 32 + fr) * 2048 + tn * 128 + (fq & 1) * 32 + (fq >> 1) * 16;
    u32x4 xr[4];
#pragma unroll
    for (int c = 0; c < 4; ++c) xr[c] = *(const u32x4*)(x + orow + (c >> 1) * 32768 + (c & 1) * 64);
    asm volatile("s_nop 7" : "+v"(acc[7][0]), "+v"(acc[7][1]), "+v"(acc[7][2]), "+v"(acc[7][3]));
    SCHED();
    char* mine = smem + wid * 16384 + lane * 16;
#pragma unroll
    for (int i = 0; i < 16; ++i) *(f32x4*)(mine + i * 1024) = acc[4 + (i >> 2)][i & 3];
    __syncthreads();
#pragma unroll
    for (int i = 0; i < 16; ++i) acc[i >> 2][i & 3] += *(const f32x4*)(smem + (wid ^ 2) * 16384 + lane * 16 + i * 1024);
    __syncthreads();
#pragma unroll
    for (int i = 0; i < 8; ++i) *(f32x4*)(mine + i * 1024) = acc[2 + (i >> 2)][i & 3];
    __syncthreads();
#pragma unroll
    for (int i = 0; i < 8; ++i) { acc[i >> 2][i & 3] += *(const f32x4*)(smem + (wid ^ 1) * 16384 + lane * 16 + i * 1024); PIN(acc[i >> 2][i & 3]); }
    __syncthreads();
    const unsigned ldsb = (unsigned)(size_t)(LAS char*)smem;
    const char* wos = wo + tn * 128;
    const char* osrc = act + (size_t)112 * 131072 + tm * 8192 + wid * 2048;
    unsigned ov = lane * 16;
    lds_cptr wq[4];
#pragma unroll
    for (int c = 0; c < 4; ++c) { wq[c] = wp[c] - wid * 16384; PIN(wq[c]); }
    PIN(ov);
#define G3_LDW(HS, SLOT) asm volatile("s_mov_b32 m0, %3\n\ts_nop 0\n\tglobal_load_lds_dwordx4 %0, %2\n\tglobal_load_lds_dwordx4 %1, %2 offset:1024" \
                     ::"v"(wv0), "v"(wv1), "s"(wos + (size_t)(HS) * 32768), "s"(ldsb + (SLOT) * 2048) : "memory")
#pragma unroll
    for (int i = 0; i < 10; ++i) G3_LDW(wid * 10 + i, wid * 10 + i);
    if (wid == 0) {
        const unsigned long long t0 = __builtin_amdgcn_s_memrealtime();
        while ((unsigned)__builtin_amdgcn_readfirstlane(__hip_atomic_load(ctr + (tm >> 1) * 64, __ATOMIC_RELAXED, __HIP_MEMORY_SCOPE_AGENT)) < 32u) {
            if (__builtin_amdgcn_s_memrealtime() - t0 > 5000000ull) break;
            __builtin_amdgcn_s_sleep(2);
        }
        __builtin_amdgcn_fence(__ATOMIC_ACQUIRE, "wavefront");
        asm volatile("" ::: "memory");
    }
    asm volatile("s_waitcnt vmcnt(0)\n\ts_barrier" ::: "memory");

    bf16x8 oa[8][4];
#define G3_LDA(G) asm volatile("global_load_dwordx4 %0, %4, %5 sc1\n\tglobal_load_dwordx4 %1, %4, %5 offset:1024 sc1\n\tglobal_load_dwordx4 %2, %4, %6 sc1\n\tglobal_load_dwordx4 %3, %4, %6 offset:1024 sc1" \
                     : "=&v"(oa[(G) & 7][0]), "=&v"(oa[(G) & 7][1]), "=&v"(oa[(G) & 7][2]), "=&v"(oa[(G) & 7][3]) \
                     : "v"(ov), "s"(osrc + (size_t)(G) * 262144), "s"(osrc + (size_t)(G) * 262144 + 131072) : "memory")
#define G3_WAIT(N, G) asm volatile("s_waitcnt vmcnt(" #N ")" : "+v"(oa[(G) & 7][0]), "+v"(oa[(G) & 7][1]), "+v"(oa[(G) & 7][2]), "+v"(oa[(G) & 7][3])::"memory")
    s16x4 wl[2][8], wh[2][8];
#define G3_WA(G, i) (wq[(i) & 3] + (((G) >= 8 && (G) < 10) ? 65536 : 0) + ((((G) * 2 + ((i) >> 2)) % 20) * 4096 - (((G) >= 8 && (G) < 10) ? 65536 : 0)))
#define G3_LD1(G, j) asm volatile("global_load_dwordx4 %0, %1, %2 offset:%c3 sc1" : "=&v"(oa[(G) & 7][j]) : "v"(ov), "s"(osrc + (size_t)(G) * 262144 + ((j) >> 1) * 131072), "i"(((j) & 1) * 1024) : "memory")
#define G3_GROUP(N, G, RD) do { G3_WAIT(N, G); SCHED(); \
        const bool rf_ = (G) == 5 || (G) == 6; \
        _Pragma("unroll") for (int i = 0; i < 8; ++i) { const bf16x8 w_ = cat8(wl[(G) & 1][i], wh[(G) & 1][i]); \
            MF16_ACC(acc[0][i & 3], w_, oa[(G) & 7][2 * (i >> 2)]); SCHED(); if (RD) wl[((G) + 1) & 1][i] = tr_read(G3_WA((G) + 1, i)); SCHED(); \
            MF16_ACC(acc[1][i & 3], w_, oa[(G) & 7][2 * (i >> 2) + 1]); SCHED(); if (RD) wh[((G) + 1) & 1][i] = tr_read(G3_WA((G) + 1, i) + 512); \
            if (rf_ && !(i & 1) && i < 6) G3_LDW(40 + wid * 6 + ((G) - 5) * 3 + (i >> 1), wid * 6 + ((G) - 5) * 3 + (i >> 1)); \
            if ((G) < 11 && (i & 1)) G3_LD1((G) + 5, i >> 1); SCHED(); } } while (0)
    G3_LDA(0); G3_LDA(1); G3_LDA(2); G3_LDA(3); G3_LDA(4);
#pragma unroll
    for (int i = 0; i < 8; ++i) { wl[0][i] = tr_read(G3_WA(0, i)); wh[0][i] = tr_read(G3_WA(0, i) + 512); }
    G3_GROUP(16, 0, true); G3_GROUP(16, 1, true); G3_GROUP(16, 2, true); G3_GROUP(16, 3, true); G3_GROUP(16, 4, true);

    asm volatile("s_waitcnt lgkmcnt(0)\n\ts_barrier" ::: "memory");
    G3_GROUP(16, 5, true); G3_GROUP(22, 6, true); G3_GROUP(28, 7, true); G3_GROUP(28, 8, true);

    asm volatile("s_waitcnt vmcnt(10)\n\ts_barrier" ::: "memory");

    G3_GROUP(10, 9, true); G3_GROUP(22, 10, true); G3_GROUP(16, 11, true); G3_GROUP(12, 12, true); G3_GROUP(8, 13, true); G3_GROUP(4, 14, true); G3_GROUP(0, 15, false);

#undef G3_GROUP
#undef G3_WA
#undef G3_WAIT
#undef G3_LDA
#undef G3_LD1
#undef G3_LDW
#undef WFR
    asm volatile("s_nop 7" : "+v"(acc[0][0]), "+v"(acc[0][1]), "+v"(acc[0][2]), "+v"(acc[0][3]), "+v"(acc[1][0]), "+v"(acc[1][1]), "+v"(acc[1][2]), "+v"(acc[1][3]));
#pragma unroll
    for (int c = 0; c < 4; ++c) {
        float v[8];
#pragma unroll
        for (int e = 0; e < 4; ++e) {
            unsigned a = __float_as_uint(acc[c >> 1][2 * (c & 1)][e]), b = __float_as_uint(acc[c >> 1][2 * (c & 1) + 1][e]);
            swap16(a, b);
            v[e] = __uint_as_float(a); v[4 + e] = __uint_as_float(b);
        }
        u32x4 w;
#pragma unroll
        for (int e = 0; e < 4; ++e) w[e] = cvtpk(v[2 * e] + bf_lo(xr[c][e]), v[2 * e + 1] + bf_hi(xr[c][e]));
        *(u32x4*)(out + orow + (c >> 1) * 32768 + (c & 1) * 64) = w;
    }

}

__global__ __launch_bounds__(256, 2) void k_fused(const char* __restrict__ qkv, char* __restrict__ act, const char* __restrict__ w2, const char* __restrict__ wo,
                                                  const char* __restrict__ x, char* __restrict__ out, unsigned* __restrict__ ctr) {
    __shared__ __attribute__((aligned(1024))) char smem[81920];
    asm volatile("" ::"s"(qkv), "s"(act), "s"(w2));
    if (__builtin_expect(blockIdx.x < 256, 1)) attn_role(smem, blockIdx.x, qkv, act, ctr);
    else gemm2_role(smem, blockIdx.x - 256, act, w2, wo, x, out, ctr);
}

extern "C" void kernel_launch(void* const* d_in, const int* in_sizes, int n_in, void* d_out, int out_size, void* d_ws, size_t ws_size,
                              hipStream_t stream) {
    const u16* x = (const u16*)d_in[0];
    const char* wqkv = (const char*)d_in[1];
    const char* wo = (const char*)d_in[2];
    const char* w1 = (const char*)d_in[4];
    const char* w3 = (const char*)d_in[5];
    const char* w2 = (const char*)d_in[6];
    char* ws = (char*)d_ws;
    k_ln<<<512, 512, 0, stream>>>(x, ws + WS_XN, (unsigned*)(ws + WS_CTR), wqkv, w1, w3);
    k_gemm1<<<256, 512, 0, stream>>>(ws + WS_XN, wqkv, w1, w3, ws + WS_QKV, ws + WS_ACT, w2, wo);
    k_fused<<<512, 256, 0, stream>>>(ws + WS_QKV, ws + WS_ACT, w2, wo, (const char*)x, (char*)d_out, (unsigned*)(ws + WS_CTR));
}
```

```cpp
#include <hip/hip_runtime.h>
#include <stdint.h>

typedef unsigned short u16;
typedef short bf16x8 __attribute__((ext_vector_type(8)));
typedef short s16x4 __attribute__((ext_vector_type(4)));
typedef float f32x2 __attribute__((ext_vector_type(2)));
typedef float f32x4 __attribute__((ext_vector_type(4)));
typedef float f32x16 __attribute__((ext_vector_type(16)));
typedef unsigned u32x2 __attribute__((ext_vector_type(2)));
typedef unsigned u32x4 __attribute__((ext_vector_type(4)));
typedef __bf16 bf16x2_t __attribute__((ext_vector_type(2)));
#define LAS __attribute__((address_space(3)))
typedef const LAS char* lds_cptr;

constexpr int SEQ = 2048, DM = 1024, FF = 3584, NH = 32;
constexpr size_t MB = 1024 * 1024;
constexpr size_t WS_CTR = 0;
constexpr size_t WS_XN = 65536;
constexpr size_t WS_QKV = WS_XN + 4 * MB;
constexpr size_t WS_ACT = WS_QKV + 12 * MB;
constexpr float QSCALE = 0.17677669529663687f * 1.4426950408889634f;

#define SCHED() __builtin_amdgcn_sched_barrier(0)
#define PIN(x) asm volatile("" : "+v"(x))
#define MF32(a, b, c) __builtin_amdgcn_mfma_f32_32x32x16_bf16(a, b, c, 0, 0, 0)
#define MF16(a, b, c) __builtin_amdgcn_mfma_f32_16x16x32_bf16(a, b, c, 0, 0, 0)

#define MF16_ACC(acc, a, b) asm volatile("v_mfma_f32_16x16x32_bf16 %0, %1, %2, %0" : "+v"(acc) : "v"(a), "v"(b))

__device__ __forceinline__ unsigned cvtpk(float lo, float hi) {
    f32x2 v = {lo, hi};
    bf16x2_t b = __builtin_convertvector(v, bf16x2_t);
    return __builtin_bit_cast(unsigned, b);
}
__device__ __forceinline__ float bf_lo(unsigned w) { return __uint_as_float(w << 16); }
__device__ __forceinline__ float bf_hi(unsigned w) { return __uint_as_float(w & 0xffff0000u); }
__device__ __forceinline__ float rbf(float x) { return bf_lo(cvtpk(x, x)); }
__device__ __forceinline__ s16x4 tr_read(lds_cptr p) {
    return __builtin_bit_cast(s16x4, __builtin_amdgcn_ds_read_tr16_b64_v4i16((LAS s16x4*)p));
}
__device__ __forceinline__ bf16x8 cat8(s16x4 lo, s16x4 hi) { return __builtin_shufflevector(lo, hi, 0, 1, 2, 3, 4, 5, 6, 7); }
__device__ __forceinline__ void dma_v(const void* g, unsigned lds) {
    asm volatile("s_mov_b32 m0, %1\n\ts_nop 0\n\tglobal_load_lds_dwordx4 %0, off" ::"v"(g), "s"(lds) : "memory");
}
__device__ __forceinline__ void dma_s1(unsigned voff, const void* sbase, unsigned lds) {
    asm volatile("s_mov_b32 m0, %2\n\ts_nop 0\n\tglobal_load_lds_dwordx4 %0, %1" ::"v"(voff), "s"(sbase), "s"(lds) : "memory");
}
__device__ __forceinline__ void dma_s2(unsigned voff, const void* sbase, unsigned lds) {
    asm volatile("s_mov_b32 m0, %2\n\ts_nop 0\n\tglobal_load_lds_dwordx4 %0, %1\n\tglobal_load_lds_dwordx4 %0, %1 offset:1024" ::"v"(voff), "s"(sbase), "s"(lds) : "memory");
}
__device__ __forceinline__ void swap16(unsigned& a, unsigned& b) {
    auto r = __builtin_amdgcn_permlane16_swap(a, b, false, false);
    a = r[0]; b = r[1];
}
__device__ __forceinline__ void swap32(unsigned& a, unsigned& b) {
    auto r = __builtin_amdgcn_permlane32_swap(a, b, false, false);
    a = r[0]; b = r[1];
}

__device__ __forceinline__ float wave_sum(float v) {
    v += __builtin_bit_cast(float, __builtin_amdgcn_update_dpp(0, __builtin_bit_cast(int, v), 0xB1, 0xf, 0xf, true));
    v += __builtin_bit_cast(float, __builtin_amdgcn_update_dpp(0, __builtin_bit_cast(int, v), 0x4E, 0xf, 0xf, true));
    v += __builtin_bit_cast(float, __builtin_amdgcn_update_dpp(0, __builtin_bit_cast(int, v), 0x141, 0xf, 0xf, true));
    v += __builtin_bit_cast(float, __builtin_amdgcn_update_dpp(0, __builtin_bit_cast(int, v), 0x140, 0xf, 0xf, true));
    auto a = __builtin_amdgcn_permlane16_swap(__float_as_uint(v), __float_as_uint(v), false, false);
    v = __uint_as_float(a[0]) + __uint_as_float(a[1]);
    auto b = __builtin_amdgcn_permlane32_swap(__float_as_uint(v), __float_as_uint(v), false, false);
    return __uint_as_float(b[0]) + __uint_as_float(b[1]);
}
#define CODE_ADDR(var, SYM) uint64_t var; \
    asm volatile("s_getpc_b64 s[20:21]\n\ts_add_u32 s20, s20, " SYM "@rel32@lo+4\n\ts_addc_u32 s21, s21, " SYM "@rel32@hi+12\n\ts_mov_b64 %0, s[20:21]" : "=s"(var)::"s20", "s21", "scc")
#define SYM_GEMM1 "_Z7k_gemm1PKcS0_S0_S0_PcS1_S0_S0_"
#define SYM_FUSED "_Z7k_fusedPKcPcS0_S0_S0_S1_Pj"
#define SYM_END "k_end"
__global__ __launch_bounds__(512) void k_ln(const u16* __restrict__ x, char* __restrict__ xnF, unsigned* __restrict__ ctr,
                                            const char* __restrict__ wqkv, const char* __restrict__ w1, const char* __restrict__ w3) {
    __shared__ __attribute__((aligned(16))) char sm[16384];
    const int tid = threadIdx.x, lane = tid & 63, wid = tid >> 6;
    if (blockIdx.x >= 256) {
        const size_t o = (size_t)((blockIdx.x - 256) * 512 + tid) * 16;
        const u32x4 p0 = *(const u32x4*)(wqkv + o), p1 = *(const u32x4*)(w1 + o), p2 = *(const u32x4*)(w3 + o);
        CODE_ADDR(c0, SYM_GEMM1); CODE_ADDR(c1, SYM_END);
        const uint64_t ca = c0 + ((blockIdx.x - 256) >> 3) * 1024;
        u32x4 p3 = p0;
        if (tid < 64 && ca + 1024 <= c1) p3 = *(const __attribute__((address_space(1))) u32x4*)(ca + tid * 16);
        asm volatile("" ::"v"(p0), "v"(p1), "v"(p2), "v"(p3));
        return;
    }
    if (blockIdx.x == 0) { ctr[tid] = 0u; ctr[tid + 512] = 0u; }
    const int row = blockIdx.x * 8 + wid;
    const u32x4* src = (const u32x4*)(x + (size_t)row * DM);
    const u32x4 a = src[lane], b = src[64 + lane];
    float v[16];
#pragma unroll
    for (int i = 0; i < 4; ++i) { v[2 * i] = bf_lo(a[i]); v[2 * i + 1] = bf_hi(a[i]); v[8 + 2 * i] = bf_lo(b[i]); v[8 + 2 * i + 1] = bf_hi(b[i]); }
    float s = 0.f;
#pragma unroll
    for (int i = 0; i < 16; ++i) s += v[i];
    s = wave_sum(s);
    const float mean = s * (1.0f / DM);
    float q = 0.f;
#pragma unroll
    for (int i = 0; i < 16; ++i) { const float d = v[i] - mean; q += d * d; }
    q = wave_sum(q);
    const float mb = rbf(mean), vb = rbf(q * (1.0f / DM));
    const float rs = 1.0f / rbf(sqrtf(rbf(vb + 1e-5f)));
    u32x4 oa, ob;
#pragma unroll
    for (int i = 0; i < 4; ++i) {
        oa[i] = cvtpk(rbf(v[2 * i] - mb) * rs, rbf(v[2 * i + 1] - mb) * rs);
        ob[i] = cvtpk(rbf(v[8 + 2 * i] - mb) * rs, rbf(v[8 + 2 * i + 1] - mb) * rs);
    }
    *(u32x4*)(sm + lane * 128 + wid * 16) = oa;
    *(u32x4*)(sm + (64 + lane) * 128 + wid * 16) = ob;
    __syncthreads();
    const int row0 = blockIdx.x * 8;
#pragma unroll
    for (int i = 0; i < 2; ++i) {
        const int p = tid + i * 512, chunk = p >> 3, r = p & 7;
        const u32x4 w = *(const u32x4*)(sm + p * 16);
        *(u32x4*)(xnF + ((size_t)(chunk >> 2) * 128 + (row0 >> 4)) * 1024 + (chunk & 3) * 256 + ((row0 & 15) + r) * 16) = w;
    }
}

__device__ __forceinline__ unsigned silu_mul2(f32x2 g, f32x2 u) {
    const f32x2 t = g * -1.4426950408889634f;
    f32x2 e; e.x = __builtin_amdgcn_exp2f(t.x); e.y = __builtin_amdgcn_exp2f(t.y);
    e = e + 1.0f;
    f32x2 r; r.x = __builtin_amdgcn_rcpf(e.x); r.y = __builtin_amdgcn_rcpf(e.y);
    const f32x2 h = (g * u) * r;
    return cvtpk(h.x, h.y);
}
__device__ __forceinline__ u32x2 silu_mul4(f32x4 g, f32x4 u) {
    return (u32x2){silu_mul2((f32x2){g[0], g[1]}, (f32x2){u[0], u[1]}), silu_mul2((f32x2){g[2], g[3]}, (f32x2){u[2], u[3]})};
}
#define ST16(p, v) asm volatile("global_store_dwordx4 %0, %1, off sc1\n\ts_nop 1" ::"v"(p), "v"(v) : "memory")
__global__ __launch_bounds__(512, 2) void k_gemm1(const char* __restrict__ xnF, const char* __restrict__ wqkv, const char* __restrict__ w1,
                                                  const char* __restrict__ w3, char* __restrict__ qkv, char* __restrict__ act,
                                                  const char* __restrict__ w2, const char* __restrict__ wo) {
    __shared__ __attribute__((aligned(1024))) char smem[147456 + 2048];

    const int tid = threadIdx.x, lane = tid & 63;
    const int wid = __builtin_amdgcn_readfirstlane(tid >> 6), wr = wid & 3, wc = wid >> 2;
    const int bx = blockIdx.x, j = (bx & 7) + 8 * ((bx >> 3) & 3), rb = bx >> 5;
    const unsigned lds0 = (unsigned)(size_t)(LAS char*)smem;
    const bool tq = j < 8;
    const int qb0 = 3 * j, qbx = tq ? 3 * j + 2 : 16 + j, pA = 2 * j - 8, pB = tq ? j : 2 * j - 7;
    const char* bsrc[5]; unsigned binc[5];
#pragma unroll
    for (int i = 0; i < 5; ++i) {
        const int kb = wr, blk = i;
        const int r = lane >> 3, j8 = lane & 7, f = ((r >> 1) & 1) | ((kb & 1) << 1), cp = (j8 >> 1) ^ f;
        const bool ffn = blk >= 3 || (blk < 2 && !tq);
        const int pitch = ffn ? 7168 : 6144;
        const uint64_t wg = (blk == 0 || blk == 3) ? (uint64_t)w1 : (uint64_t)w3, wb = ffn ? wg : (uint64_t)wqkv;
        const int cf = blk < 2 ? pA : pB, cq = blk == 2 ? qbx : qb0 + blk, cblk = ffn ? cf : cq;
        bsrc[i] = (const char*)wb + (size_t)(unsigned)(cblk * 128);
        binc[i] = 32u * pitch;
    }
    unsigned bvq, bvf;
    { const int r = lane >> 3, j8 = lane & 7, cp = (j8 >> 1) ^ (((r >> 1) & 1) | ((wr & 1) << 1));
      bvq = (wr * 8 + r) * 6144 + cp * 32 + (j8 & 1) * 16; bvf = (wr * 8 + r) * 7168 + cp * 32 + (j8 & 1) * 16; }
    const unsigned bv01 = tq ? bvq : bvf;
    const char* asrc = xnF + ((size_t)rb * 16 + 4 * wr) * 1024;
    const unsigned avoff = lane * 16;
    const unsigned adst = lds0 + wr * 4096, bdst = lds0 + 65536 + wr * 5120;
#define G1_DMA_B(SL) do { \
        dma_s1(bv01, bsrc[0], bdst + (SL) * 20480); dma_s1(bv01, bsrc[1], bdst + (SL) * 20480 + 1024); dma_s1(bvq, bsrc[2], bdst + (SL) * 20480 + 2048); \
        dma_s1(bvf, bsrc[3], bdst + (SL) * 20480 + 3072); dma_s1(bvf, bsrc[4], bdst + (SL) * 20480 + 4096); \
        _Pragma("unroll") for (int i = 0; i < 5; ++i) bsrc[i] += binc[i]; } while (0)
#define G1_DMA_A(SL) do { dma_s2(avoff, asrc, adst + (SL) * 16384); dma_s2(avoff, asrc + 2048, adst + (SL) * 16384 + 2048); asrc += 131072; } while (0)
#define G1_DMA(SL, VA) do { if (wc == 0) G1_DMA_B(SL); else { G1_DMA_A(SL); asm volatile("s_waitcnt vmcnt(" #VA ")" ::: "memory"); } } while (0)
    if (wc == 0) { G1_DMA_B(0); G1_DMA_B(1); G1_DMA_B(2); } else { G1_DMA_A(0); G1_DMA_A(1); G1_DMA_A(2); }
    const lds_cptr pa = (lds_cptr)smem + wr * 4096 + lane * 16;
    lds_cptr pb[4], pe0, pe1;
    {
        const int g = lane >> 4, q = (lane >> 2) & 3, p = lane & 3, f = (q >> 1) | ((g & 1) << 1);
        const lds_cptr b0 = (lds_cptr)smem + 65536 + g * 5120 + q * 128 + p * 8;
#pragma unroll
        for (int c = 0; c < 4; ++c) pb[c] = b0 + ((c ^ f) << 5) + wc * 3072;
        pe0 = b0 + ((wc ^ f) << 5) + 2048; pe1 = b0 + (((2 + wc) ^ f) << 5) + 2048;
    }
    f32x4 acc[4][10];
#pragma unroll
    for (int m = 0; m < 4; ++m)
#pragma unroll
        for (int n = 0; n < 10; ++n) { acc[m][n] = (f32x4){0.f, 0.f, 0.f, 0.f}; PIN(acc[m][n]); }
    asm volatile("s_waitcnt vmcnt(8)\n\ts_barrier" ::: "memory");

    if (wc == 1) asm volatile("s_barrier" ::: "memory");
    bf16x8 af[4], bf[10];
#define BFR(P, O) cat8(tr_read((P) + (O)), tr_read((P) + (O) + 512))
#define G1_READS(SL) do { \
        _Pragma("unroll") for (int c = 0; c < 4; ++c) { bf[c] = BFR(pb[c], (SL) * 20480); bf[4 + c] = BFR(pb[c], (SL) * 20480 + 1024); } \
        bf[8] = BFR(pe0, (SL) * 20480); bf[9] = BFR(pe1, (SL) * 20480); \
        _Pragma("unroll") for (int m = 0; m < 4; ++m) af[m] = *(const LAS bf16x8*)(pa + (SL) * 16384 + m * 1024); } while (0)
#define G1_STEP(SL, DMA, VB, ENDBAR) do { \
        G1_READS(SL); DMA; \
        asm volatile("s_waitcnt lgkmcnt(0)" ::: "memory"); \
        asm volatile("s_barrier" ::: "memory"); \
 SCHED(); __builtin_amdgcn_s_setprio(1); \
        _Pragma("unroll") for (int i = 0; i < 40; ++i) { const int m_ = ((i >> 2) & 1) ? 3 - (i & 3) : (i & 3); MF16_ACC(acc[m_][i >> 2], bf[i >> 2], af[m_]); } \
        __builtin_amdgcn_s_setprio(0); SCHED(); \
        if (ENDBAR) asm volatile("s_waitcnt vmcnt(" #VB ")\n\ts_barrier" ::: "memory"); } while (0)
    const char* pf = (wid < 7 ? w2 + (size_t)(bx * 7 + wid) * 4096 : wo + (size_t)bx * 8192);
    for (int it = 0; it < 7; ++it) { G1_STEP(0, G1_DMA(3, 8), 10, true); G1_STEP(1, G1_DMA(0, 8), 10, true); G1_STEP(2, G1_DMA(1, 8), 10, true); G1_STEP(3, G1_DMA(2, 8), 10, true); }
    G1_STEP(0, G1_DMA(3, 8), 10, true);
    G1_STEP(1, { dma_s2(avoff, pf, lds0 + 147456); dma_s2(avoff, pf + 2048, lds0 + 147456); if (wc == 1) asm volatile("s_waitcnt vmcnt(8)" ::: "memory"); }, 9, true);
    G1_STEP(2, { if (wc == 1) asm volatile("s_waitcnt vmcnt(4)" ::: "memory"); }, 4, true);
    {
        CODE_ADDR(cf0, SYM_FUSED); CODE_ADDR(cf1, SYM_END);
        const uint64_t pc = cf0 + (uint64_t)(((bx >> 3) * 4 + wr) & 31) * 1024, po = (uint64_t)wo + (uint64_t)bx * 8192 + 4096 + wr * 1024;
        const char* pl = (const char*)((wc == 1 && pc + 1024 <= cf1) ? pc : po);
        G1_STEP(3, dma_s1(avoff, pl, lds0 + 147456), 63, wc == 0);
    }

#undef G1_STEP
#undef G1_READS
#undef BFR
#undef G1_DMA
#undef G1_DMA_A
#undef G1_DMA_B
    asm volatile("s_nop 7" : "+v"(acc[0][9]), "+v"(acc[1][9]), "+v"(acc[2][9]), "+v"(acc[3][9]), "+v"(acc[3][8]), "+v"(acc[2][8]));
    SCHED();
    int tz = threadIdx.x; asm volatile("" : "+v"(tz));
    const int fr = tz & 15, fq = (tz >> 4) & 3;
    const int tb = (rb * 16 + wr * 4) * 1024;
    const bool ffn_main = !tq || wc == 1;
    const int cm = ((fq & 1) << 1) | (fq >> 1), ce = 2 * wc + (fq >> 1);
    {
        const int cb0 = 2 * qbx;
        const float sc = cb0 < 32 ? QSCALE : 1.0f;
        char* dst = qkv + (size_t)(cb0 + (fq & 1)) * 131072 + tb + ce * 256 + ((fr ^ (4 * ce)) * 16);
#pragma unroll
        for (int m = 0; m < 4; ++m) {
            const f32x4 va = acc[m][8] * sc, vb = acc[m][9] * sc;
            unsigned a0 = cvtpk(va[0], va[1]), a1 = cvtpk(va[2], va[3]), b0 = cvtpk(vb[0], vb[1]), b1 = cvtpk(vb[2], vb[3]);
            swap16(a0, b0); swap16(a1, b1);
            ST16(dst + m * 1024, ((u32x4){a0, a1, b0, b1}));
        }
    }
    if (ffn_main) {
        char* dst = act + (size_t)(wc ? pB : pA) * 262144 + tb + cm * 256 + fr * 16;
#pragma unroll
        for (int m = 0; m < 4; ++m)
#pragma unroll
            for (int sub = 0; sub < 2; ++sub) {
                const u32x2 a = silu_mul4(acc[m][2 * sub], acc[m][4 + 2 * sub]), b = silu_mul4(acc[m][2 * sub + 1], acc[m][4 + 2 * sub + 1]);
                unsigned a0 = a[0], a1 = a[1], b0 = b[0], b1 = b[1];
                swap16(a0, b0); swap16(a1, b1);
                ST16(dst + sub * 131072 + m * 1024, ((u32x4){a0, a1, b0, b1}));
            }
    } else {
        const int lo = tb + cm * 256 + ((fr ^ (4 * cm)) * 16);
#pragma unroll
        for (int sub = 0; sub < 4; ++sub) {
            const int cb = 2 * qb0 + sub;
            const float sc = cb < 32 ? QSCALE : 1.0f;
            char* dst = qkv + (size_t)cb * 131072 + lo;
#pragma unroll
            for (int m = 0; m < 4; ++m) {
                const f32x4 va = acc[m][2 * sub] * sc, vb = acc[m][2 * sub + 1] * sc;
                unsigned a0 = cvtpk(va[0], va[1]), a1 = cvtpk(va[2], va[3]), b0 = cvtpk(vb[0], vb[1]), b1 = cvtpk(vb[2], vb[3]);
                swap16(a0, b0); swap16(a1, b1);
                ST16(dst + m * 1024, ((u32x4){a0, a1, b0, b1}));
            }
        }
    }

}

__device__ __forceinline__ void attn_role(char* smem, const int bx, const char* __restrict__ qkv, char* __restrict__ act, unsigned* __restrict__ ctr) {

    const int tid = threadIdx.x, lane = tid & 63;
    const int wid = __builtin_amdgcn_readfirstlane(tid >> 6);
    const int head = (bx & 7) * 4 + (bx >> 6), qb = (bx >> 3) & 7;
    __builtin_amdgcn_s_setprio(2);
    const char* Qf = qkv + (size_t)head * 131072;
    const char* Kf = Qf + 4 * MB;
    const char* Vr = Qf + 8 * MB;
    const unsigned lds0 = (unsigned)(size_t)(LAS char*)smem;
    const unsigned voff = lane * 16;
    const int tq0 = qb * 8 + wid * 2;
    unsigned fo[2];
#pragma unroll
    for (int s = 0; s < 2; ++s) { const int c = 2 * s + (lane >> 5); fo[s] = ((lane >> 4) & 1) * 1024 + c * 256 + (((lane & 15) ^ (4 * c)) * 16); }
    bf16x8 q[2][2];
    asm volatile("global_load_dwordx4 %0, %4, %6\n\tglobal_load_dwordx4 %1, %5, %6\n\tglobal_load_dwordx4 %2, %4, %6 offset:2048\n\tglobal_load_dwordx4 %3, %5, %6 offset:2048"
                 : "=&v"(q[0][0]), "=&v"(q[0][1]), "=&v"(q[1][0]), "=&v"(q[1][1]) : "v"(fo[0]), "v"(fo[1]), "s"(Qf + tq0 * 2048) : "memory");
    bf16x8 sel;
    { const short one = (((lane >> 2) & 1) == ((lane >> 4) & 1)) ? (short)0x3f80 : (short)0; sel = (bf16x8){one, one, one, one, one, one, one, one}; }
    const lds_cptr kp0 = (lds_cptr)smem + fo[0], kp1 = (lds_cptr)smem + fo[1];
    lds_cptr vp0, vp1;
    { const int h = lane >> 5, c = 2 * ((lane >> 4) & 1) + ((lane >> 1) & 1), q4 = (lane >> 2) & 3;
      vp0 = (lds_cptr)smem + 8192 + c * 256 + (h ^ c) * 64 + q4 * 16 + (lane & 1) * 8; vp1 = (lds_cptr)smem + 8192 + c * 256 + ((2 + h) ^ c) * 64 + q4 * 16 + (lane & 1) * 8; }
    const char* Kw = Kf + wid * 2048;
    const char* Vw = Vr + wid * 2048;
    volatile LAS unsigned* flag = (volatile LAS unsigned*)(smem);
    const unsigned ooff = ((lane >> 4) & 1) * 1024 + (lane >> 5) * 256 + (lane & 15) * 16;
#define AT_DMA(ST, SLOT) do { const unsigned d_ = lds0 + (SLOT) + wid * 2048; \
        dma_s2(voff, Kf + (ST) * 8192 + wid * 2048, d_); dma_s2(voff, Vr + (ST) * 8192 + wid * 2048, d_ + 8192); } while (0)
    for (int attempt = 0; attempt < 2; ++attempt) {
        f32x16 o[2]; f32x4 l[2];
        bf16x8 k0, k1;
        asm volatile("global_load_dwordx4 %0, %2, %4\n\tglobal_load_dwordx4 %1, %3, %4" : "=&v"(k0), "=&v"(k1) : "v"(fo[0]), "v"(fo[1]), "s"(Kf) : "memory");
        AT_DMA(0, 0); AT_DMA(1, 16384); AT_DMA(2, 32768);
        o[0] = (f32x16){}; o[1] = (f32x16){}; l[0] = (f32x4){}; l[1] = (f32x4){};
        PIN(o[0]); PIN(o[1]); PIN(l[0]); PIN(l[1]);
        asm volatile("s_waitcnt vmcnt(12)" : "+v"(k0), "+v"(k1), "+v"(q[0][0]), "+v"(q[0][1]), "+v"(q[1][0]), "+v"(q[1][1])::"memory");
        f32x16 negm[2];
        {
            float m0 = -INFINITY, m1 = -INFINITY;
            const int nt = attempt == 0 ? 1 : 64;
            for (int kt = 0; kt < nt; ++kt) {
                if (kt) { k0 = *(const bf16x8*)(Kf + kt * 2048 + fo[0]); k1 = *(const bf16x8*)(Kf + kt * 2048 + fo[1]); }
                f32x16 s0 = MF32(k0, q[0][0], (f32x16){}), s1 = MF32(k0, q[1][0], (f32x16){});
                s0 = MF32(k1, q[0][1], s0); s1 = MF32(k1, q[1][1], s1);
#pragma unroll
                for (int r = 0; r < 16; ++r) { m0 = fmaxf(m0, s0[r]); m1 = fmaxf(m1, s1[r]); }
            }
            m0 = fmaxf(m0, __shfl_xor(m0, 32)); m1 = fmaxf(m1, __shfl_xor(m1, 32));
#pragma unroll
            for (int r = 0; r < 16; ++r) { negm[0][r] = -m0; negm[1][r] = -m1; }
            PIN(negm[0]); PIN(negm[1]);
        }
        asm volatile("s_waitcnt vmcnt(0)\n\ts_barrier" ::: "memory");
        bf16x8 kf0 = *(const LAS bf16x8*)(kp0), kf1 = *(const LAS bf16x8*)(kp1);
        bf16x8 vf0 = (bf16x8){}, vf1 = (bf16x8){};
        u32x4 pa0 = (u32x4){}, pa1 = (u32x4){}, pb0 = (u32x4){}, pb1 = (u32x4){};
        f32x16 sA = MF32(kf0, q[0][0], negm[0]); sA = MF32(kf1, q[0][1], sA);
        f32x16 sB;
#define EX(X, i) X[i] = __builtin_amdgcn_exp2f(X[i])
#define PK(X, i) cvtpk(X[i], X[(i) + 1])
#define BC8(x) __builtin_bit_cast(bf16x8, x)
#define AT_UNIT(SC, PC0, PC1, SN, QN, NM, PP0, PP1, OP, LP, K0, K1, V0, V1, L0, L1, L2, L3) do { unsigned c0, c1, c2, c3, c4, c5, c6, c7; \
            SN = MF32(K0, QN[0], NM); SCHED(); L0; SCHED(); EX(SC, 0); EX(SC, 1); EX(SC, 2); SCHED(); \
            OP = MF32(V0, BC8(PP0), OP); SCHED(); L1; SCHED(); c0 = PK(SC, 0); EX(SC, 3); EX(SC, 4); EX(SC, 5); SCHED(); \
            SN = MF32(K1, QN[1], SN); SCHED(); L2; SCHED(); c1 = PK(SC, 2); c2 = PK(SC, 4); EX(SC, 6); EX(SC, 7); EX(SC, 8); SCHED(); \
            OP = MF32(V1, BC8(PP1), OP); SCHED(); L3; SCHED(); c3 = PK(SC, 6); EX(SC, 9); EX(SC, 10); EX(SC, 11); SCHED(); \
            MF16_ACC(LP, sel, PP0); SCHED(); c4 = PK(SC, 8); c5 = PK(SC, 10); EX(SC, 12); EX(SC, 13); SCHED(); \
            MF16_ACC(LP, sel, PP1); SCHED(); EX(SC, 14); EX(SC, 15); SCHED(); c6 = PK(SC, 12); SCHED(); c7 = PK(SC, 14); \
            PC0 = (u32x4){c0, c1, c2, c3}; PC1 = (u32x4){c4, c5, c6, c7}; SCHED(); } while (0)
#define AT_M0(SLOT, V) asm volatile("s_mov_b32 m0, %0" ::"s"(lds0 + (SLOT) + wid * 2048 + (V) * 8192) : "memory")
#define AT_PAIR(ST, V) asm volatile("global_load_lds_dwordx4 %0, %1\n\tglobal_load_lds_dwordx4 %0, %1 offset:1024" ::"v"(voff), "s"(((V) ? Vw : Kw) + ((ST) & 15) * 8192) : "memory")
#define AT_STAGE(SO, SN_, DM, ST, SLA, SLB) do { _Pragma("unroll") for (int kt = 0; kt < 4; ++kt) { \
                const int ko = kt < 3 ? (SO) + (kt + 1) * 2048 : (SN_), vo = (SO) + kt * 2048; \
                bf16x8 kn0, kn1, vn0, vn1; \
                AT_UNIT(sA, pa0, pa1, sB, q[1], negm[1], pb0, pb1, o[1], l[1], kf0, kf1, vf0, vf1, \
                        (kn0 = *(const LAS bf16x8*)(kp0 + ko), kn1 = *(const LAS bf16x8*)(kp1 + ko)), vn0 = cat8(tr_read(vp0 + vo), tr_read(vp1 + vo)), \
                        vn1 = cat8(tr_read(vp0 + vo + 1024), tr_read(vp1 + vo + 1024)), {}); \
                kf0 = kn0; kf1 = kn1; vf0 = vn0; vf1 = vn1; \
                __builtin_amdgcn_s_waitcnt(0xC07F); SCHED(); \
                AT_UNIT(sB, pb0, pb1, sA, q[0], negm[0], pa0, pa1, o[0], l[0], kf0, kf1, vf0, vf1, \
                        if (DM) AT_M0((kt >> 1) ? (SLB) : (SLA), kt & 1), if (DM) AT_PAIR((ST) + (kt >> 1), kt & 1), {}, {}); } } while (0)
        int s0 = 0, s1 = 16384, s2 = 32768, s3 = 49152, s4 = 65536;
        for (int k = 0; k < 8; ++k) {
            AT_STAGE(s0, s1, true, 2 * k + 3, s3, s4);
            AT_STAGE(s1, s2, false, 0, 0, 0);
            { const int t0 = s0, t1 = s1; s0 = s2; s1 = s3; s2 = s4; s3 = t0; s4 = t1; }
            asm volatile("s_waitcnt vmcnt(0)\n\ts_barrier" ::: "memory");
        }
#undef AT_STAGE
#undef AT_M0
#undef AT_PAIR
        o[1] = MF32(vf0, BC8(pb0), o[1]); o[1] = MF32(vf1, BC8(pb1), o[1]);
        l[1] = MF16(sel, BC8(pb0), l[1]); l[1] = MF16(sel, BC8(pb1), l[1]);
#undef AT_UNIT
        asm volatile("s_nop 7\n\ts_nop 7" : "+v"(l[0]), "+v"(l[1]), "+v"(o[0]), "+v"(o[1]));
        float tsum = 0.f;
#pragma unroll
        for (int i = 0; i < 2; ++i) {
            const float r = 1.0f / l[i][0];
#pragma unroll
            for (int e = 0; e < 16; ++e) { tsum += fabsf(o[i][e]); o[i][e] *= r; }
        }
        const bool bad = !(l[0][0] < 1.0995e12f) || !(l[1][0] < 1.0995e12f) || !(tsum < INFINITY);
        if (lane == 0) flag[wid] = 0u;
        if (bad) flag[wid] = 1u;
#pragma unroll
        for (int i = 0; i < 2; ++i)
#pragma unroll
            for (int s = 0; s < 2; ++s) {
                unsigned a0 = cvtpk(o[i][8 * s + 0], o[i][8 * s + 1]), a1 = cvtpk(o[i][8 * s + 2], o[i][8 * s + 3]);
                unsigned b0 = cvtpk(o[i][8 * s + 4], o[i][8 * s + 5]), b1 = cvtpk(o[i][8 * s + 6], o[i][8 * s + 7]);
                swap32(a0, b0); swap32(a1, b1);
                const u32x4 w = (u32x4){a0, a1, b0, b1};
                asm volatile("global_store_dwordx4 %0, %1, %2 sc1\n\ts_nop 1" ::"v"(ooff), "v"(w), "s"(act + (size_t)(112 + head) * 131072 + (tq0 + i) * 2048 + s * 512) : "memory");
            }
        asm volatile("s_waitcnt vmcnt(0) lgkmcnt(0)\n\ts_barrier" ::: "memory");
        const unsigned redo = flag[0] | flag[1] | flag[2] | flag[3];
        if (attempt == 1 || !__builtin_amdgcn_readfirstlane(redo)) break;
        asm volatile("s_waitcnt lgkmcnt(0)\n\ts_barrier" ::: "memory");
    }
#undef AT_DMA
    if (tid == 0) __hip_atomic_fetch_add(ctr + qb * 64, 1u, __ATOMIC_RELAXED, __HIP_MEMORY_SCOPE_AGENT);

}

__device__ __forceinline__ void gemm2_role(char* smem, const int bx, const char* __restrict__ act, const char* __restrict__ w2, const char* __restrict__ wo,
                                           const char* __restrict__ x, char* __restrict__ out, unsigned* __restrict__ ctr) {

    const int tid = threadIdx.x, lane = tid & 63;
    const int wid = __builtin_amdgcn_readfirstlane(tid >> 6);
    const int xc = bx & 7, ii = bx >> 3, tm = (xc >> 1) * 4 + (ii >> 3), tn = (xc & 1) * 8 + (ii & 7);
    const unsigned lds0 = (unsigned)(size_t)(LAS char*)smem + wid * 16384;
    __builtin_amdgcn_s_sleep(127);
    const char* asrc = act + (size_t)wid * 131072 + tm * 8192;
    unsigned av[4];
#pragma unroll
    for (int i = 0; i < 4; ++i) av[i] = (i ^ wid) * 2048 + lane * 16;
    const char* wsrc = w2 + (size_t)wid * 32 * 2048 + tn * 128;
    const unsigned wv0 = (lane >> 3) * 2048 + ((((lane >> 1) & 3) ^ ((lane >> 4) & 1)) * 32) + (lane & 1) * 16;
    const unsigned wv1 = (lane >> 3) * 2048 + ((((lane >> 1) & 3) ^ ((lane >> 4) & 1) ^ 2) * 32) + (lane & 1) * 16 + 16384 - 1024;
    lds_cptr wp[4];
    {
        const int g = lane >> 4, q = (lane >> 2) & 3, p = lane & 3, f = (q >> 1) | ((g & 1) << 1);
#pragma unroll
        for (int c = 0; c < 4; ++c) wp[c] = (lds_cptr)smem + wid * 16384 + g * 1024 + q * 128 + ((c ^ f) << 5) + p * 8;
    }
    f32x4 acc[8][4];
#pragma unroll
    for (int i = 0; i < 8; ++i)
#pragma unroll
        for (int n = 0; n < 4; ++n) { acc[i][n] = (f32x4){}; PIN(acc[i][n]); }
    bf16x8 a0[4], a1[4], a2[4], a3[4], wfa[4], wfb[4];
#define G2_LDA(A, H) do { \
        asm volatile("global_load_dwordx4 %0, %4, %6\n\tglobal_load_dwordx4 %1, %4, %6 offset:1024\n\tglobal_load_dwordx4 %2, %5, %6\n\tglobal_load_dwordx4 %3, %5, %6 offset:1024" \
                     : "=&v"(A[0]), "=&v"(A[1]), "=&v"(A[2]), "=&v"(A[3]) : "v"(av[2 * (H)]), "v"(av[2 * (H) + 1]), "s"(asrc) : "memory"); \
        if (H) asrc += 524288; } while (0)
#define G2_LDW(SL) do {     \
        asm volatile("s_mov_b32 m0, %3\n\ts_nop 0\n\tglobal_load_lds_dwordx4 %0, %2\n\tglobal_load_lds_dwordx4 %1, %2 offset:1024" \
                     ::"v"(wv0), "v"(wv1), "s"(wsrc), "s"(lds0 + (SL) * 2048) : "memory"); \
        wsrc += ((SL) & 1) ? 262144 - 32768 : 32768; } while (0)
#define G2_WAIT(N, A) asm volatile("s_waitcnt vmcnt(" #N ")" : "+v"(A[0]), "+v"(A[1]), "+v"(A[2]), "+v"(A[3])::"memory")
#define WFR(P, O) cat8(tr_read((P) + (O)), tr_read((P) + (O) + 512))
#define G2_STEP(N, AC, AN, WC, WN, SL, RD, IA, IW) do { \
        if ((N) >= 0) G2_WAIT(N, AN); \
        if (RD) { WN[2 * ((SL) & 1)] = WFR(wp[2 * ((SL) & 1)], ((((SL) >> 1) + 1) & 3) * 4096); WN[2 * ((SL) & 1) + 1] = WFR(wp[2 * ((SL) & 1) + 1], ((((SL) >> 1) + 1) & 3) * 4096); } \
        SCHED(); \
        _Pragma("unroll") for (int i = 0; i < 16; ++i) { \
            MF16_ACC(acc[4 * ((SL) & 1) + (i >> 2)][((i >> 2) & 1) ? 3 - (i & 3) : (i & 3)], WC[((i >> 2) & 1) ? 3 - (i & 3) : (i & 3)], AC[i >> 2]); \
            if ((IA) && (i & 3) == 3) asm volatile("global_load_dwordx4 %0, %1, %2 offset:%c3" : "=&v"(AC[i >> 2]) : "v"(av[2 * ((SL) & 1) + (i >> 3)]), "s"(asrc), "i"(((i >> 2) & 1) * 1024) : "memory"); } \
        SCHED(); if ((IA) && ((SL) & 1)) asrc += 524288; if (IW) G2_LDW(SL); } while (0)
#define G2_STEP4(N0, N1, N2, N3, SL, RDL, IA, IW) do { \
        G2_STEP(N0, a0, a1, wfa, wfb, (SL), true, IA, IW); G2_STEP(N1, a1, a2, wfa, wfb, (SL) + 1, true, IA, IW); \
        G2_STEP(N2, a2, a3, wfb, wfa, (SL) + 2, RDL, IA, IW); G2_STEP(N3, a3, a0, wfb, wfa, (SL) + 3, RDL, IA, IW); } while (0)
    G2_LDW(0); G2_LDW(1); G2_LDW(2); G2_LDW(3);
    G2_LDA(a0, 0); G2_LDW(4); G2_LDA(a1, 1); G2_LDW(5); G2_LDA(a2, 0); G2_LDW(6); G2_LDA(a3, 1); G2_LDW(7);
    G2_WAIT(20, a0);
#pragma unroll
    for (int c = 0; c < 4; ++c) wfa[c] = WFR(wp[c], 0);
    for (int it = 0; it < 6; ++it) { G2_STEP4(14, 14, 14, 14, 0, true, true, true); G2_STEP4(14, 14, 14, 14, 4, true, true, true); }
    G2_STEP4(14, 12, 10, 8, 0, true, true, false);
    G2_STEP4(8, 4, 0, -1, 4, false, false, false);
#undef G2_STEP4
#undef G2_STEP
#undef G2_WAIT
#undef G2_LDW
#undef G2_LDA
    const int fr = lane & 15, fq = lane >> 4;
    const size_t orow = (size_t)(tm * 128 + wid * 32 + fr) * 2048 + tn * 128 + (fq & 1) * 32 + (fq >> 1) * 16;
    u32x4 xr[4];
#pragma unroll
    for (int c = 0; c < 4; ++c) xr[c] = *(const u32x4*)(x + orow + (c >> 1) * 32768 + (c & 1) * 64);
    asm volatile("s_nop 7" : "+v"(acc[7][0]), "+v"(acc[7][1]), "+v"(acc[7][2]), "+v"(acc[7][3]));
    SCHED();
    char* mine = smem + wid * 16384 + lane * 16;
#pragma unroll
    for (int i = 0; i < 16; ++i) *(f32x4*)(mine + i * 1024) = acc[4 + (i >> 2)][i & 3];
    __syncthreads();
#pragma unroll
    for (int i = 0; i < 16; ++i) acc[i >> 2][i & 3] += *(const f32x4*)(smem + (wid ^ 2) * 16384 + lane * 16 + i * 1024);
    __syncthreads();
#pragma unroll
    for (int i = 0; i < 8; ++i) *(f32x4*)(mine + i * 1024) = acc[2 + (i >> 2)][i & 3];
    __syncthreads();
#pragma unroll
    for (int i = 0; i < 8; ++i) { acc[i >> 2][i & 3] += *(const f32x4*)(smem + (wid ^ 1) * 16384 + lane * 16 + i * 1024); PIN(acc[i >> 2][i & 3]); }
    __syncthreads();
    const unsigned ldsb = (unsigned)(size_t)(LAS char*)smem;
    const char* wos = wo + tn * 128;
    const char* osrc = act + (size_t)112 * 131072 + tm * 8192 + wid * 2048;
    unsigned ov = lane * 16;
    lds_cptr wq[4];
#pragma unroll
    for (int c = 0; c < 4; ++c) { wq[c] = wp[c] - wid * 16384; PIN(wq[c]); }
    PIN(ov);
#define G3_LDW(HS, SLOT) asm volatile("s_mov_b32 m0, %3\n\ts_nop 0\n\tglobal_load_lds_dwordx4 %0, %2\n\tglobal_load_lds_dwordx4 %1, %2 offset:1024" \
                     ::"v"(wv0), "v"(wv1), "s"(wos + (size_t)(HS) * 32768), "s"(ldsb + (SLOT) * 2048) : "memory")
#pragma unroll
    for (int i = 0; i < 10; ++i) G3_LDW(wid * 10 + i, wid * 10 + i);
    if (wid == 0) {
        const unsigned long long t0 = __builtin_amdgcn_s_memrealtime();
        while ((unsigned)__builtin_amdgcn_readfirstlane(__hip_atomic_load(ctr + (tm >> 1) * 64, __ATOMIC_RELAXED, __HIP_MEMORY_SCOPE_AGENT)) < 32u) {
            if (__builtin_amdgcn_s_memrealtime() - t0 > 5000000ull) break;
            __builtin_amdgcn_s_sleep(2);
        }
        __builtin_amdgcn_fence(__ATOMIC_ACQUIRE, "wavefront");
        asm volatile("" ::: "memory");
    }
    asm volatile("s_waitcnt vmcnt(0)\n\ts_barrier" ::: "memory");

    bf16x8 oa[8][4];
#define G3_LDA(G) asm volatile("global_load_dwordx4 %0, %4, %5 sc1\n\tglobal_load_dwordx4 %1, %4, %5 offset:1024 sc1\n\tglobal_load_dwordx4 %2, %4, %6 sc1\n\tglobal_load_dwordx4 %3, %4, %6 offset:1024 sc1" \
                     : "=&v"(oa[(G) & 7][0]), "=&v"(oa[(G) & 7][1]), "=&v"(oa[(G) & 7][2]), "=&v"(oa[(G) & 7][3]) \
                     : "v"(ov), "s"(osrc + (size_t)(G) * 262144), "s"(osrc + (size_t)(G) * 262144 + 131072) : "memory")
#define G3_WAIT(N, G) asm volatile("s_waitcnt vmcnt(" #N ")" : "+v"(oa[(G) & 7][0]), "+v"(oa[(G) & 7][1]), "+v"(oa[(G) & 7][2]), "+v"(oa[(G) & 7][3])::"memory")
    s16x4 wl[2][8], wh[2][8];
#define G3_WA(G, i) (wq[(i) & 3] + (((G) >= 8 && (G) < 10) ? 65536 : 0) + ((((G) * 2 + ((i) >> 2)) % 20) * 4096 - (((G) >= 8 && (G) < 10) ? 65536 : 0)))
#define G3_LD1(G, j) asm volatile("global_load_dwordx4 %0, %1, %2 offset:%c3 sc1" : "=&v"(oa[(G) & 7][j]) : "v"(ov), "s"(osrc + (size_t)(G) * 262144 + ((j) >> 1) * 131072), "i"(((j) & 1) * 1024) : "memory")
#define G3_GROUP(N, G, RD) do { G3_WAIT(N, G); SCHED(); \
        const bool rf_ = (G) == 5 || (G) == 6; \
        _Pragma("unroll") for (int i = 0; i < 8; ++i) { const bf16x8 w_ = cat8(wl[(G) & 1][i], wh[(G) & 1][i]); \
            MF16_ACC(acc[0][i & 3], w_, oa[(G) & 7][2 * (i >> 2)]); SCHED(); if (RD) wl[((G) + 1) & 1][i] = tr_read(G3_WA((G) + 1, i)); SCHED(); \
            MF16_ACC(acc[1][i & 3], w_, oa[(G) & 7][2 * (i >> 2) + 1]); SCHED(); if (RD) wh[((G) + 1) & 1][i] = tr_read(G3_WA((G) + 1, i) + 512); \
            if (rf_ && !(i & 1) && i < 6) G3_LDW(40 + wid * 6 + ((G) - 5) * 3 + (i >> 1), wid * 6 + ((G) - 5) * 3 + (i >> 1)); \
            if ((G) < 11 && (i & 1)) G3_LD1((G) + 5, i >> 1); SCHED(); } } while (0)
    G3_LDA(0); G3_LDA(1); G3_LDA(2); G3_LDA(3); G3_LDA(4);
#pragma unroll
    for (int i = 0; i < 8; ++i) { wl[0][i] = tr_read(G3_WA(0, i)); wh[0][i] = tr_read(G3_WA(0, i) + 512); }
    G3_GROUP(16, 0, true); G3_GROUP(16, 1, true); G3_GROUP(16, 2, true); G3_GROUP(16, 3, true); G3_GROUP(16, 4, true);

    asm volatile("s_waitcnt lgkmcnt(0)\n\ts_barrier" ::: "memory");
    G3_GROUP(16, 5, true); G3_GROUP(22, 6, true); G3_GROUP(28, 7, true); G3_GROUP(28, 8, true);

    asm volatile("s_waitcnt vmcnt(10)\n\ts_barrier" ::: "memory");

    G3_GROUP(10, 9, true); G3_GROUP(22, 10, true); G3_GROUP(16, 11, true); G3_GROUP(12, 12, true); G3_GROUP(8, 13, true); G3_GROUP(4, 14, true); G3_GROUP(0, 15, false);

#undef G3_GROUP
#undef G3_WA
#undef G3_WAIT
#undef G3_LDA
#undef G3_LD1
#undef G3_LDW
#undef WFR
    asm volatile("s_nop 7" : "+v"(acc[0][0]), "+v"(acc[0][1]), "+v"(acc[0][2]), "+v"(acc[0][3]), "+v"(acc[1][0]), "+v"(acc[1][1]), "+v"(acc[1][2]), "+v"(acc[1][3]));
#pragma unroll
    for (int c = 0; c < 4; ++c) {
        float v[8];
#pragma unroll
        for (int e = 0; e < 4; ++e) {
            unsigned a = __float_as_uint(acc[c >> 1][2 * (c & 1)][e]), b = __float_as_uint(acc[c >> 1][2 * (c & 1) + 1][e]);
            swap16(a, b);
            v[e] = __uint_as_float(a); v[4 + e] = __uint_as_float(b);
        }
        u32x4 w;
#pragma unroll
        for (int e = 0; e < 4; ++e) w[e] = cvtpk(v[2 * e] + bf_lo(xr[c][e]), v[2 * e + 1] + bf_hi(xr[c][e]));
        *(u32x4*)(out + orow + (c >> 1) * 32768 + (c & 1) * 64) = w;
    }

}

__global__ __launch_bounds__(256, 2) void k_fused(const char* __restrict__ qkv, char* __restrict__ act, const char* __restrict__ w2, const char* __restrict__ wo,
                                                  const char* __restrict__ x, char* __restrict__ out, unsigned* __restrict__ ctr) {
    __shared__ __attribute__((aligned(1024))) char smem[81920];
    asm volatile("" ::"s"(qkv), "s"(act), "s"(w2));
    if (__builtin_expect(blockIdx.x < 256, 1)) attn_role(smem, blockIdx.x, qkv, act, ctr);
    else gemm2_role(smem, blockIdx.x - 256, act, w2, wo, x, out, ctr);
}

extern "C" __global__ void k_end() {}

extern "C" void kernel_launch(void* const* d_in, const int* in_sizes, int n_in, void* d_out, int out_size, void* d_ws, size_t ws_size,
                              hipStream_t stream) {
    const u16* x = (const u16*)d_in[0];
    const char* wqkv = (const char*)d_in[1];
    const char* wo = (const char*)d_in[2];
    const char* w1 = (const char*)d_in[4];
    const char* w3 = (const char*)d_in[5];
    const char* w2 = (const char*)d_in[6];
    char* ws = (char*)d_ws;
    k_ln<<<512, 512, 0, stream>>>(x, ws + WS_XN, (unsigned*)(ws + WS_CTR), wqkv, w1, w3);
    k_gemm1<<<256, 512, 0, stream>>>(ws + WS_XN, wqkv, w1, w3, ws + WS_QKV, ws + WS_ACT, w2, wo);
    k_fused<<<512, 256, 0, stream>>>(ws + WS_QKV, ws + WS_ACT, w2, wo, (const char*)x, (char*)d_out, (unsigned*)(ws + WS_CTR));
}
```

```cpp
#include <hip/hip_runtime.h>
#include <stdint.h>

typedef unsigned short u16;
typedef short bf16x8 __attribute__((ext_vector_type(8)));
typedef short s16x4 __attribute__((ext_vector_type(4)));
typedef float f32x2 __attribute__((ext_vector_type(2)));
typedef float f32x4 __attribute__((ext_vector_type(4)));
typedef float f32x16 __attribute__((ext_vector_type(16)));
typedef unsigned u32x2 __attribute__((ext_vector_type(2)));
typedef unsigned u32x4 __attribute__((ext_vector_type(4)));
typedef __bf16 bf16x2_t __attribute__((ext_vector_type(2)));
#define LAS __attribute__((address_space(3)))
typedef const LAS char* lds_cptr;

constexpr int DM = 1024;
constexpr size_t MB = 1024 * 1024;
constexpr size_t WS_CTR = 0;
constexpr size_t WS_XN = 65536;
constexpr size_t WS_QKV = WS_XN + 4 * MB;
constexpr size_t WS_ACT = WS_QKV + 12 * MB;
constexpr float QSCALE = 0.17677669529663687f * 1.4426950408889634f;

#define SCHED() __builtin_amdgcn_sched_barrier(0)
#define PIN(x) asm volatile("" : "+v"(x))
#define MF32(a, b, c) __builtin_amdgcn_mfma_f32_32x32x16_bf16(a, b, c, 0, 0, 0)
#define MF16(a, b, c) __builtin_amdgcn_mfma_f32_16x16x32_bf16(a, b, c, 0, 0, 0)

#define MF16_ACC(acc, a, b) asm volatile("v_mfma_f32_16x16x32_bf16 %0, %1, %2, %0" : "+v"(acc) : "v"(a), "v"(b))

__device__ __forceinline__ unsigned cvtpk(float lo, float hi) {
    f32x2 v = {lo, hi};
    bf16x2_t b = __builtin_convertvector(v, bf16x2_t);
    return __builtin_bit_cast(unsigned, b);
}
__device__ __forceinline__ float bf_lo(unsigned w) { return __uint_as_float(w << 16); }
__device__ __forceinline__ float bf_hi(unsigned w) { return __uint_as_float(w & 0xffff0000u); }
__device__ __forceinline__ float rbf(float x) { return bf_lo(cvtpk(x, x)); }
__device__ __forceinline__ s16x4 tr_read(lds_cptr p) {
    return __builtin_bit_cast(s16x4, __builtin_amdgcn_ds_read_tr16_b64_v4i16((LAS s16x4*)p));
}
__device__ __forceinline__ bf16x8 cat8(s16x4 lo, s16x4 hi) { return __builtin_shufflevector(lo, hi, 0, 1, 2, 3, 4, 5, 6, 7); }
__device__ __forceinline__ void dma_s1(unsigned voff, const void* sbase, unsigned lds) {
    asm volatile("s_mov_b32 m0, %2\n\ts_nop 0\n\tglobal_load_lds_dwordx4 %0, %1" ::"v"(voff), "s"(sbase), "s"(lds) : "memory");
}
__device__ __forceinline__ void dma_s2(unsigned voff, const void* sbase, unsigned lds) {
    asm volatile("s_mov_b32 m0, %2\n\ts_nop 0\n\tglobal_load_lds_dwordx4 %0, %1\n\tglobal_load_lds_dwordx4 %0, %1 offset:1024" ::"v"(voff), "s"(sbase), "s"(lds) : "memory");
}
__device__ __forceinline__ void swap16(unsigned& a, unsigned& b) {
    auto r = __builtin_amdgcn_permlane16_swap(a, b, false, false);
    a = r[0]; b = r[1];
}
__device__ __forceinline__ void swap32(unsigned& a, unsigned& b) {
    auto r = __builtin_amdgcn_permlane32_swap(a, b, false, false);
    a = r[0]; b = r[1];
}

__device__ __forceinline__ float wave_sum(float v) {
    v += __builtin_bit_cast(float, __builtin_amdgcn_update_dpp(0, __builtin_bit_cast(int, v), 0xB1, 0xf, 0xf, true));
    v += __builtin_bit_cast(float, __builtin_amdgcn_update_dpp(0, __builtin_bit_cast(int, v), 0x4E, 0xf, 0xf, true));
    v += __builtin_bit_cast(float, __builtin_amdgcn_update_dpp(0, __builtin_bit_cast(int, v), 0x141, 0xf, 0xf, true));
    v += __builtin_bit_cast(float, __builtin_amdgcn_update_dpp(0, __builtin_bit_cast(int, v), 0x140, 0xf, 0xf, true));
    auto a = __builtin_amdgcn_permlane16_swap(__float_as_uint(v), __float_as_uint(v), false, false);
    v = __uint_as_float(a[0]) + __uint_as_float(a[1]);
    auto b = __builtin_amdgcn_permlane32_swap(__float_as_uint(v), __float_as_uint(v), false, false);
    return __uint_as_float(b[0]) + __uint_as_float(b[1]);
}
#define CODE_ADDR(var, SYM) uint64_t var; \
    asm volatile("s_getpc_b64 s[20:21]\n\ts_add_u32 s20, s20, " SYM "@rel32@lo+4\n\ts_addc_u32 s21, s21, " SYM "@rel32@hi+12\n\ts_mov_b64 %0, s[20:21]" : "=s"(var)::"s20", "s21", "scc")
extern "C" __global__ __launch_bounds__(512) void k_ln(const u16* __restrict__ x, char* __restrict__ xnF, unsigned* __restrict__ ctr,
                                            const char* __restrict__ wqkv, const char* __restrict__ w1, const char* __restrict__ w3) {
    __shared__ __attribute__((aligned(16))) char sm[16384];
    const int tid = threadIdx.x, lane = tid & 63, wid = tid >> 6;
    if (blockIdx.x >= 256) {
        const size_t o = (size_t)((blockIdx.x - 256) * 512 + tid) * 16;
        const u32x4 p0 = *(const u32x4*)(wqkv + o), p1 = *(const u32x4*)(w1 + o), p2 = *(const u32x4*)(w3 + o);
        CODE_ADDR(c0, "k_gemm1"); CODE_ADDR(c1, "k_end");
        const uint64_t ca = c0 + ((blockIdx.x - 256) >> 3) * 1024;
        u32x4 p3 = p0;
        if (tid < 64 && ca + 1024 <= c1) p3 = *(const __attribute__((address_space(1))) u32x4*)(ca + tid * 16);
        asm volatile("" ::"v"(p0), "v"(p1), "v"(p2), "v"(p3));
        return;
    }
    if (blockIdx.x == 0) { ctr[tid] = 0u; ctr[tid + 512] = 0u; }
    const int row = blockIdx.x * 8 + wid;
    const u32x4* src = (const u32x4*)(x + (size_t)row * DM);
    const u32x4 a = src[lane], b = src[64 + lane];
    float v[16];
#pragma unroll
    for (int i = 0; i < 4; ++i) { v[2 * i] = bf_lo(a[i]); v[2 * i + 1] = bf_hi(a[i]); v[8 + 2 * i] = bf_lo(b[i]); v[8 + 2 * i + 1] = bf_hi(b[i]); }
    float s = 0.f;
#pragma unroll
    for (int i = 0; i < 16; ++i) s += v[i];
    s = wave_sum(s);
    const float mean = s * (1.0f / DM);
    float q = 0.f;
#pragma unroll
    for (int i = 0; i < 16; ++i) { const float d = v[i] - mean; q += d * d; }
    q = wave_sum(q);
    const float mb = rbf(mean), vb = rbf(q * (1.0f / DM));
    const float rs = 1.0f / rbf(sqrtf(rbf(vb + 1e-5f)));
    u32x4 oa, ob;
#pragma unroll
    for (int i = 0; i < 4; ++i) {
        oa[i] = cvtpk(rbf(v[2 * i] - mb) * rs, rbf(v[2 * i + 1] - mb) * rs);
        ob[i] = cvtpk(rbf(v[8 + 2 * i] - mb) * rs, rbf(v[8 + 2 * i + 1] - mb) * rs);
    }
    *(u32x4*)(sm + lane * 128 + wid * 16) = oa;
    *(u32x4*)(sm + (64 + lane) * 128 + wid * 16) = ob;
    __syncthreads();
    const int row0 = blockIdx.x * 8;
#pragma unroll
    for (int i = 0; i < 2; ++i) {
        const int p = tid + i * 512, chunk = p >> 3, r = p & 7;
        const u32x4 w = *(const u32x4*)(sm + p * 16);
        *(u32x4*)(xnF + ((size_t)(chunk >> 2) * 128 + (row0 >> 4)) * 1024 + (chunk & 3) * 256 + ((row0 & 15) + r) * 16) = w;
    }
}

__device__ __forceinline__ unsigned silu_mul2(f32x2 g, f32x2 u) {
    const f32x2 t = g * -1.4426950408889634f;
    f32x2 e; e.x = __builtin_amdgcn_exp2f(t.x); e.y = __builtin_amdgcn_exp2f(t.y);
    e = e + 1.0f;
    f32x2 r; r.x = __builtin_amdgcn_rcpf(e.x); r.y = __builtin_amdgcn_rcpf(e.y);
    const f32x2 h = (g * u) * r;
    return cvtpk(h.x, h.y);
}
__device__ __forceinline__ u32x2 silu_mul4(f32x4 g, f32x4 u) {
    return (u32x2){silu_mul2((f32x2){g[0], g[1]}, (f32x2){u[0], u[1]}), silu_mul2((f32x2){g[2], g[3]}, (f32x2){u[2], u[3]})};
}
#define ST16(p, v) asm volatile("global_store_dwordx4 %0, %1, off sc1\n\ts_nop 1" ::"v"(p), "v"(v) : "memory")
extern "C" __global__ __launch_bounds__(512, 2) void k_gemm1(const char* __restrict__ xnF, const char* __restrict__ wqkv, const char* __restrict__ w1,
                                                  const char* __restrict__ w3, char* __restrict__ qkv, char* __restrict__ act,
                                                  const char* __restrict__ w2, const char* __restrict__ wo) {
    __shared__ __attribute__((aligned(1024))) char smem[147456 + 2048];

    const int tid = threadIdx.x, lane = tid & 63;
    const int wid = __builtin_amdgcn_readfirstlane(tid >> 6), wr = wid & 3, wc = wid >> 2;
    const int bx = blockIdx.x, j = (bx & 7) + 8 * ((bx >> 3) & 3), rb = bx >> 5;
    const unsigned lds0 = (unsigned)(size_t)(LAS char*)smem;
    const bool tq = j < 8;
    const int qb0 = 3 * j, qbx = tq ? 3 * j + 2 : 16 + j, pA = 2 * j - 8, pB = tq ? j : 2 * j - 7;
    const char* bsrc[5]; unsigned binc[5];
#pragma unroll
    for (int i = 0; i < 5; ++i) {
        const int kb = wr, blk = i;
        const int r = lane >> 3, j8 = lane & 7, f = ((r >> 1) & 1) | ((kb & 1) << 1), cp = (j8 >> 1) ^ f;
        const bool ffn = blk >= 3 || (blk < 2 && !tq);
        const int pitch = ffn ? 7168 : 6144;
        const uint64_t wg = (blk == 0 || blk == 3) ? (uint64_t)w1 : (uint64_t)w3, wb = ffn ? wg : (uint64_t)wqkv;
        const int cf = blk < 2 ? pA : pB, cq = blk == 2 ? qbx : qb0 + blk, cblk = ffn ? cf : cq;
        bsrc[i] = (const char*)wb + (size_t)(unsigned)(cblk * 128);
        binc[i] = 32u * pitch;
    }
    unsigned bvq, bvf;
    { const int r = lane >> 3, j8 = lane & 7, cp = (j8 >> 1) ^ (((r >> 1) & 1) | ((wr & 1) << 1));
      bvq = (wr * 8 + r) * 6144 + cp * 32 + (j8 & 1) * 16; bvf = (wr * 8 + r) * 7168 + cp * 32 + (j8 & 1) * 16; }
    const unsigned bv01 = tq ? bvq : bvf;
    const char* asrc = xnF + ((size_t)rb * 16 + 4 * wr) * 1024;
    const unsigned avoff = lane * 16;
    const unsigned adst = lds0 + wr * 4096, bdst = lds0 + 65536 + wr * 5120;
#define G1_DMA_B(SL) do { \
        dma_s1(bv01, bsrc[0], bdst + (SL) * 20480); dma_s1(bv01, bsrc[1], bdst + (SL) * 20480 + 1024); dma_s1(bvq, bsrc[2], bdst + (SL) * 20480 + 2048); \
        dma_s1(bvf, bsrc[3], bdst + (SL) * 20480 + 3072); dma_s1(bvf, bsrc[4], bdst + (SL) * 20480 + 4096); \
        _Pragma("unroll") for (int i = 0; i < 5; ++i) bsrc[i] += binc[i]; } while (0)
#define G1_DMA_A(SL) do { dma_s2(avoff, asrc, adst + (SL) * 16384); dma_s2(avoff, asrc + 2048, adst + (SL) * 16384 + 2048); asrc += 131072; } while (0)
#define G1_DMA(SL, VA) do { if (wc == 0) G1_DMA_B(SL); else { G1_DMA_A(SL); asm volatile("s_waitcnt vmcnt(" #VA ")" ::: "memory"); } } while (0)
    if (wc == 0) { G1_DMA_B(0); G1_DMA_B(1); G1_DMA_B(2); } else { G1_DMA_A(0); G1_DMA_A(1); G1_DMA_A(2); }
    lds_cptr pa = (lds_cptr)smem + wr * 4096 + lane * 16;
    lds_cptr pb[4], pe0, pe1;
    {
        const int g = lane >> 4, q = (lane >> 2) & 3, p = lane & 3, f = (q >> 1) | ((g & 1) << 1);
        const lds_cptr b0 = (lds_cptr)smem + 65536 + g * 5120 + q * 128 + p * 8;
#pragma unroll
        for (int c = 0; c < 4; ++c) pb[c] = b0 + ((c ^ f) << 5) + wc * 3072;
        pe0 = b0 + ((wc ^ f) << 5) + 2048; pe1 = b0 + (((2 + wc) ^ f) << 5) + 2048;
    }
    const uint64_t pfa = (uint64_t)w2 + (uint64_t)(bx * 7 + wid) * 4096, pfb = (uint64_t)wo + (uint64_t)bx * 8192;
    const char* pf = (const char*)(wid < 7 ? pfa : pfb);
    PIN(pa); PIN(pb[0]); PIN(pb[1]); PIN(pb[2]); PIN(pb[3]); PIN(pe0); PIN(pe1);
    asm volatile("" ::"s"(pf), "s"(qkv), "s"(act), "s"(wo));
    f32x4 acc[4][10];
#pragma unroll
    for (int m = 0; m < 4; ++m)
#pragma unroll
        for (int n = 0; n < 10; ++n) { acc[m][n] = (f32x4){0.f, 0.f, 0.f, 0.f}; PIN(acc[m][n]); }
    SCHED();
    asm volatile("s_waitcnt vmcnt(8)\n\ts_barrier" ::: "memory");

    if (wc == 1) asm volatile("s_barrier" ::: "memory");
    bf16x8 af[4], bf[10];
#define BFR(P, O) cat8(tr_read((P) + (O)), tr_read((P) + (O) + 512))
#define G1_READS(SL) do { \
        _Pragma("unroll") for (int c = 0; c < 4; ++c) { bf[c] = BFR(pb[c], (SL) * 20480); bf[4 + c] = BFR(pb[c], (SL) * 20480 + 1024); } \
        bf[8] = BFR(pe0, (SL) * 20480); bf[9] = BFR(pe1, (SL) * 20480); \
        _Pragma("unroll") for (int m = 0; m < 4; ++m) af[m] = *(const LAS bf16x8*)(pa + (SL) * 16384 + m * 1024); } while (0)
#define G1_STEP(SL, DMA, VB, ENDBAR) do { \
        G1_READS(SL); DMA; \
        asm volatile("s_waitcnt lgkmcnt(0)" ::: "memory"); \
        asm volatile("s_barrier" ::: "memory"); \
        SCHED(); __builtin_amdgcn_s_setprio(1); \
        _Pragma("unroll") for (int i = 0; i < 40; ++i) { const int m_ = ((i >> 2) & 1) ? 3 - (i & 3) : (i & 3); MF16_ACC(acc[m_][i >> 2], bf[i >> 2], af[m_]); } \
        __builtin_amdgcn_s_setprio(0); SCHED(); \
        if (ENDBAR) asm volatile("s_waitcnt vmcnt(" #VB ")\n\ts_barrier" ::: "memory"); } while (0)
    for (int it = 0; it < 7; ++it) { G1_STEP(0, G1_DMA(3, 8), 10, true); G1_STEP(1, G1_DMA(0, 8), 10, true); G1_STEP(2, G1_DMA(1, 8), 10, true); G1_STEP(3, G1_DMA(2, 8), 10, true); }
    G1_STEP(0, G1_DMA(3, 8), 10, true);
    G1_STEP(1, { dma_s2(avoff, pf, lds0 + 147456); dma_s2(avoff, pf + 2048, lds0 + 147456); if (wc == 1) asm volatile("s_waitcnt vmcnt(8)" ::: "memory"); }, 9, true);
    G1_STEP(2, { if (wc == 1) asm volatile("s_waitcnt vmcnt(4)" ::: "memory"); }, 4, true);
    {
        CODE_ADDR(cf0, "k_fused"); CODE_ADDR(cf1, "k_end");
        const uint64_t pc = cf0 + (uint64_t)(((bx >> 3) * 4 + wr) & 31) * 1024, po = (uint64_t)wo + (uint64_t)bx * 8192 + 4096 + wr * 1024;
        const char* pl = (const char*)((wc == 1 && pc + 1024 <= cf1) ? pc : po);
        G1_STEP(3, dma_s1(avoff, pl, lds0 + 147456), 63, wc == 0);
    }

#undef G1_STEP
#undef G1_READS
#undef BFR
#undef G1_DMA
#undef G1_DMA_A
#undef G1_DMA_B
    asm volatile("s_nop 7" : "+v"(acc[0][9]), "+v"(acc[1][9]), "+v"(acc[2][9]), "+v"(acc[3][9]), "+v"(acc[3][8]), "+v"(acc[2][8]));
    SCHED();
    int tz = threadIdx.x; asm volatile("" : "+v"(tz));
    const int fr = tz & 15, fq = (tz >> 4) & 3;
    const int tb = (rb * 16 + wr * 4) * 1024;
    const bool ffn_main = !tq || wc == 1;
    const int cm = ((fq & 1) << 1) | (fq >> 1), ce = 2 * wc + (fq >> 1);
    {
        const int cb0 = 2 * qbx;
        const float sc = cb0 < 32 ? QSCALE : 1.0f;
        char* dst = qkv + (size_t)(cb0 + (fq & 1)) * 131072 + tb + ce * 256 + ((fr ^ (4 * ce)) * 16);
#pragma unroll
        for (int m = 0; m < 4; ++m) {
            const f32x4 va = acc[m][8] * sc, vb = acc[m][9] * sc;
            unsigned a0 = cvtpk(va[0], va[1]), a1 = cvtpk(va[2], va[3]), b0 = cvtpk(vb[0], vb[1]), b1 = cvtpk(vb[2], vb[3]);
            swap16(a0, b0); swap16(a1, b1);
            ST16(dst + m * 1024, ((u32x4){a0, a1, b0, b1}));
        }
    }
    if (ffn_main) {
        char* dst = act + (size_t)(wc ? pB : pA) * 262144 + tb + cm * 256 + fr * 16;
#pragma unroll
        for (int m = 0; m < 4; ++m)
#pragma unroll
            for (int sub = 0; sub < 2; ++sub) {
                const u32x2 a = silu_mul4(acc[m][2 * sub], acc[m][4 + 2 * sub]), b = silu_mul4(acc[m][2 * sub + 1], acc[m][4 + 2 * sub + 1]);
                unsigned a0 = a[0], a1 = a[1], b0 = b[0], b1 = b[1];
                swap16(a0, b0); swap16(a1, b1);
                ST16(dst + sub * 131072 + m * 1024, ((u32x4){a0, a1, b0, b1}));
            }
    } else {
        const int lo = tb + cm * 256 + ((fr ^ (4 * cm)) * 16);
#pragma unroll
        for (int sub = 0; sub < 4; ++sub) {
            const int cb = 2 * qb0 + sub;
            const float sc = cb < 32 ? QSCALE : 1.0f;
            char* dst = qkv + (size_t)cb * 131072 + lo;
#pragma unroll
            for (int m = 0; m < 4; ++m) {
                const f32x4 va = acc[m][2 * sub] * sc, vb = acc[m][2 * sub + 1] * sc;
                unsigned a0 = cvtpk(va[0], va[1]), a1 = cvtpk(va[2], va[3]), b0 = cvtpk(vb[0], vb[1]), b1 = cvtpk(vb[2], vb[3]);
                swap16(a0, b0); swap16(a1, b1);
                ST16(dst + m * 1024, ((u32x4){a0, a1, b0, b1}));
            }
        }
    }

}

__device__ __forceinline__ void attn_role(char* smem, const int bx, const char* __restrict__ qkv, char* __restrict__ act, unsigned* __restrict__ ctr) {

    const int tid = threadIdx.x, lane = tid & 63;
    const int wid = __builtin_amdgcn_readfirstlane(tid >> 6);
    const int head = (bx & 7) * 4 + (bx >> 6), qb = (bx >> 3) & 7;
    __builtin_amdgcn_s_setprio(2);
    const char* Qf = qkv + (size_t)head * 131072;
    const char* Kf = Qf + 4 * MB;
    const char* Vr = Qf + 8 * MB;
    const unsigned lds0 = (unsigned)(size_t)(LAS char*)smem;
    const unsigned voff = lane * 16;
    const int tq0 = qb * 8 + wid * 2;
    unsigned fo[2];
#pragma unroll
    for (int s = 0; s < 2; ++s) { const int c = 2 * s + (lane >> 5); fo[s] = ((lane >> 4) & 1) * 1024 + c * 256 + (((lane & 15) ^ (4 * c)) * 16); }
    bf16x8 q[2][2];
    asm volatile("global_load_dwordx4 %0, %4, %6\n\tglobal_load_dwordx4 %1, %5, %6\n\tglobal_load_dwordx4 %2, %4, %6 offset:2048\n\tglobal_load_dwordx4 %3, %5, %6 offset:2048"
                 : "=&v"(q[0][0]), "=&v"(q[0][1]), "=&v"(q[1][0]), "=&v"(q[1][1]) : "v"(fo[0]), "v"(fo[1]), "s"(Qf + tq0 * 2048) : "memory");
    bf16x8 sel;
    { const short one = (((lane >> 2) & 1) == ((lane >> 4) & 1)) ? (short)0x3f80 : (short)0; sel = (bf16x8){one, one, one, one, one, one, one, one}; }
    const lds_cptr kp0 = (lds_cptr)smem + fo[0], kp1 = (lds_cptr)smem + fo[1];
    lds_cptr vp0, vp1;
    { const int h = lane >> 5, c = 2 * ((lane >> 4) & 1) + ((lane >> 1) & 1), q4 = (lane >> 2) & 3;
      vp0 = (lds_cptr)smem + 8192 + c * 256 + (h ^ c) * 64 + q4 * 16 + (lane & 1) * 8; vp1 = (lds_cptr)smem + 8192 + c * 256 + ((2 + h) ^ c) * 64 + q4 * 16 + (lane & 1) * 8; }
    const char* Kw = Kf + wid * 2048;
    const char* Vw = Vr + wid * 2048;
    volatile LAS unsigned* flag = (volatile LAS unsigned*)(smem);
    const unsigned ooff = ((lane >> 4) & 1) * 1024 + (lane >> 5) * 256 + (lane & 15) * 16;
#define AT_DMA(ST, SLOT) do { const unsigned d_ = lds0 + (SLOT) + wid * 2048; \
        dma_s2(voff, Kf + (ST) * 8192 + wid * 2048, d_); dma_s2(voff, Vr + (ST) * 8192 + wid * 2048, d_ + 8192); } while (0)
    for (int attempt = 0; attempt < 2; ++attempt) {
        f32x16 o[2]; f32x4 l[2];
        bf16x8 k0, k1;
        asm volatile("global_load_dwordx4 %0, %2, %4\n\tglobal_load_dwordx4 %1, %3, %4" : "=&v"(k0), "=&v"(k1) : "v"(fo[0]), "v"(fo[1]), "s"(Kf) : "memory");
        AT_DMA(0, 0); AT_DMA(1, 16384); AT_DMA(2, 32768);
        o[0] = (f32x16){}; o[1] = (f32x16){}; l[0] = (f32x4){}; l[1] = (f32x4){};
        PIN(o[0]); PIN(o[1]); PIN(l[0]); PIN(l[1]);
        asm volatile("s_waitcnt vmcnt(12)" : "+v"(k0), "+v"(k1), "+v"(q[0][0]), "+v"(q[0][1]), "+v"(q[1][0]), "+v"(q[1][1])::"memory");
        f32x16 negm[2];
        {
            float m0 = -INFINITY, m1 = -INFINITY;
            const int nt = attempt == 0 ? 1 : 64;
            for (int kt = 0; kt < nt; ++kt) {
                if (kt) { k0 = *(const bf16x8*)(Kf + kt * 2048 + fo[0]); k1 = *(const bf16x8*)(Kf + kt * 2048 + fo[1]); }
                f32x16 s0 = MF32(k0, q[0][0], (f32x16){}), s1 = MF32(k0, q[1][0], (f32x16){});
                s0 = MF32(k1, q[0][1], s0); s1 = MF32(k1, q[1][1], s1);
#pragma unroll
                for (int r = 0; r < 16; ++r) { m0 = fmaxf(m0, s0[r]); m1 = fmaxf(m1, s1[r]); }
            }
            m0 = fmaxf(m0, __shfl_xor(m0, 32)); m1 = fmaxf(m1, __shfl_xor(m1, 32));
#pragma unroll
            for (int r = 0; r < 16; ++r) { negm[0][r] = -m0; negm[1][r] = -m1; }
            PIN(negm[0]); PIN(negm[1]);
        }
        asm volatile("s_waitcnt vmcnt(0)\n\ts_barrier" ::: "memory");
        bf16x8 kf0 = *(const LAS bf16x8*)(kp0), kf1 = *(const LAS bf16x8*)(kp1);
        bf16x8 vf0 = (bf16x8){}, vf1 = (bf16x8){};
        u32x4 pa0 = (u32x4){}, pa1 = (u32x4){}, pb0 = (u32x4){}, pb1 = (u32x4){};
        f32x16 sA = MF32(kf0, q[0][0], negm[0]); sA = MF32(kf1, q[0][1], sA);
        f32x16 sB;
#define EX(X, i) X[i] = __builtin_amdgcn_exp2f(X[i])
#define PK(X, i) cvtpk(X[i], X[(i) + 1])
#define BC8(x) __builtin_bit_cast(bf16x8, x)
#define AT_UNIT(SC, PC0, PC1, SN, QN, NM, PP0, PP1, OP, LP, K0, K1, V0, V1, L0, L1, L2, L3) do { unsigned c0, c1, c2, c3, c4, c5, c6, c7; \
            SN = MF32(K0, QN[0], NM); SCHED(); L0; SCHED(); EX(SC, 0); EX(SC, 1); EX(SC, 2); SCHED(); \
            OP = MF32(V0, BC8(PP0), OP); SCHED(); L1; SCHED(); c0 = PK(SC, 0); EX(SC, 3); EX(SC, 4); EX(SC, 5); SCHED(); \
            SN = MF32(K1, QN[1], SN); SCHED(); L2; SCHED(); c1 = PK(SC, 2); c2 = PK(SC, 4); EX(SC, 6); EX(SC, 7); EX(SC, 8); SCHED(); \
            OP = MF32(V1, BC8(PP1), OP); SCHED(); L3; SCHED(); c3 = PK(SC, 6); EX(SC, 9); EX(SC, 10); EX(SC, 11); SCHED(); \
            MF16_ACC(LP, sel, PP0); SCHED(); c4 = PK(SC, 8); c5 = PK(SC, 10); EX(SC, 12); EX(SC, 13); SCHED(); \
            MF16_ACC(LP, sel, PP1); SCHED(); EX(SC, 14); EX(SC, 15); SCHED(); c6 = PK(SC, 12); SCHED(); c7 = PK(SC, 14); \
            PC0 = (u32x4){c0, c1, c2, c3}; PC1 = (u32x4){c4, c5, c6, c7}; SCHED(); } while (0)
#define AT_M0(SLOT, V) asm volatile("s_mov_b32 m0, %0" ::"s"(lds0 + (SLOT) + wid * 2048 + (V) * 8192) : "memory")
#define AT_PAIR(ST, V) asm volatile("global_load_lds_dwordx4 %0, %1\n\tglobal_load_lds_dwordx4 %0, %1 offset:1024" ::"v"(voff), "s"(((V) ? Vw : Kw) + ((ST) & 15) * 8192) : "memory")
#define AT_STAGE(SO, SN_, DM, ST, SLA, SLB) do { _Pragma("unroll") for (int kt = 0; kt < 4; ++kt) { \
                const int ko = kt < 3 ? (SO) + (kt + 1) * 2048 : (SN_), vo = (SO) + kt * 2048; \
                bf16x8 kn0, kn1, vn0, vn1; \
                AT_UNIT(sA, pa0, pa1, sB, q[1], negm[1], pb0, pb1, o[1], l[1], kf0, kf1, vf0, vf1, \
                        (kn0 = *(const LAS bf16x8*)(kp0 + ko), kn1 = *(const LAS bf16x8*)(kp1 + ko)), vn0 = cat8(tr_read(vp0 + vo), tr_read(vp1 + vo)), \
                        vn1 = cat8(tr_read(vp0 + vo + 1024), tr_read(vp1 + vo + 1024)), {}); \
                kf0 = kn0; kf1 = kn1; vf0 = vn0; vf1 = vn1; \
                __builtin_amdgcn_s_waitcnt(0xC07F); SCHED(); \
                AT_UNIT(sB, pb0, pb1, sA, q[0], negm[0], pa0, pa1, o[0], l[0], kf0, kf1, vf0, vf1, \
                        if (DM) AT_M0((kt >> 1) ? (SLB) : (SLA), kt & 1), if (DM) AT_PAIR((ST) + (kt >> 1), kt & 1), {}, {}); } } while (0)
        int s0 = 0, s1 = 16384, s2 = 32768, s3 = 49152, s4 = 65536;
        for (int k = 0; k < 8; ++k) {
            AT_STAGE(s0, s1, true, 2 * k + 3, s3, s4);
            AT_STAGE(s1, s2, false, 0, 0, 0);
            { const int t0 = s0, t1 = s1; s0 = s2; s1 = s3; s2 = s4; s3 = t0; s4 = t1; }
            asm volatile("s_waitcnt vmcnt(0)\n\ts_barrier" ::: "memory");
        }
#undef AT_STAGE
#undef AT_M0
#undef AT_PAIR
        o[1] = MF32(vf0, BC8(pb0), o[1]); o[1] = MF32(vf1, BC8(pb1), o[1]);
        l[1] = MF16(sel, BC8(pb0), l[1]); l[1] = MF16(sel, BC8(pb1), l[1]);
#undef AT_UNIT
        asm volatile("s_nop 7\n\ts_nop 7" : "+v"(l[0]), "+v"(l[1]), "+v"(o[0]), "+v"(o[1]));
        float tsum = 0.f;
#pragma unroll
        for (int i = 0; i < 2; ++i) {
            const float r = 1.0f / l[i][0];
#pragma unroll
            for (int e = 0; e < 16; ++e) { tsum += fabsf(o[i][e]); o[i][e] *= r; }
        }
        const bool bad = !(l[0][0] < 1.0995e12f) || !(l[1][0] < 1.0995e12f) || !(tsum < INFINITY);
        if (lane == 0) flag[wid] = 0u;
        if (bad) flag[wid] = 1u;
#pragma unroll
        for (int i = 0; i < 2; ++i)
#pragma unroll
            for (int s = 0; s < 2; ++s) {
                unsigned a0 = cvtpk(o[i][8 * s + 0], o[i][8 * s + 1]), a1 = cvtpk(o[i][8 * s + 2], o[i][8 * s + 3]);
                unsigned b0 = cvtpk(o[i][8 * s + 4], o[i][8 * s + 5]), b1 = cvtpk(o[i][8 * s + 6], o[i][8 * s + 7]);
                swap32(a0, b0); swap32(a1, b1);
                const u32x4 w = (u32x4){a0, a1, b0, b1};
                asm volatile("global_store_dwordx4 %0, %1, %2 sc1\n\ts_nop 1" ::"v"(ooff), "v"(w), "s"(act + (size_t)(112 + head) * 131072 + (tq0 + i) * 2048 + s * 512) : "memory");
            }
        asm volatile("s_waitcnt vmcnt(0) lgkmcnt(0)\n\ts_barrier" ::: "memory");
        const unsigned redo = flag[0] | flag[1] | flag[2] | flag[3];
        if (attempt == 1 || !__builtin_amdgcn_readfirstlane(redo)) break;
        asm volatile("s_waitcnt lgkmcnt(0)\n\ts_barrier" ::: "memory");
    }
#undef AT_DMA
    if (tid == 0) __hip_atomic_fetch_add(ctr + qb * 64, 1u, __ATOMIC_RELAXED, __HIP_MEMORY_SCOPE_AGENT);

}

__device__ __forceinline__ void gemm2_role(char* smem, const int bx, const char* __restrict__ act, const char* __restrict__ w2, const char* __restrict__ wo,
                                           const char* __restrict__ x, char* __restrict__ out, unsigned* __restrict__ ctr) {

    const int tid = threadIdx.x, lane = tid & 63;
    const int wid = __builtin_amdgcn_readfirstlane(tid >> 6);
    const int xc = bx & 7, ii = bx >> 3, tm = (xc >> 1) * 4 + (ii >> 3), tn = (xc & 1) * 8 + (ii & 7);
    const unsigned lds0 = (unsigned)(size_t)(LAS char*)smem + wid * 16384;
    __builtin_amdgcn_s_sleep(127);
    const char* asrc = act + (size_t)wid * 131072 + tm * 8192;
    unsigned av[4];
#pragma unroll
    for (int i = 0; i < 4; ++i) av[i] = (i ^ wid) * 2048 + lane * 16;
    const char* wsrc = w2 + (size_t)wid * 32 * 2048 + tn * 128;
    const unsigned wv0 = (lane >> 3) * 2048 + ((((lane >> 1) & 3) ^ ((lane >> 4) & 1)) * 32) + (lane & 1) * 16;
    const unsigned wv1 = (lane >> 3) * 2048 + ((((lane >> 1) & 3) ^ ((lane >> 4) & 1) ^ 2) * 32) + (lane & 1) * 16 + 16384 - 1024;
    lds_cptr wp[4];
    {
        const int g = lane >> 4, q = (lane >> 2) & 3, p = lane & 3, f = (q >> 1) | ((g & 1) << 1);
#pragma unroll
        for (int c = 0; c < 4; ++c) wp[c] = (lds_cptr)smem + wid * 16384 + g * 1024 + q * 128 + ((c ^ f) << 5) + p * 8;
    }
    f32x4 acc[8][4];
#pragma unroll
    for (int i = 0; i < 8; ++i)
#pragma unroll
        for (int n = 0; n < 4; ++n) { acc[i][n] = (f32x4){}; PIN(acc[i][n]); }
    bf16x8 a0[4], a1[4], a2[4], a3[4], wfa[4], wfb[4];
#define G2_LDA(A, H) do { \
        asm volatile("global_load_dwordx4 %0, %4, %6\n\tglobal_load_dwordx4 %1, %4, %6 offset:1024\n\tglobal_load_dwordx4 %2, %5, %6\n\tglobal_load_dwordx4 %3, %5, %6 offset:1024" \
                     : "=&v"(A[0]), "=&v"(A[1]), "=&v"(A[2]), "=&v"(A[3]) : "v"(av[2 * (H)]), "v"(av[2 * (H) + 1]), "s"(asrc) : "memory"); \
        if (H) asrc += 524288; } while (0)
#define G2_LDW(SL) do {     \
        asm volatile("s_mov_b32 m0, %3\n\ts_nop 0\n\tglobal_load_lds_dwordx4 %0, %2\n\tglobal_load_lds_dwordx4 %1, %2 offset:1024" \
                     ::"v"(wv0), "v"(wv1), "s"(wsrc), "s"(lds0 + (SL) * 2048) : "memory"); \
        wsrc += ((SL) & 1) ? 262144 - 32768 : 32768; } while (0)
#define G2_WAIT(N, A) asm volatile("s_waitcnt vmcnt(" #N ")" : "+v"(A[0]), "+v"(A[1]), "+v"(A[2]), "+v"(A[3])::"memory")
#define WFR(P, O) cat8(tr_read((P) + (O)), tr_read((P) + (O) + 512))
#define G2_STEP(N, AC, AN, WC, WN, SL, RD, IA, IW) do { \
        if ((N) >= 0) G2_WAIT(N, AN); \
        if (RD) { WN[2 * ((SL) & 1)] = WFR(wp[2 * ((SL) & 1)], ((((SL) >> 1) + 1) & 3) * 4096); WN[2 * ((SL) & 1) + 1] = WFR(wp[2 * ((SL) & 1) + 1], ((((SL) >> 1) + 1) & 3) * 4096); } \
        SCHED(); \
        _Pragma("unroll") for (int i = 0; i < 16; ++i) { \
            MF16_ACC(acc[4 * ((SL) & 1) + (i >> 2)][((i >> 2) & 1) ? 3 - (i & 3) : (i & 3)], WC[((i >> 2) & 1) ? 3 - (i & 3) : (i & 3)], AC[i >> 2]); \
            if ((IA) && (i & 3) == 3) asm volatile("global_load_dwordx4 %0, %1, %2 offset:%c3" : "=&v"(AC[i >> 2]) : "v"(av[2 * ((SL) & 1) + (i >> 3)]), "s"(asrc), "i"(((i >> 2) & 1) * 1024) : "memory"); } \
        SCHED(); if ((IA) && ((SL) & 1)) asrc += 524288; if (IW) G2_LDW(SL); } while (0)
#define G2_STEP4(N0, N1, N2, N3, SL, RDL, IA, IW) do { \
        G2_STEP(N0, a0, a1, wfa, wfb, (SL), true, IA, IW); G2_STEP(N1, a1, a2, wfa, wfb, (SL) + 1, true, IA, IW); \
        G2_STEP(N2, a2, a3, wfb, wfa, (SL) + 2, RDL, IA, IW); G2_STEP(N3, a3, a0, wfb, wfa, (SL) + 3, RDL, IA, IW); } while (0)
    G2_LDW(0); G2_LDW(1); G2_LDW(2); G2_LDW(3);
    G2_LDA(a0, 0); G2_LDW(4); G2_LDA(a1, 1); G2_LDW(5); G2_LDA(a2, 0); G2_LDW(6); G2_LDA(a3, 1); G2_LDW(7);
    G2_WAIT(20, a0);
#pragma unroll
    for (int c = 0; c < 4; ++c) wfa[c] = WFR(wp[c], 0);
    for (int it = 0; it < 6; ++it) { G2_STEP4(14, 14, 14, 14, 0, true, true, true); G2_STEP4(14, 14, 14, 14, 4, true, true, true); }
    G2_STEP4(14, 12, 10, 8, 0, true, true, false);
    G2_STEP4(8, 4, 0, -1, 4, false, false, false);
#undef G2_STEP4
#undef G2_STEP
#undef G2_WAIT
#undef G2_LDW
#undef G2_LDA
    const int fr = lane & 15, fq = lane >> 4;
    const size_t orow = (size_t)(tm * 128 + wid * 32 + fr) * 2048 + tn * 128 + (fq & 1) * 32 + (fq >> 1) * 16;
    u32x4 xr[4];
#pragma unroll
    for (int c = 0; c < 4; ++c) xr[c] = *(const u32x4*)(x + orow + (c >> 1) * 32768 + (c & 1) * 64);
    asm volatile("s_nop 7" : "+v"(acc[7][0]), "+v"(acc[7][1]), "+v"(acc[7][2]), "+v"(acc[7][3]));
    SCHED();
    char* mine = smem + wid * 16384 + lane * 16;
#pragma unroll
    for (int i = 0; i < 16; ++i) *(f32x4*)(mine + i * 1024) = acc[4 + (i >> 2)][i & 3];
    __syncthreads();
#pragma unroll
    for (int i = 0; i < 16; ++i) acc[i >> 2][i & 3] += *(const f32x4*)(smem + (wid ^ 2) * 16384 + lane * 16 + i * 1024);
    __syncthreads();
#pragma unroll
    for (int i = 0; i < 8; ++i) *(f32x4*)(mine + i * 1024) = acc[2 + (i >> 2)][i & 3];
    __syncthreads();
#pragma unroll
    for (int i = 0; i < 8; ++i) { acc[i >> 2][i & 3] += *(const f32x4*)(smem + (wid ^ 1) * 16384 + lane * 16 + i * 1024); PIN(acc[i >> 2][i & 3]); }
    __syncthreads();
    const unsigned ldsb = (unsigned)(size_t)(LAS char*)smem;
    const char* wos = wo + tn * 128;
    const char* osrc = act + (size_t)112 * 131072 + tm * 8192 + wid * 2048;
    unsigned ov = lane * 16;
    lds_cptr wq[4];
#pragma unroll
    for (int c = 0; c < 4; ++c) { wq[c] = wp[c] - wid * 16384; PIN(wq[c]); }
    PIN(ov);
#define G3_LDW(HS, SLOT) asm volatile("s_mov_b32 m0, %3\n\ts_nop 0\n\tglobal_load_lds_dwordx4 %0, %2\n\tglobal_load_lds_dwordx4 %1, %2 offset:1024" \
                     ::"v"(wv0), "v"(wv1), "s"(wos + (size_t)(HS) * 32768), "s"(ldsb + (SLOT) * 2048) : "memory")
#pragma unroll
    for (int i = 0; i < 10; ++i) G3_LDW(wid * 10 + i, wid * 10 + i);
    if (wid == 0) {
        const unsigned long long t0 = __builtin_amdgcn_s_memrealtime();
        while ((unsigned)__builtin_amdgcn_readfirstlane(__hip_atomic_load(ctr + (tm >> 1) * 64, __ATOMIC_RELAXED, __HIP_MEMORY_SCOPE_AGENT)) < 32u) {
            if (__builtin_amdgcn_s_memrealtime() - t0 > 5000000ull) break;
            __builtin_amdgcn_s_sleep(2);
        }
        __builtin_amdgcn_fence(__ATOMIC_ACQUIRE, "wavefront");
        asm volatile("" ::: "memory");
    }
    asm volatile("s_waitcnt vmcnt(0)\n\ts_barrier" ::: "memory");

    bf16x8 oa[8][4];
#define G3_LDA(G) asm volatile("global_load_dwordx4 %0, %4, %5 sc1\n\tglobal_load_dwordx4 %1, %4, %5 offset:1024 sc1\n\tglobal_load_dwordx4 %2, %4, %6 sc1\n\tglobal_load_dwordx4 %3, %4, %6 offset:1024 sc1" \
                     : "=&v"(oa[(G) & 7][0]), "=&v"(oa[(G) & 7][1]), "=&v"(oa[(G) & 7][2]), "=&v"(oa[(G) & 7][3]) \
                     : "v"(ov), "s"(osrc + (size_t)(G) * 262144), "s"(osrc + (size_t)(G) * 262144 + 131072) : "memory")
#define G3_WAIT(N, G) asm volatile("s_waitcnt vmcnt(" #N ")" : "+v"(oa[(G) & 7][0]), "+v"(oa[(G) & 7][1]), "+v"(oa[(G) & 7][2]), "+v"(oa[(G) & 7][3])::"memory")
    s16x4 wl[2][8], wh[2][8];
#define G3_WA(G, i) (wq[(i) & 3] + (((G) >= 8 && (G) < 10) ? 65536 : 0) + ((((G) * 2 + ((i) >> 2)) % 20) * 4096 - (((G) >= 8 && (G) < 10) ? 65536 : 0)))
#define G3_LD1(G, j) asm volatile("global_load_dwordx4 %0, %1, %2 offset:%c3 sc1" : "=&v"(oa[(G) & 7][j]) : "v"(ov), "s"(osrc + (size_t)(G) * 262144 + ((j) >> 1) * 131072), "i"(((j) & 1) * 1024) : "memory")
#define G3_GROUP(N, G, RD) do { G3_WAIT(N, G); SCHED(); \
        const bool rf_ = (G) == 5 || (G) == 6; \
        _Pragma("unroll") for (int i = 0; i < 8; ++i) { const bf16x8 w_ = cat8(wl[(G) & 1][i], wh[(G) & 1][i]); \
            MF16_ACC(acc[0][i & 3], w_, oa[(G) & 7][2 * (i >> 2)]); SCHED(); if (RD) wl[((G) + 1) & 1][i] = tr_read(G3_WA((G) + 1, i)); SCHED(); \
            MF16_ACC(acc[1][i & 3], w_, oa[(G) & 7][2 * (i >> 2) + 1]); SCHED(); if (RD) wh[((G) + 1) & 1][i] = tr_read(G3_WA((G) + 1, i) + 512); \
            if (rf_ && !(i & 1) && i < 6) G3_LDW(40 + wid * 6 + ((G) - 5) * 3 + (i >> 1), wid * 6 + ((G) - 5) * 3 + (i >> 1)); \
            if ((G) < 11 && (i & 1)) G3_LD1((G) + 5, i >> 1); SCHED(); } } while (0)
    G3_LDA(0); G3_LDA(1); G3_LDA(2); G3_LDA(3); G3_LDA(4);
#pragma unroll
    for (int i = 0; i < 8; ++i) { wl[0][i] = tr_read(G3_WA(0, i)); wh[0][i] = tr_read(G3_WA(0, i) + 512); }
    G3_GROUP(16, 0, true); G3_GROUP(16, 1, true); G3_GROUP(16, 2, true); G3_GROUP(16, 3, true); G3_GROUP(16, 4, true);

    asm volatile("s_waitcnt lgkmcnt(0)\n\ts_barrier" ::: "memory");
    G3_GROUP(16, 5, true); G3_GROUP(22, 6, true); G3_GROUP(28, 7, true); G3_GROUP(28, 8, true);

    asm volatile("s_waitcnt vmcnt(10)\n\ts_barrier" ::: "memory");

    G3_GROUP(10, 9, true); G3_GROUP(22, 10, true); G3_GROUP(16, 11, true); G3_GROUP(12, 12, true); G3_GROUP(8, 13, true); G3_GROUP(4, 14, true); G3_GROUP(0, 15, false);

#undef G3_GROUP
#undef G3_WA
#undef G3_WAIT
#undef G3_LDA
#undef G3_LD1
#undef G3_LDW
#undef WFR
    asm volatile("s_nop 7" : "+v"(acc[0][0]), "+v"(acc[0][1]), "+v"(acc[0][2]), "+v"(acc[0][3]), "+v"(acc[1][0]), "+v"(acc[1][1]), "+v"(acc[1][2]), "+v"(acc[1][3]));
#pragma unroll
    for (int c = 0; c < 4; ++c) {
        float v[8];
#pragma unroll
        for (int e = 0; e < 4; ++e) {
            unsigned a = __float_as_uint(acc[c >> 1][2 * (c & 1)][e]), b = __float_as_uint(acc[c >> 1][2 * (c & 1) + 1][e]);
            swap16(a, b);
            v[e] = __uint_as_float(a); v[4 + e] = __uint_as_float(b);
        }
        u32x4 w;
#pragma unroll
        for (int e = 0; e < 4; ++e) w[e] = cvtpk(v[2 * e] + bf_lo(xr[c][e]), v[2 * e + 1] + bf_hi(xr[c][e]));
        *(u32x4*)(out + orow + (c >> 1) * 32768 + (c & 1) * 64) = w;
    }

}

extern "C" __global__ __launch_bounds__(256, 2) void k_fused(const char* __restrict__ qkv, char* __restrict__ act, const char* __restrict__ w2, const char* __restrict__ wo,
                                                  const char* __restrict__ x, char* __restrict__ out, unsigned* __restrict__ ctr) {
    __shared__ __attribute__((aligned(1024))) char smem[81920];
    asm volatile("" ::"s"(qkv), "s"(act), "s"(w2));
    if (__builtin_expect(blockIdx.x < 256, 1)) attn_role(smem, blockIdx.x, qkv, act, ctr);
    else gemm2_role(smem, blockIdx.x - 256, act, w2, wo, x, out, ctr);
}

extern "C" __global__ void k_end() {}

extern "C" void kernel_launch(void* const* d_in, const int* in_sizes, int n_in, void* d_out, int out_size, void* d_ws, size_t ws_size,
                              hipStream_t stream) {
    const u16* x = (const u16*)d_in[0];
    const char* wqkv = (const char*)d_in[1];
    const char* wo = (const char*)d_in[2];
    const char* w1 = (const char*)d_in[4];
    const char* w3 = (const char*)d_in[5];
    const char* w2 = (const char*)d_in[6];
    char* ws = (char*)d_ws;
    k_ln<<<512, 512, 0, stream>>>(x, ws + WS_XN, (unsigned*)(ws + WS_CTR), wqkv, w1, w3);
    k_gemm1<<<256, 512, 0, stream>>>(ws + WS_XN, wqkv, w1, w3, ws + WS_QKV, ws + WS_ACT, w2, wo);
    k_fused<<<512, 256, 0, stream>>>(ws + WS_QKV, ws + WS_ACT, w2, wo, (const char*)x, (char*)d_out, (unsigned*)(ws + WS_CTR));
}
```

```cpp
#include <hip/hip_runtime.h>
#include <stdint.h>

typedef unsigned short u16;
typedef short bf16x8 __attribute__((ext_vector_type(8)));
typedef short s16x4 __attribute__((ext_vector_type(4)));
typedef float f32x2 __attribute__((ext_vector_type(2)));
typedef float f32x4 __attribute__((ext_vector_type(4)));
typedef float f32x16 __attribute__((ext_vector_type(16)));
typedef unsigned u32x2 __attribute__((ext_vector_type(2)));
typedef unsigned u32x4 __attribute__((ext_vector_type(4)));
typedef __bf16 bf16x2_t __attribute__((ext_vector_type(2)));
#define LAS __attribute__((address_space(3)))
typedef const LAS char* lds_cptr;

constexpr int DM = 1024;
constexpr size_t MB = 1024 * 1024;
constexpr size_t WS_CTR = 0;
constexpr size_t WS_XN = 65536;
constexpr size_t WS_QKV = WS_XN + 4 * MB;
constexpr size_t WS_ACT = WS_QKV + 12 * MB;
constexpr float QSCALE = 0.17677669529663687f * 1.4426950408889634f;

#define SCHED() __builtin_amdgcn_sched_barrier(0)
#define PIN(x) asm volatile("" : "+v"(x))
#define MF32(a, b, c) __builtin_amdgcn_mfma_f32_32x32x16_bf16(a, b, c, 0, 0, 0)
#define MF16(a, b, c) __builtin_amdgcn_mfma_f32_16x16x32_bf16(a, b, c, 0, 0, 0)

#define MF16_ACC(acc, a, b) asm volatile("v_mfma_f32_16x16x32_bf16 %0, %1, %2, %0" : "+v"(acc) : "v"(a), "v"(b))

__device__ __forceinline__ unsigned cvtpk(float lo, float hi) {
    f32x2 v = {lo, hi};
    bf16x2_t b = __builtin_convertvector(v, bf16x2_t);
    return __builtin_bit_cast(unsigned, b);
}
__device__ __forceinline__ float bf_lo(unsigned w) { return __uint_as_float(w << 16); }
__device__ __forceinline__ float bf_hi(unsigned w) { return __uint_as_float(w & 0xffff0000u); }
__device__ __forceinline__ float rbf(float x) { return bf_lo(cvtpk(x, x)); }
__device__ __forceinline__ s16x4 tr_read(lds_cptr p) {
    return __builtin_bit_cast(s16x4, __builtin_amdgcn_ds_read_tr16_b64_v4i16((LAS s16x4*)p));
}
__device__ __forceinline__ bf16x8 cat8(s16x4 lo, s16x4 hi) { return __builtin_shufflevector(lo, hi, 0, 1, 2, 3, 4, 5, 6, 7); }
__device__ __forceinline__ void dma_s1(unsigned voff, const void* sbase, unsigned lds) {
    asm volatile("s_mov_b32 m0, %2\n\ts_nop 0\n\tglobal_load_lds_dwordx4 %0, %1" ::"v"(voff), "s"(sbase), "s"(lds) : "memory");
}
__device__ __forceinline__ void dma_s2(unsigned voff, const void* sbase, unsigned lds) {
    asm volatile("s_mov_b32 m0, %2\n\ts_nop 0\n\tglobal_load_lds_dwordx4 %0, %1\n\tglobal_load_lds_dwordx4 %0, %1 offset:1024" ::"v"(voff), "s"(sbase), "s"(lds) : "memory");
}
__device__ __forceinline__ void swap16(unsigned& a, unsigned& b) {
    auto r = __builtin_amdgcn_permlane16_swap(a, b, false, false);
    a = r[0]; b = r[1];
}
__device__ __forceinline__ void swap32(unsigned& a, unsigned& b) {
    auto r = __builtin_amdgcn_permlane32_swap(a, b, false, false);
    a = r[0]; b = r[1];
}

#define ST16(p, v) asm volatile("global_store_dwordx4 %0, %1, off sc1\n\ts_nop 1" ::"v"(p), "v"(v) : "memory")

__device__ __forceinline__ float wave_sum(float v) {
    v += __builtin_bit_cast(float, __builtin_amdgcn_update_dpp(0, __builtin_bit_cast(int, v), 0xB1, 0xf, 0xf, true));
    v += __builtin_bit_cast(float, __builtin_amdgcn_update_dpp(0, __builtin_bit_cast(int, v), 0x4E, 0xf, 0xf, true));
    v += __builtin_bit_cast(float, __builtin_amdgcn_update_dpp(0, __builtin_bit_cast(int, v), 0x141, 0xf, 0xf, true));
    v += __builtin_bit_cast(float, __builtin_amdgcn_update_dpp(0, __builtin_bit_cast(int, v), 0x140, 0xf, 0xf, true));
    auto a = __builtin_amdgcn_permlane16_swap(__float_as_uint(v), __float_as_uint(v), false, false);
    v = __uint_as_float(a[0]) + __uint_as_float(a[1]);
    auto b = __builtin_amdgcn_permlane32_swap(__float_as_uint(v), __float_as_uint(v), false, false);
    return __uint_as_float(b[0]) + __uint_as_float(b[1]);
}
#define CODE_ADDR(var, SYM) uint64_t var; \
    asm volatile("s_getpc_b64 s[20:21]\n\ts_add_u32 s20, s20, " SYM "@rel32@lo+4\n\ts_addc_u32 s21, s21, " SYM "@rel32@hi+12\n\ts_mov_b64 %0, s[20:21]" : "=s"(var)::"s20", "s21", "scc")
extern "C" __global__ __launch_bounds__(512) void k_ln(const u16* __restrict__ x, char* __restrict__ xnF, unsigned* __restrict__ ctr,
                                            const char* __restrict__ wqkv, const char* __restrict__ w1, const char* __restrict__ w3) {
    __shared__ __attribute__((aligned(16))) char sm[16384];
    const int tid = threadIdx.x, lane = tid & 63, wid = tid >> 6;
    if (blockIdx.x >= 256) {
        const size_t o = (size_t)((blockIdx.x - 256) * 512 + tid) * 16;
        const u32x4 p0 = *(const u32x4*)(wqkv + o), p1 = *(const u32x4*)(w1 + o), p2 = *(const u32x4*)(w3 + o);
        CODE_ADDR(c0, "k_gemm1"); CODE_ADDR(c1, "k_end");
        const uint64_t ca = c0 + ((blockIdx.x - 256) >> 3) * 1024;
        u32x4 p3 = p0;
        if (tid < 64 && ca + 1024 <= c1) p3 = *(const __attribute__((address_space(1))) u32x4*)(ca + tid * 16);
        asm volatile("" ::"v"(p0), "v"(p1), "v"(p2), "v"(p3));
        return;
    }
    if (blockIdx.x == 0) { ctr[tid] = 0u; ctr[tid + 512] = 0u; }
    const int row = blockIdx.x * 8 + wid;
    const u32x4* src = (const u32x4*)(x + (size_t)row * DM);
    const u32x4 a = src[lane], b = src[64 + lane];
    float v[16];
#pragma unroll
    for (int i = 0; i < 4; ++i) { v[2 * i] = bf_lo(a[i]); v[2 * i + 1] = bf_hi(a[i]); v[8 + 2 * i] = bf_lo(b[i]); v[8 + 2 * i + 1] = bf_hi(b[i]); }
    float s = 0.f;
#pragma unroll
    for (int i = 0; i < 16; ++i) s += v[i];
    s = wave_sum(s);
    const float mean = s * (1.0f / DM);
    float q = 0.f;
#pragma unroll
    for (int i = 0; i < 16; ++i) { const float d = v[i] - mean; q += d * d; }
    q = wave_sum(q);
    const float mb = rbf(mean), vb = rbf(q * (1.0f / DM));
    const float rs = 1.0f / rbf(sqrtf(rbf(vb + 1e-5f)));
    u32x4 oa, ob;
#pragma unroll
    for (int i = 0; i < 4; ++i) {
        oa[i] = cvtpk(rbf(v[2 * i] - mb) * rs, rbf(v[2 * i + 1] - mb) * rs);
        ob[i] = cvtpk(rbf(v[8 + 2 * i] - mb) * rs, rbf(v[8 + 2 * i + 1] - mb) * rs);
    }
    *(u32x4*)(sm + lane * 128 + wid * 16) = oa;
    *(u32x4*)(sm + (64 + lane) * 128 + wid * 16) = ob;
    __syncthreads();
    const int row0 = blockIdx.x * 8;
#pragma unroll
    for (int i = 0; i < 2; ++i) {
        const int p = tid + i * 512, chunk = p >> 3, r = p & 7;
        const u32x4 w = *(const u32x4*)(sm + p * 16);
        ST16(xnF + ((size_t)(chunk >> 2) * 128 + (row0 >> 4)) * 1024 + (chunk & 3) * 256 + ((row0 & 15) + r) * 16, w);
    }
}

__device__ __forceinline__ unsigned silu_mul2(f32x2 g, f32x2 u) {
    const f32x2 t = g * -1.4426950408889634f;
    f32x2 e; e.x = __builtin_amdgcn_exp2f(t.x); e.y = __builtin_amdgcn_exp2f(t.y);
    e = e + 1.0f;
    f32x2 r; r.x = __builtin_amdgcn_rcpf(e.x); r.y = __builtin_amdgcn_rcpf(e.y);
    const f32x2 h = (g * u) * r;
    return cvtpk(h.x, h.y);
}
__device__ __forceinline__ u32x2 silu_mul4(f32x4 g, f32x4 u) {
    return (u32x2){silu_mul2((f32x2){g[0], g[1]}, (f32x2){u[0], u[1]}), silu_mul2((f32x2){g[2], g[3]}, (f32x2){u[2], u[3]})};
}
extern "C" __global__ __launch_bounds__(512, 2) void k_gemm1(const char* __restrict__ xnF, const char* __restrict__ wqkv, const char* __restrict__ w1,
                                                  const char* __restrict__ w3, char* __restrict__ qkv, char* __restrict__ act,
                                                  const char* __restrict__ w2, const char* __restrict__ wo) {
    __shared__ __attribute__((aligned(1024))) char smem[147456 + 2048];

    const int tid = threadIdx.x, lane = tid & 63;
    const int wid = __builtin_amdgcn_readfirstlane(tid >> 6), wr = wid & 3, wc = wid >> 2;
    const int bx = blockIdx.x, j = (bx & 7) + 8 * ((bx >> 3) & 3), rb = bx >> 5;
    const unsigned lds0 = (unsigned)(size_t)(LAS char*)smem;
    const bool tq = j < 8;
    const int qb0 = 3 * j, qbx = tq ? 3 * j + 2 : 16 + j, pA = 2 * j - 8, pB = tq ? j : 2 * j - 7;
    const char* bsrc[5]; unsigned binc[5];
#pragma unroll
    for (int i = 0; i < 5; ++i) {
        const int blk = i;
        const bool ffn = blk >= 3 || (blk < 2 && !tq);
        const int pitch = ffn ? 7168 : 6144;
        const uint64_t wg = (blk == 0 || blk == 3) ? (uint64_t)w1 : (uint64_t)w3, wb = ffn ? wg : (uint64_t)wqkv;
        const int cf = blk < 2 ? pA : pB, cq = blk == 2 ? qbx : qb0 + blk, cblk = ffn ? cf : cq;
        bsrc[i] = (const char*)wb + (size_t)(unsigned)(cblk * 128);
        binc[i] = 32u * pitch;
    }
    unsigned bvq, bvf;
    { const int r = lane >> 3, j8 = lane & 7, cp = (j8 >> 1) ^ (((r >> 1) & 1) | ((wr & 1) << 1));
      bvq = (wr * 8 + r) * 6144 + cp * 32 + (j8 & 1) * 16; bvf = (wr * 8 + r) * 7168 + cp * 32 + (j8 & 1) * 16; }
    const unsigned bv01 = tq ? bvq : bvf;
    const char* asrc = xnF + ((size_t)rb * 16 + 4 * wr) * 1024;
    const unsigned avoff = lane * 16;
    const unsigned adst = lds0 + wr * 4096, bdst = lds0 + 65536 + wr * 5120;
#define G1_DMA_B(SL) do { \
        dma_s1(bv01, bsrc[0], bdst + (SL) * 20480); dma_s1(bv01, bsrc[1], bdst + (SL) * 20480 + 1024); dma_s1(bvq, bsrc[2], bdst + (SL) * 20480 + 2048); \
        dma_s1(bvf, bsrc[3], bdst + (SL) * 20480 + 3072); dma_s1(bvf, bsrc[4], bdst + (SL) * 20480 + 4096); \
        _Pragma("unroll") for (int i = 0; i < 5; ++i) bsrc[i] += binc[i]; } while (0)
#define G1_DMA_A(SL) do { dma_s2(avoff, asrc, adst + (SL) * 16384); dma_s2(avoff, asrc + 2048, adst + (SL) * 16384 + 2048); asrc += 131072; } while (0)
#define G1_DMA(SL, VA) do { if (wc == 0) G1_DMA_B(SL); else { G1_DMA_A(SL); asm volatile("s_waitcnt vmcnt(" #VA ")" ::: "memory"); } } while (0)
    if (wc == 0) { G1_DMA_B(0); G1_DMA_B(1); G1_DMA_B(2); } else { G1_DMA_A(0); G1_DMA_A(1); G1_DMA_A(2); }
    lds_cptr pa = (lds_cptr)smem + wr * 4096 + lane * 16;
    lds_cptr pb[4], pe0, pe1;
    {
        const int g = lane >> 4, q = (lane >> 2) & 3, p = lane & 3, f = (q >> 1) | ((g & 1) << 1);
        const lds_cptr b0 = (lds_cptr)smem + 65536 + g * 5120 + q * 128 + p * 8;
#pragma unroll
        for (int c = 0; c < 4; ++c) pb[c] = b0 + ((c ^ f) << 5) + wc * 3072;
        pe0 = b0 + ((wc ^ f) << 5) + 2048; pe1 = b0 + (((2 + wc) ^ f) << 5) + 2048;
    }
    const uint64_t pfa = (uint64_t)w2 + (uint64_t)(bx * 7 + wid) * 4096, pfb = (uint64_t)wo + (uint64_t)bx * 8192;
    const char* pf = (const char*)(wid < 7 ? pfa : pfb);
    PIN(pa); PIN(pb[0]); PIN(pb[1]); PIN(pb[2]); PIN(pb[3]); PIN(pe0); PIN(pe1);
    asm volatile("" ::"s"(pf), "s"(qkv), "s"(act), "s"(wo));
    f32x4 acc[4][10];
#pragma unroll
    for (int m = 0; m < 4; ++m)
#pragma unroll
        for (int n = 0; n < 10; ++n) { acc[m][n] = (f32x4){0.f, 0.f, 0.f, 0.f}; PIN(acc[m][n]); }
    SCHED();
    asm volatile("s_waitcnt vmcnt(8)\n\ts_barrier" ::: "memory");

    if (wc == 1) asm volatile("s_barrier" ::: "memory");
    bf16x8 af[4], bf[10];
#define BFR(P, O) cat8(tr_read((P) + (O)), tr_read((P) + (O) + 512))
#define G1_READS(SL) do { \
        _Pragma("unroll") for (int c = 0; c < 4; ++c) { bf[c] = BFR(pb[c], (SL) * 20480); bf[4 + c] = BFR(pb[c], (SL) * 20480 + 1024); } \
        bf[8] = BFR(pe0, (SL) * 20480); bf[9] = BFR(pe1, (SL) * 20480); \
        _Pragma("unroll") for (int m = 0; m < 4; ++m) af[m] = *(const LAS bf16x8*)(pa + (SL) * 16384 + m * 1024); } while (0)
#define G1_STEP(SL, DMA, VB, ENDBAR) do { \
        G1_READS(SL); DMA; \
        asm volatile("s_waitcnt lgkmcnt(0)" ::: "memory"); \
        asm volatile("s_barrier" ::: "memory"); \
        SCHED(); \
        _Pragma("unroll") for (int i = 0; i < 40; ++i) { const int m_ = ((i >> 2) & 1) ? 3 - (i & 3) : (i & 3); MF16_ACC(acc[m_][i >> 2], bf[i >> 2], af[m_]); } \
        SCHED(); \
        if (ENDBAR) asm volatile("s_waitcnt vmcnt(" #VB ")\n\ts_barrier" ::: "memory"); } while (0)
    for (int it = 0; it < 7; ++it) { G1_STEP(0, G1_DMA(3, 8), 10, true); G1_STEP(1, G1_DMA(0, 8), 10, true); G1_STEP(2, G1_DMA(1, 8), 10, true); G1_STEP(3, G1_DMA(2, 8), 10, true); }
    G1_STEP(0, G1_DMA(3, 8), 10, true);
    G1_STEP(1, { dma_s2(avoff, pf, lds0 + 147456); dma_s2(avoff, pf + 2048, lds0 + 147456); if (wc == 1) asm volatile("s_waitcnt vmcnt(8)" ::: "memory"); }, 9, true);
    G1_STEP(2, { if (wc == 1) asm volatile("s_waitcnt vmcnt(4)" ::: "memory"); }, 4, true);
    {
        CODE_ADDR(cf0, "k_fused"); CODE_ADDR(cf1, "k_end");
        const uint64_t pc = cf0 + (uint64_t)(((bx >> 3) * 4 + wr) & 31) * 1024, po = (uint64_t)wo + (uint64_t)bx * 8192 + 4096 + wr * 1024;
        const char* pl = (const char*)((wc == 1 && pc + 1024 <= cf1) ? pc : po);
        G1_STEP(3, dma_s1(avoff, pl, lds0 + 147456), 63, wc == 0);
    }

#undef G1_STEP
#undef G1_READS
#undef BFR
#undef G1_DMA
#undef G1_DMA_A
#undef G1_DMA_B
    asm volatile("s_nop 7" : "+v"(acc[0][9]), "+v"(acc[1][9]), "+v"(acc[2][9]), "+v"(acc[3][9]), "+v"(acc[3][8]), "+v"(acc[2][8]));
    SCHED();
    int tz = threadIdx.x; asm volatile("" : "+v"(tz));
    const int fr = tz & 15, fq = (tz >> 4) & 3;
    const int tb = (rb * 16 + wr * 4) * 1024;
    const bool ffn_main = !tq || wc == 1;
    const int cm = ((fq & 1) << 1) | (fq >> 1), ce = 2 * wc + (fq >> 1);
    {
        const int cb0 = 2 * qbx;
        const float sc = cb0 < 32 ? QSCALE : 1.0f;
        char* dst = qkv + (size_t)(cb0 + (fq & 1)) * 131072 + tb + ce * 256 + ((fr ^ (4 * ce)) * 16);
#pragma unroll
        for (int m = 0; m < 4; ++m) {
            const f32x4 va = acc[m][8] * sc, vb = acc[m][9] * sc;
            unsigned a0 = cvtpk(va[0], va[1]), a1 = cvtpk(va[2], va[3]), b0 = cvtpk(vb[0], vb[1]), b1 = cvtpk(vb[2], vb[3]);
            swap16(a0, b0); swap16(a1, b1);
            ST16(dst + m * 1024, ((u32x4){a0, a1, b0, b1}));
        }
    }
    if (ffn_main) {
        char* dst = act + (size_t)(wc ? pB : pA) * 262144 + tb + cm * 256 + fr * 16;
#pragma unroll
        for (int m = 0; m < 4; ++m)
#pragma unroll
            for (int sub = 0; sub < 2; ++sub) {
                const u32x2 a = silu_mul4(acc[m][2 * sub], acc[m][4 + 2 * sub]), b = silu_mul4(acc[m][2 * sub + 1], acc[m][4 + 2 * sub + 1]);
                unsigned a0 = a[0], a1 = a[1], b0 = b[0], b1 = b[1];
                swap16(a0, b0); swap16(a1, b1);
                ST16(dst + sub * 131072 + m * 1024, ((u32x4){a0, a1, b0, b1}));
            }
    } else {
        const int lo = tb + cm * 256 + ((fr ^ (4 * cm)) * 16);
#pragma unroll
        for (int sub = 0; sub < 4; ++sub) {
            const int cb = 2 * qb0 + sub;
            const float sc = cb < 32 ? QSCALE : 1.0f;
            char* dst = qkv + (size_t)cb * 131072 + lo;
#pragma unroll
            for (int m = 0; m < 4; ++m) {
                const f32x4 va = acc[m][2 * sub] * sc, vb = acc[m][2 * sub + 1] * sc;
                unsigned a0 = cvtpk(va[0], va[1]), a1 = cvtpk(va[2], va[3]), b0 = cvtpk(vb[0], vb[1]), b1 = cvtpk(vb[2], vb[3]);
                swap16(a0, b0); swap16(a1, b1);
                ST16(dst + m * 1024, ((u32x4){a0, a1, b0, b1}));
            }
        }
    }

}

__device__ __forceinline__ void attn_role(char* smem, const int bx, const char* __restrict__ qkv, char* __restrict__ act, unsigned* __restrict__ ctr) {

    const int tid = threadIdx.x, lane = tid & 63;
    const int wid = __builtin_amdgcn_readfirstlane(tid >> 6);
    const int head = (bx & 7) * 4 + (bx >> 6), qb = (bx >> 3) & 7;
    __builtin_amdgcn_s_setprio(2);
    const char* Qf = qkv + (size_t)head * 131072;
    const char* Kf = Qf + 4 * MB;
    const char* Vr = Qf + 8 * MB;
    const unsigned lds0 = (unsigned)(size_t)(LAS char*)smem;
    const unsigned voff = lane * 16;
    const int tq0 = qb * 8 + wid * 2;
    unsigned fo[2];
#pragma unroll
    for (int s = 0; s < 2; ++s) { const int c = 2 * s + (lane >> 5); fo[s] = ((lane >> 4) & 1) * 1024 + c * 256 + (((lane & 15) ^ (4 * c)) * 16); }
    bf16x8 q[2][2];
    asm volatile("global_load_dwordx4 %0, %4, %6\n\tglobal_load_dwordx4 %1, %5, %6\n\tglobal_load_dwordx4 %2, %4, %6 offset:2048\n\tglobal_load_dwordx4 %3, %5, %6 offset:2048"
                 : "=&v"(q[0][0]), "=&v"(q[0][1]), "=&v"(q[1][0]), "=&v"(q[1][1]) : "v"(fo[0]), "v"(fo[1]), "s"(Qf + tq0 * 2048) : "memory");
    bf16x8 sel;
    { const short one = (((lane >> 2) & 1) == ((lane >> 4) & 1)) ? (short)0x3f80 : (short)0; sel = (bf16x8){one, one, one, one, one, one, one, one}; }
    const lds_cptr kp0 = (lds_cptr)smem + fo[0], kp1 = (lds_cptr)smem + fo[1];
    lds_cptr vp0, vp1;
    { const int h = lane >> 5, c = 2 * ((lane >> 4) & 1) + ((lane >> 1) & 1), q4 = (lane >> 2) & 3;
      vp0 = (lds_cptr)smem + 8192 + c * 256 + (h ^ c) * 64 + q4 * 16 + (lane & 1) * 8; vp1 = (lds_cptr)smem + 8192 + c * 256 + ((2 + h) ^ c) * 64 + q4 * 16 + (lane & 1) * 8; }
    const char* Kw = Kf + wid * 2048;
    const char* Vw = Vr + wid * 2048;
    volatile LAS unsigned* flag = (volatile LAS unsigned*)(smem);
    const unsigned ooff = ((lane >> 4) & 1) * 1024 + (lane >> 5) * 256 + (lane & 15) * 16;
#define AT_DMA(ST, SLOT) do { const unsigned d_ = lds0 + (SLOT) + wid * 2048; \
        dma_s2(voff, Kf + (ST) * 8192 + wid * 2048, d_); dma_s2(voff, Vr + (ST) * 8192 + wid * 2048, d_ + 8192); } while (0)
    for (int attempt = 0; attempt < 2; ++attempt) {
        f32x16 o[2]; f32x4 l[2];
        bf16x8 k0, k1;
        asm volatile("global_load_dwordx4 %0, %2, %4\n\tglobal_load_dwordx4 %1, %3, %4" : "=&v"(k0), "=&v"(k1) : "v"(fo[0]), "v"(fo[1]), "s"(Kf) : "memory");
        AT_DMA(0, 0); AT_DMA(1, 16384); AT_DMA(2, 32768);
        o[0] = (f32x16){}; o[1] = (f32x16){}; l[0] = (f32x4){}; l[1] = (f32x4){};
        PIN(o[0]); PIN(o[1]); PIN(l[0]); PIN(l[1]);
        asm volatile("s_waitcnt vmcnt(12)" : "+v"(k0), "+v"(k1), "+v"(q[0][0]), "+v"(q[0][1]), "+v"(q[1][0]), "+v"(q[1][1])::"memory");
        f32x16 negm[2];
        {
            float m0 = -INFINITY, m1 = -INFINITY;
            const int nt = attempt == 0 ? 1 : 64;
            for (int kt = 0; kt < nt; ++kt) {
                if (kt) { k0 = *(const bf16x8*)(Kf + kt * 2048 + fo[0]); k1 = *(const bf16x8*)(Kf + kt * 2048 + fo[1]); }
                f32x16 s0 = MF32(k0, q[0][0], (f32x16){}), s1 = MF32(k0, q[1][0], (f32x16){});
                s0 = MF32(k1, q[0][1], s0); s1 = MF32(k1, q[1][1], s1);
#pragma unroll
                for (int r = 0; r < 16; ++r) { m0 = fmaxf(m0, s0[r]); m1 = fmaxf(m1, s1[r]); }
            }
            m0 = fmaxf(m0, __shfl_xor(m0, 32)); m1 = fmaxf(m1, __shfl_xor(m1, 32));
#pragma unroll
            for (int r = 0; r < 16; ++r) { negm[0][r] = -m0; negm[1][r] = -m1; }
            PIN(negm[0]); PIN(negm[1]);
        }
        asm volatile("s_waitcnt vmcnt(2)\n\ts_barrier" ::: "memory");
        bf16x8 kf0 = *(const LAS bf16x8*)(kp0), kf1 = *(const LAS bf16x8*)(kp1);
        bf16x8 vf0 = (bf16x8){}, vf1 = (bf16x8){};
        u32x4 pa0 = (u32x4){}, pa1 = (u32x4){}, pb0 = (u32x4){}, pb1 = (u32x4){};
        f32x16 sA = MF32(kf0, q[0][0], negm[0]); sA = MF32(kf1, q[0][1], sA);
        f32x16 sB;
#define EX(X, i) X[i] = __builtin_amdgcn_exp2f(X[i])
#define PK(X, i) cvtpk(X[i], X[(i) + 1])
#define BC8(x) __builtin_bit_cast(bf16x8, x)
#define AT_UNIT(SC, PC0, PC1, SN, QN, NM, PP0, PP1, OP, LP, K0, K1, V0, V1, L0, L1, L2, L3) do { unsigned c0, c1, c2, c3, c4, c5, c6, c7; \
            SN = MF32(K0, QN[0], NM); SCHED(); L0; SCHED(); EX(SC, 0); EX(SC, 1); EX(SC, 2); SCHED(); \
            OP = MF32(V0, BC8(PP0), OP); SCHED(); L1; SCHED(); c0 = PK(SC, 0); EX(SC, 3); EX(SC, 4); EX(SC, 5); SCHED(); \
            SN = MF32(K1, QN[1], SN); SCHED(); L2; SCHED(); c1 = PK(SC, 2); c2 = PK(SC, 4); EX(SC, 6); EX(SC, 7); EX(SC, 8); SCHED(); \
            OP = MF32(V1, BC8(PP1), OP); SCHED(); L3; SCHED(); c3 = PK(SC, 6); EX(SC, 9); EX(SC, 10); EX(SC, 11); SCHED(); \
            MF16_ACC(LP, sel, PP0); SCHED(); c4 = PK(SC, 8); c5 = PK(SC, 10); EX(SC, 12); EX(SC, 13); SCHED(); \
            MF16_ACC(LP, sel, PP1); SCHED(); EX(SC, 14); EX(SC, 15); SCHED(); c6 = PK(SC, 12); SCHED(); c7 = PK(SC, 14); \
            PC0 = (u32x4){c0, c1, c2, c3}; PC1 = (u32x4){c4, c5, c6, c7}; SCHED(); } while (0)
#define AT_M0(SLOT, V) asm volatile("s_mov_b32 m0, %0" ::"s"(lds0 + (SLOT) + wid * 2048 + (V) * 8192) : "memory")
#define AT_PAIR(ST, V) asm volatile("global_load_lds_dwordx4 %0, %1\n\tglobal_load_lds_dwordx4 %0, %1 offset:1024" ::"v"(voff), "s"(((V) ? Vw : Kw) + ((ST) & 15) * 8192) : "memory")
#define AT_STAGE(SO, SN_, DM, ST, SLA, SLB) do { _Pragma("unroll") for (int kt = 0; kt < 4; ++kt) { \
                const int ko = kt < 3 ? (SO) + (kt + 1) * 2048 : (SN_), vo = (SO) + kt * 2048; \
                bf16x8 kn0, kn1, vn0, vn1; \
                AT_UNIT(sA, pa0, pa1, sB, q[1], negm[1], pb0, pb1, o[1], l[1], kf0, kf1, vf0, vf1, \
                        (kn0 = *(const LAS bf16x8*)(kp0 + ko), kn1 = *(const LAS bf16x8*)(kp1 + ko)), vn0 = cat8(tr_read(vp0 + vo), tr_read(vp1 + vo)), \
                        vn1 = cat8(tr_read(vp0 + vo + 1024), tr_read(vp1 + vo + 1024)), {}); \
                kf0 = kn0; kf1 = kn1; vf0 = vn0; vf1 = vn1; \
                __builtin_amdgcn_s_waitcnt(0xC07F); SCHED(); \
                AT_UNIT(sB, pb0, pb1, sA, q[0], negm[0], pa0, pa1, o[0], l[0], kf0, kf1, vf0, vf1, \
                        if (DM) AT_M0((kt >> 1) ? (SLB) : (SLA), kt & 1), if (DM) AT_PAIR((ST) + (kt >> 1), kt & 1), {}, {}); } } while (0)
        int s0 = 0, s1 = 16384, s2 = 32768, s3 = 49152, s4 = 65536;
        for (int k = 0; k < 8; ++k) {
            AT_STAGE(s0, s1, true, 2 * k + 3, s3, s4);
            AT_STAGE(s1, s2, false, 0, 0, 0);
            { const int t0 = s0, t1 = s1; s0 = s2; s1 = s3; s2 = s4; s3 = t0; s4 = t1; }
            asm volatile("s_waitcnt vmcnt(0)\n\ts_barrier" ::: "memory");
        }
#undef AT_STAGE
#undef AT_M0
#undef AT_PAIR
        o[1] = MF32(vf0, BC8(pb0), o[1]); o[1] = MF32(vf1, BC8(pb1), o[1]);
        l[1] = MF16(sel, BC8(pb0), l[1]); l[1] = MF16(sel, BC8(pb1), l[1]);
#undef AT_UNIT
        asm volatile("s_nop 7\n\ts_nop 7" : "+v"(l[0]), "+v"(l[1]), "+v"(o[0]), "+v"(o[1]));
        float tsum = 0.f;
#pragma unroll
        for (int i = 0; i < 2; ++i) {
            const float r = 1.0f / l[i][0];
#pragma unroll
            for (int e = 0; e < 16; ++e) { tsum += fabsf(o[i][e]); o[i][e] *= r; }
        }
        const bool bad = !(l[0][0] < 1.0995e12f) || !(l[1][0] < 1.0995e12f) || !(tsum < INFINITY);
        if (lane == 0) flag[wid] = 0u;
        if (bad) flag[wid] = 1u;
#pragma unroll
        for (int i = 0; i < 2; ++i)
#pragma unroll
            for (int s = 0; s < 2; ++s) {
                unsigned a0 = cvtpk(o[i][8 * s + 0], o[i][8 * s + 1]), a1 = cvtpk(o[i][8 * s + 2], o[i][8 * s + 3]);
                unsigned b0 = cvtpk(o[i][8 * s + 4], o[i][8 * s + 5]), b1 = cvtpk(o[i][8 * s + 6], o[i][8 * s + 7]);
                swap32(a0, b0); swap32(a1, b1);
                const u32x4 w = (u32x4){a0, a1, b0, b1};
                asm volatile("global_store_dwordx4 %0, %1, %2 sc1\n\ts_nop 1" ::"v"(ooff), "v"(w), "s"(act + (size_t)(112 + head) * 131072 + (tq0 + i) * 2048 + s * 512) : "memory");
            }
        asm volatile("s_waitcnt vmcnt(0) lgkmcnt(0)\n\ts_barrier" ::: "memory");
        const unsigned redo = flag[0] | flag[1] | flag[2] | flag[3];
        if (attempt == 1 || !__builtin_amdgcn_readfirstlane(redo)) break;
        asm volatile("s_waitcnt lgkmcnt(0)\n\ts_barrier" ::: "memory");
    }
#undef AT_DMA
    if (tid == 0) __hip_atomic_fetch_add(ctr + qb * 64, 1u, __ATOMIC_RELAXED, __HIP_MEMORY_SCOPE_AGENT);

}

__device__ __forceinline__ void gemm2_role(char* smem, const int bx, const char* __restrict__ act, const char* __restrict__ w2, const char* __restrict__ wo,
                                           const char* __restrict__ x, char* __restrict__ out, unsigned* __restrict__ ctr) {

    const int tid = threadIdx.x, lane = tid & 63;
    const int wid = __builtin_amdgcn_readfirstlane(tid >> 6);
    const int xc = bx & 7, ii = bx >> 3, tm = (xc >> 1) * 4 + (ii >> 3), tn = (xc & 1) * 8 + (ii & 7);
    const unsigned lds0 = (unsigned)(size_t)(LAS char*)smem + wid * 16384;
    __builtin_amdgcn_s_sleep(127);
    const char* asrc = act + (size_t)wid * 131072 + tm * 8192;
    unsigned av[4];
#pragma unroll
    for (int i = 0; i < 4; ++i) av[i] = (i ^ wid) * 2048 + lane * 16;
    const char* wsrc = w2 + (size_t)wid * 32 * 2048 + tn * 128;
    const unsigned wv0 = (lane >> 3) * 2048 + ((((lane >> 1) & 3) ^ ((lane >> 4) & 1)) * 32) + (lane & 1) * 16;
    const unsigned wv1 = (lane >> 3) * 2048 + ((((lane >> 1) & 3) ^ ((lane >> 4) & 1) ^ 2) * 32) + (lane & 1) * 16 + 16384 - 1024;
    lds_cptr wp[4];
    {
        const int g = lane >> 4, q = (lane >> 2) & 3, p = lane & 3, f = (q >> 1) | ((g & 1) << 1);
#pragma unroll
        for (int c = 0; c < 4; ++c) wp[c] = (lds_cptr)smem + wid * 16384 + g * 1024 + q * 128 + ((c ^ f) << 5) + p * 8;
    }
    f32x4 acc[8][4];
#pragma unroll
    for (int i = 0; i < 8; ++i)
#pragma unroll
        for (int n = 0; n < 4; ++n) { acc[i][n] = (f32x4){}; PIN(acc[i][n]); }
    bf16x8 a0[4], a1[4], a2[4], a3[4], wfa[4], wfb[4];
#define G2_LDA(A, H) do { \
        asm volatile("global_load_dwordx4 %0, %4, %6\n\tglobal_load_dwordx4 %1, %4, %6 offset:1024\n\tglobal_load_dwordx4 %2, %5, %6\n\tglobal_load_dwordx4 %3, %5, %6 offset:1024" \
                     : "=&v"(A[0]), "=&v"(A[1]), "=&v"(A[2]), "=&v"(A[3]) : "v"(av[2 * (H)]), "v"(av[2 * (H) + 1]), "s"(asrc) : "memory"); \
        if (H) asrc += 524288; } while (0)
#define G2_LDW(SL) do {     \
        asm volatile("s_mov_b32 m0, %3\n\ts_nop 0\n\tglobal_load_lds_dwordx4 %0, %2\n\tglobal_load_lds_dwordx4 %1, %2 offset:1024" \
                     ::"v"(wv0), "v"(wv1), "s"(wsrc), "s"(lds0 + (SL) * 2048) : "memory"); \
        wsrc += ((SL) & 1) ? 262144 - 32768 : 32768; } while (0)
#define G2_WAIT(N, A) asm volatile("s_waitcnt vmcnt(" #N ")" : "+v"(A[0]), "+v"(A[1]), "+v"(A[2]), "+v"(A[3])::"memory")
#define WFR(P, O) cat8(tr_read((P) + (O)), tr_read((P) + (O) + 512))
#define G2_STEP(N, AC, AN, WC, WN, SL, RD, IA, IW) do { \
        if ((N) >= 0) G2_WAIT(N, AN); \
        if (RD) { WN[2 * ((SL) & 1)] = WFR(wp[2 * ((SL) & 1)], ((((SL) >> 1) + 1) & 3) * 4096); WN[2 * ((SL) & 1) + 1] = WFR(wp[2 * ((SL) & 1) + 1], ((((SL) >> 1) + 1) & 3) * 4096); } \
        SCHED(); \
        _Pragma("unroll") for (int i = 0; i < 16; ++i) { \
            MF16_ACC(acc[4 * ((SL) & 1) + (i >> 2)][((i >> 2) & 1) ? 3 - (i & 3) : (i & 3)], WC[((i >> 2) & 1) ? 3 - (i & 3) : (i & 3)], AC[i >> 2]); \
            if ((IA) && (i & 3) == 3) asm volatile("global_load_dwordx4 %0, %1, %2 offset:%c3" : "=&v"(AC[i >> 2]) : "v"(av[2 * ((SL) & 1) + (i >> 3)]), "s"(asrc), "i"(((i >> 2) & 1) * 1024) : "memory"); } \
        SCHED(); if ((IA) && ((SL) & 1)) asrc += 524288; if (IW) G2_LDW(SL); } while (0)
#define G2_STEP4(N0, N1, N2, N3, SL, RDL, IA, IW) do { \
        G2_STEP(N0, a0, a1, wfa, wfb, (SL), true, IA, IW); G2_STEP(N1, a1, a2, wfa, wfb, (SL) + 1, true, IA, IW); \
        G2_STEP(N2, a2, a3, wfb, wfa, (SL) + 2, RDL, IA, IW); G2_STEP(N3, a3, a0, wfb, wfa, (SL) + 3, RDL, IA, IW); } while (0)
    G2_LDW(0); G2_LDW(1); G2_LDW(2); G2_LDW(3);
    G2_LDA(a0, 0); G2_LDW(4); G2_LDA(a1, 1); G2_LDW(5); G2_LDA(a2, 0); G2_LDW(6); G2_LDA(a3, 1); G2_LDW(7);
    G2_WAIT(20, a0);
#pragma unroll
    for (int c = 0; c < 4; ++c) wfa[c] = WFR(wp[c], 0);
    for (int it = 0; it < 6; ++it) { G2_STEP4(14, 14, 14, 14, 0, true, true, true); G2_STEP4(14, 14, 14, 14, 4, true, true, true); }
    G2_STEP4(14, 12, 10, 8, 0, true, true, false);
    G2_STEP4(8, 4, 0, -1, 4, false, false, false);
#undef G2_STEP4
#undef G2_STEP
#undef G2_WAIT
#undef G2_LDW
#undef G2_LDA
    const int fr = lane & 15, fq = lane >> 4;
    const size_t orow = (size_t)(tm * 128 + wid * 32 + fr) * 2048 + tn * 128 + (fq & 1) * 32 + (fq >> 1) * 16;
    u32x4 xr[4];
#pragma unroll
    for (int c = 0; c < 4; ++c) xr[c] = *(const u32x4*)(x + orow + (c >> 1) * 32768 + (c & 1) * 64);
    asm volatile("s_nop 7" : "+v"(acc[7][0]), "+v"(acc[7][1]), "+v"(acc[7][2]), "+v"(acc[7][3]));
    SCHED();
    char* mine = smem + wid * 16384 + lane * 16;
#pragma unroll
    for (int i = 0; i < 16; ++i) *(f32x4*)(mine + i * 1024) = acc[4 + (i >> 2)][i & 3];
    __syncthreads();
#pragma unroll
    for (int i = 0; i < 16; ++i) acc[i >> 2][i & 3] += *(const f32x4*)(smem + (wid ^ 2) * 16384 + lane * 16 + i * 1024);
    __syncthreads();
#pragma unroll
    for (int i = 0; i < 8; ++i) *(f32x4*)(mine + i * 1024) = acc[2 + (i >> 2)][i & 3];
    __syncthreads();
#pragma unroll
    for (int i = 0; i < 8; ++i) { acc[i >> 2][i & 3] += *(const f32x4*)(smem + (wid ^ 1) * 16384 + lane * 16 + i * 1024); PIN(acc[i >> 2][i & 3]); }
    __syncthreads();
    const unsigned ldsb = (unsigned)(size_t)(LAS char*)smem;
    const char* wos = wo + tn * 128;
    const char* osrc = act + (size_t)112 * 131072 + tm * 8192 + wid * 2048;
    unsigned ov = lane * 16;
    lds_cptr wq[4];
#pragma unroll
    for (int c = 0; c < 4; ++c) { wq[c] = wp[c] - wid * 16384; PIN(wq[c]); }
    PIN(ov);
#define G3_LDW(HS, SLOT) asm volatile("s_mov_b32 m0, %3\n\ts_nop 0\n\tglobal_load_lds_dwordx4 %0, %2\n\tglobal_load_lds_dwordx4 %1, %2 offset:1024" \
                     ::"v"(wv0), "v"(wv1), "s"(wos + (size_t)(HS) * 32768), "s"(ldsb + (SLOT) * 2048) : "memory")
#pragma unroll
    for (int i = 0; i < 10; ++i) G3_LDW(wid * 10 + i, wid * 10 + i);
    if (wid == 0) {
        const unsigned long long t0 = __builtin_amdgcn_s_memrealtime();
        while ((unsigned)__builtin_amdgcn_readfirstlane(__hip_atomic_load(ctr + (tm >> 1) * 64, __ATOMIC_RELAXED, __HIP_MEMORY_SCOPE_AGENT)) < 32u) {
            if (__builtin_amdgcn_s_memrealtime() - t0 > 5000000ull) break;
            __builtin_amdgcn_s_sleep(2);
        }
        __builtin_amdgcn_fence(__ATOMIC_ACQUIRE, "wavefront");
        asm volatile("" ::: "memory");
    }
    asm volatile("s_waitcnt vmcnt(0)\n\ts_barrier" ::: "memory");

    bf16x8 oa[8][4];
#define G3_LDA(G) asm volatile("global_load_dwordx4 %0, %4, %5 sc1\n\tglobal_load_dwordx4 %1, %4, %5 offset:1024 sc1\n\tglobal_load_dwordx4 %2, %4, %6 sc1\n\tglobal_load_dwordx4 %3, %4, %6 offset:1024 sc1" \
                     : "=&v"(oa[(G) & 7][0]), "=&v"(oa[(G) & 7][1]), "=&v"(oa[(G) & 7][2]), "=&v"(oa[(G) & 7][3]) \
                     : "v"(ov), "s"(osrc + (size_t)(G) * 262144), "s"(osrc + (size_t)(G) * 262144 + 131072) : "memory")
#define G3_WAIT(N, G) asm volatile("s_waitcnt vmcnt(" #N ")" : "+v"(oa[(G) & 7][0]), "+v"(oa[(G) & 7][1]), "+v"(oa[(G) & 7][2]), "+v"(oa[(G) & 7][3])::"memory")
    s16x4 wl[2][8], wh[2][8];
#define G3_WA(G, i) (wq[(i) & 3] + (((G) >= 8 && (G) < 10) ? 65536 : 0) + ((((G) * 2 + ((i) >> 2)) % 20) * 4096 - (((G) >= 8 && (G) < 10) ? 65536 : 0)))
#define G3_LD1(G, j) asm volatile("global_load_dwordx4 %0, %1, %2 offset:%c3 sc1" : "=&v"(oa[(G) & 7][j]) : "v"(ov), "s"(osrc + (size_t)(G) * 262144 + ((j) >> 1) * 131072), "i"(((j) & 1) * 1024) : "memory")
#define G3_GROUP(N, G, RD) do { G3_WAIT(N, G); SCHED(); \
        const bool rf_ = (G) == 5 || (G) == 6; \
        _Pragma("unroll") for (int i = 0; i < 8; ++i) { const bf16x8 w_ = cat8(wl[(G) & 1][i], wh[(G) & 1][i]); \
            MF16_ACC(acc[0][i & 3], w_, oa[(G) & 7][2 * (i >> 2)]); SCHED(); if (RD) wl[((G) + 1) & 1][i] = tr_read(G3_WA((G) + 1, i)); SCHED(); \
            MF16_ACC(acc[1][i & 3], w_, oa[(G) & 7][2 * (i >> 2) + 1]); SCHED(); if (RD) wh[((G) + 1) & 1][i] = tr_read(G3_WA((G) + 1, i) + 512); \
            if (rf_ && !(i & 1) && i < 6) G3_LDW(40 + wid * 6 + ((G) - 5) * 3 + (i >> 1), wid * 6 + ((G) - 5) * 3 + (i >> 1)); \
            if ((G) < 11 && (i & 1)) G3_LD1((G) + 5, i >> 1); SCHED(); } } while (0)
    G3_LDA(0); G3_LDA(1); G3_LDA(2); G3_LDA(3); G3_LDA(4);
#pragma unroll
    for (int i = 0; i < 8; ++i) { wl[0][i] = tr_read(G3_WA(0, i)); wh[0][i] = tr_read(G3_WA(0, i) + 512); }
    G3_GROUP(16, 0, true); G3_GROUP(16, 1, true); G3_GROUP(16, 2, true); G3_GROUP(16, 3, true); G3_GROUP(16, 4, true);

    asm volatile("s_waitcnt lgkmcnt(0)\n\ts_barrier" ::: "memory");
    G3_GROUP(16, 5, true); G3_GROUP(22, 6, true); G3_GROUP(28, 7, true); G3_GROUP(28, 8, true);

    asm volatile("s_waitcnt vmcnt(10)\n\ts_barrier" ::: "memory");

    G3_GROUP(10, 9, true); G3_GROUP(22, 10, true); G3_GROUP(16, 11, true); G3_GROUP(12, 12, true); G3_GROUP(8, 13, true); G3_GROUP(4, 14, true); G3_GROUP(0, 15, false);

#undef G3_GROUP
#undef G3_WA
#undef G3_WAIT
#undef G3_LDA
#undef G3_LD1
#undef G3_LDW
#undef WFR
    asm volatile("s_nop 7" : "+v"(acc[0][0]), "+v"(acc[0][1]), "+v"(acc[0][2]), "+v"(acc[0][3]), "+v"(acc[1][0]), "+v"(acc[1][1]), "+v"(acc[1][2]), "+v"(acc[1][3]));
#pragma unroll
    for (int c = 0; c < 4; ++c) {
        float v[8];
#pragma unroll
        for (int e = 0; e < 4; ++e) {
            unsigned a = __float_as_uint(acc[c >> 1][2 * (c & 1)][e]), b = __float_as_uint(acc[c >> 1][2 * (c & 1) + 1][e]);
            swap16(a, b);
            v[e] = __uint_as_float(a); v[4 + e] = __uint_as_float(b);
        }
        u32x4 w;
#pragma unroll
        for (int e = 0; e < 4; ++e) w[e] = cvtpk(v[2 * e] + bf_lo(xr[c][e]), v[2 * e + 1] + bf_hi(xr[c][e]));
        *(u32x4*)(out + orow + (c >> 1) * 32768 + (c & 1) * 64) = w;
    }

}

extern "C" __global__ __launch_bounds__(256, 2) void k_fused(const char* __restrict__ qkv, char* __restrict__ act, const char* __restrict__ w2, const char* __restrict__ wo,
                                                  const char* __restrict__ x, char* __restrict__ out, unsigned* __restrict__ ctr) {
    __shared__ __attribute__((aligned(1024))) char smem[81920];
    asm volatile("" ::"s"(qkv), "s"(act), "s"(w2));
    if (__builtin_expect(blockIdx.x < 256, 1)) attn_role(smem, blockIdx.x, qkv, act, ctr);
    else gemm2_role(smem, blockIdx.x - 256, act, w2, wo, x, out, ctr);
}

extern "C" __global__ void k_end() {}

extern "C" void kernel_launch(void* const* d_in, const int* in_sizes, int n_in, void* d_out, int out_size, void* d_ws, size_t ws_size,
                              hipStream_t stream) {
    const u16* x = (const u16*)d_in[0];
    const char* wqkv = (const char*)d_in[1];
    const char* wo = (const char*)d_in[2];
    const char* w1 = (const char*)d_in[4];
    const char* w3 = (const char*)d_in[5];
    const char* w2 = (const char*)d_in[6];
    char* ws = (char*)d_ws;
    k_ln<<<512, 512, 0, stream>>>(x, ws + WS_XN, (unsigned*)(ws + WS_CTR), wqkv, w1, w3);
    k_gemm1<<<256, 512, 0, stream>>>(ws + WS_XN, wqkv, w1, w3, ws + WS_QKV, ws + WS_ACT, w2, wo);
    k_fused<<<512, 256, 0, stream>>>(ws + WS_QKV, ws + WS_ACT, w2, wo, (const char*)x, (char*)d_out, (unsigned*)(ws + WS_CTR));
}
```
